```python
import torch

def _softmax(x, axis=-1):
    return torch.softmax(x, dim=axis)

def _gelu(x):
    return torch.nn.functional.gelu(x, approximate='tanh')

def _silu(x):
    return torch.nn.functional.silu(x)

def _sigmoid(x):
    return torch.sigmoid(x)

def _layernorm(x, axis=-1, eps=1e-05):
    m = x.mean(dim=axis, keepdim=True)
    v = x.var(dim=axis, keepdim=True, unbiased=False)
    return (x - m) / torch.sqrt(v + eps)

def _scatter_add(tgt, idx, src, axis=0):
    return tgt.clone().index_add_(axis, idx, src)

def _segment_sum(src, seg, n):
    out = torch.zeros((n,) + src.shape[1:], dtype=src.dtype)
    return out.index_add_(0, seg, src)

def _pad(x, pw):
    flat = tuple((v for pair in reversed(pw) for v in pair))
    return torch.nn.functional.pad(x, flat)
kernel_name = 'interleave_layers__linear_attn_block__mixer_gmlp__prompt'

def setup_inputs():

    _w = lambda a: (lambda t: t.to(torch.bfloat16) if t.is_floating_point() else t)(torch.as_tensor(a))

    def _A_setup():
        B, S, D = (1, 2048, 576)
        return {'x': _w((lambda *s: torch.randn(*s, dtype=torch.bfloat16) * 0.05)(B, S, D)), 'wqkv': _w((lambda *s: torch.randn(*s, dtype=torch.bfloat16) * 0.1)(D, 3 * D)), 'wo': _w((lambda *s: torch.randn(*s, dtype=torch.bfloat16) * 0.05)(D, D))}
    _A_in = _A_setup()

    def _B_setup():
        B, S, D, F = (1, 2048, 576, 4608)
        Fh = F // 2
        return {'x': _w((lambda *s: torch.randn(*s, dtype=torch.bfloat16) * 0.05)(B, S, D)), 'w_in': _w((lambda *s: torch.randn(*s, dtype=torch.bfloat16) * 0.2)(D, 2 * Fh)), 'w_sp': _w(0.01 * (lambda *s: torch.randn(*s, dtype=torch.bfloat16) * 0.2)(S, S)), 'b_sp': _w(1.0 + 0.01 * (lambda *s: torch.randn(*s, dtype=torch.bfloat16) * 0.05)(S)), 'w_out': _w((lambda *s: torch.randn(*s, dtype=torch.bfloat16) * 0.05)(Fh, D))}
    _B_in = _B_setup()
    return {'A_x': _A_in['x'], 'A_wqkv': _A_in['wqkv'], 'A_wo': _A_in['wo'], 'B_x': _B_in['x'], 'B_w_in': _B_in['w_in'], 'B_w_sp': _B_in['w_sp'], 'B_b_sp': _B_in['b_sp'], 'B_w_out': _B_in['w_out']}

def reference(A_x, A_wqkv, A_wo, B_x, B_w_in, B_w_sp, B_b_sp, B_w_out):

    def _A_ref(x, wqkv, wo):
        B, S, D = x.shape
        H = 32
        Dh = D // H
        qkv = (lambda x, *d: x.permute(*d))((x @ wqkv).reshape(B, S, 3, H, Dh), 2, 0, 3, 1, 4)
        q, k, v = (qkv[0], qkv[1], qkv[2])
        phi = lambda t: torch.where(t > 0, t + 1, torch.exp(t)) + 1e-06
        pq, pk = (phi(q), phi(k))
        kv = torch.einsum('bhsd,bhse->bhde', pk, v)
        z = torch.einsum('bhsd->bhd', pk)
        num = torch.einsum('bhqd,bhde->bhqe', pq, kv)
        den = torch.einsum('bhqd,bhd->bhq', pq, z)[..., None] + 1e-06
        o = num / den
        return (lambda x, *d: x.permute(*d))(o, 0, 2, 1, 3).reshape(B, S, D) @ wo

    def _B_ref(x, w_in, w_sp, b_sp, w_out):
        B, S, D = x.shape
        F = w_in.shape[-1]
        Fh = F // 2
        z = _gelu(torch.einsum('bsd,df->bsf', x, w_in))
        z1 = z[..., :Fh].clone()
        z2 = _layernorm(z[..., Fh:], axis=-1)
        g = torch.einsum('ts,bsf->btf', w_sp, z2) + b_sp[None, :, None]
        return torch.einsum('bsf,fd->bsd', z1 * g, w_out)
    x = A_x
    x = x + _A_ref(_layernorm(x, axis=-1), A_wqkv, A_wo)
    x = x + _B_ref(_layernorm(x, axis=-1), B_w_in, B_w_sp, B_b_sp, B_w_out)
    x = x + _A_ref(_layernorm(x, axis=-1), A_wqkv, A_wo)
    x = x + _B_ref(_layernorm(x, axis=-1), B_w_in, B_w_sp, B_b_sp, B_w_out)
    return x
```

```cpp
#include <hip/hip_runtime.h>
#include <hip/hip_bf16.h>
#include <stdint.h>

#define LAS __attribute__((address_space(3)))
#define GAS __attribute__((address_space(1)))
typedef unsigned short u16;
typedef unsigned u32;
typedef unsigned long long u64;
typedef short bf16x8 __attribute__((ext_vector_type(8)));
typedef float f32x2 __attribute__((ext_vector_type(2)));
typedef float f32x4 __attribute__((ext_vector_type(4)));
typedef unsigned u32x2 __attribute__((ext_vector_type(2)));
typedef unsigned u32x4 __attribute__((ext_vector_type(4)));
typedef __bf16 bf2_t __attribute__((ext_vector_type(2)));

#define WAIT_V(n) asm volatile("s_waitcnt vmcnt(%0)" ::"i"(n) : "memory")
#define WAIT_L0() asm volatile("s_waitcnt lgkmcnt(0)" ::: "memory")
#define FENCE() asm volatile("" ::: "memory")
#define SB() __builtin_amdgcn_sched_barrier(0)
#define BAR() do { FENCE(); __builtin_amdgcn_s_barrier(); FENCE(); } while (0)
#define WCNT(vm, lg) __builtin_amdgcn_s_waitcnt(((vm) & 15) | (7 << 4) | ((lg) << 8) | (((vm) >> 4) << 14))
#define MFMA(acc, x, y) asm volatile("v_mfma_f32_16x16x32_bf16 %0, %1, %2, %0" : "+v"(acc) : "v"(x), "v"(y))
#define MFMAB(x, y, c) __builtin_amdgcn_mfma_f32_16x16x32_bf16(x, y, c, 0, 0, 0)
#define PINV(x) asm volatile("" : "+v"(x))
#define RLX_AGENT __ATOMIC_RELAXED, __HIP_MEMORY_SCOPE_AGENT

constexpr int S_ = 2048, D_ = 576, FH_ = 2304;
constexpr u32 SENT = 0xFFFFFFFFu;
constexpr u32 SPIN_CAP = 1u << 16;

constexpr u32 WS_WQ = 0;
constexpr u32 WS_CSQ = WS_WQ + 16 * 129024;
constexpr u32 WS_WO = WS_CSQ + 8192;
constexpr u32 WS_WIN = WS_WO + 8 * 18 * 4608;
constexpr u32 WS_CSIN = WS_WIN + 288 * 18 * 1024;
constexpr u32 WS_WOUT = WS_CSIN + 4608 * 4;
constexpr u32 WS_X = WS_WOUT + 8 * 72 * 4608;
constexpr u32 WS_XS = WS_X + 128 * 18 * 1024;
constexpr u32 WS_O = WS_XS + 2048 * 64;
constexpr u32 WS_Z = WS_O + 128 * 18 * 1024;
constexpr u32 WS_H = WS_Z + 144 * 64 * 1024;
constexpr u32 WS_KVP = WS_H + 128 * 8 * 9216;
constexpr u32 WS_ZST = WS_KVP + 2 * 256 * 2816;
constexpr u32 WS_CNT = WS_ZST + 2 * 256 * 1024;
constexpr u32 WS_END = WS_CNT + 2 * 16 * 256;
constexpr u32 WS_DBG = WS_END;

#define STAMP(k, i) do { } while (0)

__device__ __forceinline__ u32 pk2(float lo, float hi) {
    f32x2 v = {lo, hi};
    bf2_t b = __builtin_convertvector(v, bf2_t);
    return __builtin_bit_cast(u32, b);
}
__device__ __forceinline__ float bflo(u32 w) { return __uint_as_float(w << 16); }
__device__ __forceinline__ float bfhi(u32 w) { return __uint_as_float(w & 0xffff0000u); }
__device__ __forceinline__ bf16x8 mk8(u32 a, u32 b, u32 c, u32 d) { u32x4 v = {a, b, c, d}; return __builtin_bit_cast(bf16x8, v); }
__device__ __forceinline__ float phi(float t) {
    float e = __builtin_amdgcn_exp2f(t * 1.44269504089f);
    return (t > 0.f ? t + 1.f : e) + 1e-6f;
}
__device__ __forceinline__ u32 ldsw(u32 a) { return *(volatile LAS u32*)(size_t)a; }
__device__ __forceinline__ void stsw(u32 a, u32 v) { *(volatile LAS u32*)(size_t)a = v; }
#define LDSR(addr, off) (*(LAS bf16x8*)((LAS char*)(size_t)(addr) + (off)))
__device__ __forceinline__ void lds_wait(u32 a, u32 target) {
    FENCE();
    for (u32 n = 0; n < SPIN_CAP; ++n) { const u32 v = ldsw(a); if (!__any(v < target)) break; }
    FENCE();
}
__device__ __forceinline__ void lds_wait_sleep(u32 a, u32 target) {
    FENCE();
    for (u32 n = 0; n < SPIN_CAP; ++n) { const u32 v = ldsw(a); if (!__any(v < target)) break; __builtin_amdgcn_s_sleep(1); }
    FENCE();
}
#define DMA4(v0, v1, v2, v3, sbase, m0v)                                                                            \
    asm volatile("s_mov_b32 m0, %5\n\ts_nop 0\n\tglobal_load_lds_dwordx4 %0, %4\n\t"                                \
                 "global_load_lds_dwordx4 %1, %4 offset:1024\n\tglobal_load_lds_dwordx4 %2, %4 offset:2048\n\t"     \
                 "global_load_lds_dwordx4 %3, %4 offset:3072" ::"v"(v0), "v"(v1), "v"(v2), "v"(v3), "s"(sbase), "s"(m0v) : "memory")
#define DMA2(v0, v1, sbase, m0v)                                                                                    \
    asm volatile("s_mov_b32 m0, %3\n\ts_nop 0\n\tglobal_load_lds_dwordx4 %0, %2\n\t"                                \
                 "global_load_lds_dwordx4 %1, %2 offset:1024" ::"v"(v0), "v"(v1), "s"(sbase), "s"(m0v) : "memory")

__device__ __forceinline__ u32x4 tcol(const u32x2 (&ld)[8], int i) {
    const u32 sel = (i & 1) ? 0x07060302u : 0x05040100u;
    u32x4 o;
    o.x = __builtin_amdgcn_perm(ld[1][i >> 1], ld[0][i >> 1], sel);
    o.y = __builtin_amdgcn_perm(ld[3][i >> 1], ld[2][i >> 1], sel);
    o.z = __builtin_amdgcn_perm(ld[5][i >> 1], ld[4][i >> 1], sel);
    o.w = __builtin_amdgcn_perm(ld[7][i >> 1], ld[6][i >> 1], sel);
    return o;
}
__device__ __forceinline__ float sum8(u32x4 o) {
    return ((bflo(o.x) + bfhi(o.x)) + (bflo(o.y) + bfhi(o.y))) + ((bflo(o.z) + bfhi(o.z)) + (bflo(o.w) + bfhi(o.w)));
}

__global__ __launch_bounds__(576) void k0_prep(const u16* __restrict__ x, const u16* __restrict__ wqkv, const u16* __restrict__ wo,
                                               const u16* __restrict__ win, const u16* __restrict__ wout, char* __restrict__ ws) {
    __shared__ float red[9][64];
    const int tid = threadIdx.x, w = tid >> 6, lane = tid & 63, c = lane & 15, g = lane >> 4;
    int b = blockIdx.x;
    if (b < 64) {
        const int p = b >> 2, fp = b & 3;
        const int fi = 2 * fp + (c >> 3), cc = 2 * (c & 7);
        int col; bool ok = true;
        if (fi == 4) { ok = cc < 12; const int tt = (cc >> 2) == 0 ? 1 : ((cc >> 2) == 1 ? 2 : 0); col = 576 * tt + 18 * (2 * p + ((cc >> 1) & 1)) + 16; }
        else if (fi < 4) col = 576 * (1 + (fi >> 1)) + 18 * (2 * p + (fi & 1)) + cc;
        else col = 18 * (2 * p + ((fi - 5) & 1)) + cc;
        const bool live = fi < 7;
        ok = ok && live;
        u32 ld[2][8];
#pragma unroll
        for (int it = 0; it < 2; ++it)
#pragma unroll
            for (int j = 0; j < 8; ++j) { u32 v = *(const u32*)(wqkv + (size_t)(32 * (2 * w + it) + 8 * g + j) * 1728 + col); ld[it][j] = ok ? v : 0u; }
        float cs[2] = {0.f, 0.f};
#pragma unroll
        for (int it = 0; it < 2; ++it) {
            const int kt = 2 * w + it;
            const u32 tile = fi < 5 ? (kt * 5 + fi) * 1024 : 92160 + (kt * 2 + fi - 5) * 1024;
#pragma unroll
            for (int i = 0; i < 2; ++i) {
                const u32 sel = i ? 0x07060302u : 0x05040100u;
                u32x4 o;
                o.x = __builtin_amdgcn_perm(ld[it][1], ld[it][0], sel); o.y = __builtin_amdgcn_perm(ld[it][3], ld[it][2], sel);
                o.z = __builtin_amdgcn_perm(ld[it][5], ld[it][4], sel); o.w = __builtin_amdgcn_perm(ld[it][7], ld[it][6], sel);
                if (live) *(u32x4*)(ws + WS_WQ + p * 129024 + tile + (cc + i + 16 * g) * 16) = o;
                cs[i] += sum8(o);
            }
        }
#pragma unroll
        for (int i = 0; i < 2; ++i) { cs[i] += __shfl_xor(cs[i], 16); cs[i] += __shfl_xor(cs[i], 32); if (g == 0) red[w][2 * c + i] = cs[i]; }
        __syncthreads();
        if (tid < 32) {
            float s = 0.f;
#pragma unroll
            for (int k = 0; k < 9; ++k) s += red[k][tid];
            const int f2 = 2 * fp + (tid >> 4);
            if (f2 < 7) ((float*)(ws + WS_CSQ))[(p * 7 + f2) * 16 + (tid & 15)] = s;
        }
        return;
    }
    b -= 64;
    if (b < 32) {
        if (w == 8) return;
        const int row = 64 * b + 8 * w + (lane >> 3), c8 = lane & 7;
        u32x4 v[9];
#pragma unroll
        for (int i = 0; i < 9; ++i) v[i] = *(const u32x4*)(x + (size_t)row * 576 + (8 * i + c8) * 8);
        float s1 = 0.f, s2 = 0.f;
#pragma unroll
        for (int i = 0; i < 9; ++i) {
            const int ch = 8 * i + c8;
            *(u32x4*)(ws + WS_X + ((row >> 4) * 18 + (ch >> 2)) * 1024 + ((row & 15) + 16 * (ch & 3)) * 16) = v[i];
#pragma unroll
            for (int k = 0; k < 4; ++k) { const float a = bflo(v[i][k]), bb = bfhi(v[i][k]); s1 += a + bb; s2 += a * a + bb * bb; }
        }
        f32x2 o = {s1, s2};
        *(f32x2*)(ws + WS_XS + row * 64 + c8 * 8) = o;
        return;
    }
    b -= 32;
    if (b < 72) {
        u32x2 ld[2][8];
#pragma unroll
        for (int it = 0; it < 2; ++it)
#pragma unroll
            for (int j = 0; j < 8; ++j) ld[it][j] = *(const u32x2*)(win + (size_t)(32 * (2 * w + it) + 8 * g + j) * 4608 + 64 * b + 4 * c);
        float cs[4] = {0.f, 0.f, 0.f, 0.f};
#pragma unroll
        for (int it = 0; it < 2; ++it)
#pragma unroll
            for (int i = 0; i < 4; ++i) {
                const u32x4 o = tcol(ld[it], i);
                *(u32x4*)(ws + WS_WIN + ((4 * b + (c >> 2)) * 18 + 2 * w + it) * 1024 + (4 * (c & 3) + i + 16 * g) * 16) = o;
                cs[i] += sum8(o);
            }
#pragma unroll
        for (int i = 0; i < 4; ++i) { cs[i] += __shfl_xor(cs[i], 16); cs[i] += __shfl_xor(cs[i], 32); if (g == 0) red[w][4 * c + i] = cs[i]; }
        __syncthreads();
        if (tid < 64) {
            float s = 0.f;
#pragma unroll
            for (int k = 0; k < 9; ++k) s += red[k][tid];
            ((float*)(ws + WS_CSIN))[64 * b + tid] = s;
        }
        return;
    }
    b -= 72;
    if (b < 45) {
        const bool iso = b < 9;
        const int sl0 = iso ? 2 * b : 2 * (b - 9);
        const u16* src = iso ? wo : wout;
        const int col = 64 * w + 4 * c, nt = col / 72, cl = col - 72 * nt;
        u32x2 ld[2][8];
#pragma unroll
        for (int it = 0; it < 2; ++it) {
            const int sl = sl0 + it;
#pragma unroll
            for (int j = 0; j < 8; ++j) {
                int r;
                if (iso) {
                    if (sl < 16) r = 18 * (2 * sl + (j >> 2)) + 4 * g + (j & 3);
                    else r = 18 * (2 * ((sl - 16) * 8 + 2 * g + (j >> 2)) + ((j >> 1) & 1)) + 16 + (j & 1);
                } else {
                    const int G = sl / 9, t = sl - 9 * G;
                    r = t < 8 ? 288 * G + 144 * (t >> 2) + 32 * (t & 3) + 16 * (j >> 2) + 4 * g + (j & 3)
                              : 288 * G + 144 * (j >> 2) + 128 + 4 * g + (j & 3);
                }
                ld[it][j] = *(const u32x2*)(src + (size_t)r * 576 + col);
            }
        }
#pragma unroll
        for (int it = 0; it < 2; ++it) {
            const u32 base = iso ? WS_WO + (nt * 18 + sl0 + it) * 4608 : WS_WOUT + (nt * 72 + sl0 + it) * 4608;
#pragma unroll
            for (int i = 0; i < 4; ++i) {
                const int n = cl >> 4, ii = (cl & 15) + i;
                const u32 off = n < 4 ? n * 1024 + (ii + 16 * g) * 16 : 4096 + g * 128 + ii * 16;
                *(u32x4*)(ws + base + off) = tcol(ld[it], i);
            }
        }
        return;
    }
    b -= 45;
    {
        const u32 n16 = (WS_END - WS_KVP) / 16, nz = (WS_CNT - WS_KVP) / 16;
        for (u32 i = b * 576 + tid; i < n16; i += 43 * 576) {
            const u32 v = i < nz ? SENT : 0u;
            u32x4 o = {v, v, v, v};
            *(u32x4*)(ws + WS_KVP + i * 16) = o;
        }
    }
}

__device__ __forceinline__ void x_stats(const char* ws, int row, float& mean, float& var) {
    const f32x4* p = (const f32x4*)(ws + WS_XS + row * 64);
    const f32x4 a = p[0], b = p[1], c = p[2], d = p[3];
    const float s1 = ((a[0] + a[2]) + (b[0] + b[2])) + ((c[0] + c[2]) + (d[0] + d[2]));
    const float s2 = ((a[1] + a[3]) + (b[1] + b[3])) + ((c[1] + c[3]) + (d[1] + d[3]));
    mean = s1 * (1.f / 576.f);
    var = fmaxf(s2 * (1.f / 576.f) - mean * mean, 0.f);
}

constexpr u32 K1_PW = 129024, K1_OP = K1_PW + 4 * 176 * 16, K1_ST = K1_OP + 4096, K1_FL = K1_ST + 1024, K1_LDS = K1_FL + 64;

__global__ __launch_bounds__(512, 2) void k1_attn(char* __restrict__ ws, int layer) {
    extern __shared__ __attribute__((aligned(16))) char shm_[];
    const u32 lds = (u32)(size_t)(LAS char*)shm_;
    const int tid = threadIdx.x, wid = __builtin_amdgcn_readfirstlane(tid >> 6), lane = tid & 63, c = lane & 15, g = lane >> 4;
    const int xcd = blockIdx.x & 7, bi = blockIdx.x >> 3;
    const int pm = (xcd & 1) * 8 + (bi & 7), p = (xcd >> 1) * 4 + (bi >> 3);
    const u32 FL = lds + K1_FL;
    if (wid == 4) {
        const char* src = ws + WS_WQ + p * 129024;
        const u32 vo = lane * 16;
#define K1_U10(u) do { DMA4(vo, vo, vo, vo, src + (u) * 10240, lds + (u) * 10240); DMA4(vo, vo, vo, vo, src + (u) * 10240 + 4096, lds + (u) * 10240 + 4096); \
                       DMA2(vo, vo, src + (u) * 10240 + 8192, lds + (u) * 10240 + 8192); } while (0)
#define K1_U12(u) do { DMA4(vo, vo, vo, vo, src + 92160 + (u) * 12288, lds + 92160 + (u) * 12288); DMA4(vo, vo, vo, vo, src + 92160 + (u) * 12288 + 4096, lds + 92160 + (u) * 12288 + 4096); \
                       DMA4(vo, vo, vo, vo, src + 92160 + (u) * 12288 + 8192, lds + 92160 + (u) * 12288 + 8192); } while (0)
        K1_U10(0); K1_U10(1);
        BAR();
        K1_U10(2);
#pragma unroll
        for (int u = 3; u < 9; ++u) { WAIT_V(20); stsw(FL, u - 2); FENCE(); K1_U10(u); }
        WAIT_V(20); stsw(FL, 7); FENCE(); K1_U12(0);
        WAIT_V(22); stsw(FL, 8); FENCE(); K1_U12(1);
        WAIT_V(24); stsw(FL, 9); FENCE(); K1_U12(2);
        WAIT_V(24); stsw(FL, 10); FENCE();
        WAIT_V(12); stsw(FL, 11); FENCE();
        WAIT_V(0); stsw(FL, 12);
        return;
    }
    if (wid > 4) {
        const int ht = tid - 320;
        {
            u32x4 z = {0u, 0u, 0u, 0u};
            *(LAS u32x4*)(size_t)(lds + K1_OP + ht * 16) = z;
            if (ht < 64) *(LAS u32x4*)(size_t)(lds + K1_OP + 3072 + ht * 16) = z;
            if (ht >= 128 && ht < 144) stsw(FL + (ht - 128) * 4, 0u);
        }
        float mean = 0.f, var = 0.f;
        if (ht < 128) x_stats(ws, 128 * pm + ht, mean, var);
        WAIT_L0();
        BAR();
        if (ht < 128) {
            const float r = __builtin_amdgcn_rsqf(var + 1e-5f);
            f32x2 o = {r, -mean * r};
            *(LAS f32x2*)(size_t)(lds + K1_ST + ht * 8) = o;
            WAIT_L0();
            stsw(FL + 4 + (wid - 5) * 4, 1u);
        }
        lds_wait_sleep(FL + 16 + (lane & 3) * 4, 1u);
        const int it = ht < 173 ? ht : 172;
        f32x4 v = *(LAS f32x4*)(size_t)(lds + K1_PW + it * 16);
#pragma unroll
        for (int k = 1; k < 4; ++k) v += *(LAS f32x4*)(size_t)(lds + K1_PW + k * 2816 + it * 16);
        const auto rs = __builtin_amdgcn_make_buffer_rsrc(ws, 0, WS_END, 0x00020000);
        const u32 gbase = WS_KVP + (layer * 16 + p) * 16 * 2816;
        {
            u32x4 o = __builtin_bit_cast(u32x4, v);
#pragma unroll
            for (int k = 0; k < 4; ++k) o[k] = o[k] == SENT ? 0x7fc00000u : o[k];
            if (ht < 173) __builtin_amdgcn_raw_buffer_store_b128(o, rs, it * 16, gbase + pm * 2816, 16);
        }
        u32x4 t[16];
        for (u32 spins = 0; spins < SPIN_CAP; ++spins) {
            bool bad = false;
#pragma unroll
            for (int k = 0; k < 16; ++k) t[k] = __builtin_amdgcn_raw_buffer_load_b128(rs, it * 16, gbase + k * 2816, 16);
#pragma unroll
            for (int k = 0; k < 16; ++k) bad = bad || t[k][0] == SENT || t[k][1] == SENT || t[k][2] == SENT || t[k][3] == SENT;
            if (!__any(bad)) break;
            __builtin_amdgcn_s_sleep(2);
        }
        v = __builtin_bit_cast(f32x4, t[0]);
#pragma unroll
        for (int k = 1; k < 16; ++k) v += __builtin_bit_cast(f32x4, t[k]);
        const u32 lo = pk2(v[0], v[1]), hi = pk2(v[2], v[3]);
        const u32x2 lh = {lo, hi};
        const u32 OP = lds + K1_OP;
        if (ht < 128) *(LAS u32x2*)(size_t)(OP + ht * 16) = lh;
        else if (ht < 144) { stsw(OP + (ht - 128 + 32) * 16 + 8, lo); stsw(OP + 1024 + (ht - 128 + 32) * 16 + 12, hi); }
        else if (ht < 168) {
            const int q = ht - 144, hb = q >= 12, qq = q - 12 * hb, tt = qq >> 2, gg = qq & 3;
            const u32 fb = OP + 2048 + hb * 1024 + gg * 256;
            if (tt < 2) *(LAS u32x2*)(size_t)(fb + (4 * hb + tt) * 16) = lh;
            else {
#pragma unroll
                for (int k = 0; k < 4; ++k) *(LAS u32x2*)(size_t)(fb + (4 * k + 2 + hb) * 16) = lh;
            }
        } else if (ht < 173) {
            const int q = ht - 168;
            if (q < 2) stsw(OP + 2048 + (32 + q) * 16 + 8, lo);
            else if (q < 4) stsw(OP + 3072 + (32 + 2 + q) * 16 + 12, hi);
            else {
#pragma unroll
                for (int k = 0; k < 4; ++k) { stsw(OP + 2048 + (32 + 4 * k + 2) * 16 + 8, lo); stsw(OP + 3072 + (32 + 4 * k + 3) * 16 + 12, hi); }
            }
        }
        WAIT_L0();
        stsw(FL + 32 + (wid - 5) * 4, 1u);
        return;
    }
    STAMP(layer * 4 + 0, 0);
    const float* csq = (const float*)(ws + WS_CSQ) + p * 112;
    float cs1[5];
    const auto rs = __builtin_amdgcn_make_buffer_rsrc(ws, 0, WS_END, 0x00020000);
    const u32 xb = WS_X + (8 * pm + 2 * wid) * 18432, vo = lane * 16;
    bf16x8 xa[2][18];
#define K1_LX(m, kt) xa[m][kt] = __builtin_bit_cast(bf16x8, __builtin_amdgcn_raw_buffer_load_b128(rs, vo, xb + ((m) * 18 + (kt)) * 1024, 0))
#pragma unroll
    for (int kt = 0; kt < 6; ++kt) { K1_LX(0, kt); SB(); K1_LX(1, kt); SB(); }
    f32x4 acc[2][5];
#pragma unroll
    for (int f = 0; f < 5; ++f) { acc[0][f] = acc[1][f] = (f32x4){0.f, 0.f, 0.f, 0.f}; PINV(acc[0][f]); PINV(acc[1][f]); }
    u32 wb0 = lds + lane * 16, wb1 = wb0 + 61440, wb2 = wb0 + 92160;
    PINV(wb0); PINV(wb1); PINV(wb2);
#define K1_W1(kt, f) ((kt) < 12 ? LDSR(wb0, ((kt) * 5 + (f)) * 1024) : LDSR(wb1, (((kt) - 12) * 5 + (f)) * 1024))
    BAR();
    lds_wait(FL, 1u);
    STAMP(layer * 4 + 0, 1);
    bf16x8 wf[2][5];
#pragma unroll
    for (int f = 0; f < 5; ++f) { wf[0][f] = K1_W1(0, f); SB(); }
#pragma unroll
    for (int f = 0; f < 5; ++f) { wf[1][f] = K1_W1(1, f); SB(); }
    u32 fl = 0;
#pragma unroll
    for (int kt = 0; kt < 18; ++kt) {
        if ((kt & 1) == 0 && kt + 2 < 18) { if (__builtin_expect(__any(fl < (u32)(kt / 2 + 2)), 0)) lds_wait(FL, kt / 2 + 2); FENCE(); }
#pragma unroll
        for (int f = 0; f < 5; ++f) {
            MFMA(acc[0][f], xa[0][kt], wf[kt & 1][f]);
            if (f == 1 && kt + 6 < 18) K1_LX(0, kt + 6);
            if (f == 3 && kt + 6 < 18) K1_LX(1, kt + 6);
            if (kt == 16) cs1[f] = csq[f * 16 + c];
            if (f == 0 && (kt & 1) && kt < 16) { FENCE(); fl = ldsw(FL); FENCE(); }
            SB();
            MFMA(acc[1][f], xa[1][kt], wf[kt & 1][f]);
            if (kt + 2 < 18) wf[kt & 1][f] = K1_W1(kt + 2, f);
            SB();
        }
    }
    asm volatile("s_nop 15" ::: "memory");
    STAMP(layer * 4 + 0, 2);
    lds_wait(FL + 4 + (lane & 1) * 4, 1u);
    {
        u32 op[5][4], rk[4];
#pragma unroll
        for (int m = 0; m < 2; ++m) {
            const f32x4 sa = *(LAS f32x4*)(size_t)(lds + K1_ST + (32 * wid + 16 * m + 4 * g) * 8);
            const f32x4 sb = *(LAS f32x4*)(size_t)(lds + K1_ST + (32 * wid + 16 * m + 4 * g) * 8 + 16);
            const float rr[4] = {sa[0], sa[2], sb[0], sb[2]}, mm[4] = {sa[1], sa[3], sb[1], sb[3]};
#pragma unroll
            for (int f = 0; f < 5; ++f) {
                float v[4];
#pragma unroll
                for (int r = 0; r < 4; ++r) v[r] = rr[r] * acc[m][f][r] + mm[r] * cs1[f];
                if (f < 2) {
#pragma unroll
                    for (int r = 0; r < 4; ++r) v[r] = phi(v[r]);
                }
                if (f == 4) {
                    rk[2 * m] = pk2(phi(v[0]), phi(v[1])); rk[2 * m + 1] = pk2(phi(v[2]), phi(v[3]));
#pragma unroll
                    for (int r = 0; r < 4; ++r) v[r] = c >= 12 ? 1.f : v[r];
                }
                op[f][2 * m] = pk2(v[0], v[1]); op[f][2 * m + 1] = pk2(v[2], v[3]);
            }
        }
        const bf16x8 KAa = mk8(op[0][0], op[0][1], op[0][2], op[0][3]), KAb = mk8(op[1][0], op[1][1], op[1][2], op[1][3]);
        const bf16x8 VBa = mk8(op[2][0], op[2][1], op[2][2], op[2][3]), VBb = mk8(op[3][0], op[3][1], op[3][2], op[3][3]);
        const bf16x8 RV = mk8(op[4][0], op[4][1], op[4][2], op[4][3]), RK = mk8(rk[0], rk[1], rk[2], rk[3]);
        const f32x4 z4 = {0.f, 0.f, 0.f, 0.f};
        const f32x4 T1a = MFMAB(KAa, VBa, z4), T1b = MFMAB(KAb, VBb, z4), T2a = MFMAB(KAa, RV, z4), T2b = MFMAB(KAb, RV, z4);
        const f32x4 T3a = MFMAB(RK, VBa, z4), T3b = MFMAB(RK, VBb, z4), T4 = MFMAB(RK, RV, z4);
        const u32 pw = lds + K1_PW + wid * 2816;
        *(LAS f32x4*)(size_t)(pw + lane * 16) = T1a;
        *(LAS f32x4*)(size_t)(pw + 1024 + lane * 16) = T1b;
        if (g == 0) { f32x4 u = {T3a[0], T3a[1], T3b[2], T3b[3]}; *(LAS f32x4*)(size_t)(pw + (128 + c) * 16) = u; }
        if (c == 4 || c == 5 || c == 12) *(LAS f32x4*)(size_t)(pw + (144 + (c == 12 ? 2 : c - 4) * 4 + g) * 16) = T2a;
        if (c == 6 || c == 7 || c == 12) *(LAS f32x4*)(size_t)(pw + (156 + (c == 12 ? 2 : c - 6) * 4 + g) * 16) = T2b;
        if (g == 0 && ((c >= 4 && c < 8) || c == 12)) *(LAS f32x4*)(size_t)(pw + (168 + (c == 12 ? 4 : c - 4)) * 16) = T4;
        WAIT_L0();
        stsw(FL + 16 + wid * 4, 1u);
    }
    STAMP(layer * 4 + 0, 3);
    f32x4 cs2[3];
    cs2[0] = *(const f32x4*)(csq + 5 * 16 + 4 * g); cs2[1] = *(const f32x4*)(csq + 6 * 16 + 4 * g); cs2[2] = *(const f32x4*)(csq + 4 * 16 + 4 * g);
    SB();
    f32x4 qa[2][3];
#pragma unroll
    for (int f = 0; f < 3; ++f) { qa[0][f] = qa[1][f] = (f32x4){0.f, 0.f, 0.f, 0.f}; PINV(qa[0][f]); PINV(qa[1][f]); }
#define K1_W2(kt, f) ((f) < 2 ? LDSR(wb2, ((kt) * 2 + (f)) * 1024) : K1_W1(kt, 4))
    lds_wait(FL, 10u);
    bf16x8 wq[3][3];
#pragma unroll
    for (int k = 0; k < 3; ++k)
#pragma unroll
        for (int f = 0; f < 3; ++f) { wq[k][f] = K1_W2(k, f); SB(); }
#pragma unroll
    for (int kt = 0; kt < 18; ++kt) {
        if (kt == 3) lds_wait(FL, 11u);
        if (kt == 9) lds_wait(FL, 12u);
#pragma unroll
        for (int f = 0; f < 3; ++f) {
            MFMA(qa[0][f], wq[kt % 3][f], xa[0][kt]);
            SB();
            MFMA(qa[1][f], wq[kt % 3][f], xa[1][kt]);
            if (kt + 3 < 18) wq[kt % 3][f] = K1_W2(kt + 3, f);
            SB();
        }
    }
    asm volatile("s_nop 15" ::: "memory");
    STAMP(layer * 4 + 0, 4);
    bf16x8 PQ[2][2];
#pragma unroll
    for (int m = 0; m < 2; ++m) {
        const f32x2 st = *(LAS f32x2*)(size_t)(lds + K1_ST + (32 * wid + 16 * m + c) * 8);
        u32 w2[3][2];
#pragma unroll
        for (int f = 0; f < 3; ++f) {
            float v[4];
#pragma unroll
            for (int r = 0; r < 4; ++r) v[r] = phi(st[0] * qa[m][f][r] + st[1] * cs2[f][r]);
            w2[f][0] = pk2(v[0], v[1]); w2[f][1] = pk2(v[2], v[3]);
        }
        if (g != 2) { w2[2][0] = 0u; w2[2][1] = 0u; }
        PQ[m][0] = mk8(w2[0][0], w2[0][1], w2[2][0], w2[2][1]);
        PQ[m][1] = mk8(w2[1][0], w2[1][1], w2[2][0], w2[2][1]);
    }
    lds_wait(FL + 32 + (lane % 3) * 4, 1u);
    STAMP(layer * 4 + 0, 5);
    {
        const u32 OP = lds + K1_OP + lane * 16;
        const bf16x8 A1a = LDSR(OP, 0), A1b = LDSR(OP, 1024), A2a = LDSR(OP, 2048), A2b = LDSR(OP, 3072);
        const f32x4 z4 = {0.f, 0.f, 0.f, 0.f};
#pragma unroll
        for (int m = 0; m < 2; ++m) {
            const f32x4 O1a = MFMAB(A1a, PQ[m][0], z4), O1b = MFMAB(A1b, PQ[m][1], z4);
            f32x4 O2 = MFMAB(A2a, PQ[m][0], z4);
            O2 = MFMAB(A2b, PQ[m][1], O2);
            const float ia = 1.f / (O2[2] + 1e-6f), ib = 1.f / (O2[3] + 1e-6f);
            u32x4 o = {pk2(O1a[0] * ia, O1a[1] * ia), pk2(O1a[2] * ia, O1a[3] * ia), pk2(O1b[0] * ib, O1b[1] * ib), pk2(O1b[2] * ib, O1b[3] * ib)};
            char* ob = ws + WS_O + (8 * pm + 2 * wid + m) * 18432;
            *(u32x4*)(ob + p * 1024 + lane * 16) = o;
            const float ir = g == 0 ? ia : ib;
            if (g < 2) *(u32*)(ob + (16 + (p >> 3)) * 1024 + (c + 16 * ((p & 7) >> 1)) * 16 + (p & 1) * 8 + g * 4) = pk2(O2[0] * ir, O2[1] * ir);
        }
    }
    STAMP(layer * 4 + 0, 6);
}

constexpr u32 K2_ST = 4 * 64 * 76 * 4, K2_LDS = K2_ST + 4 * 64 * 8;
template <int KIND>
__global__ __launch_bounds__(256, 2) void k2_proj(char* __restrict__ ws, u16* __restrict__ out, int layer) {
    extern __shared__ __attribute__((aligned(16))) char shm_[];
    const u32 lds = (u32)(size_t)(LAS char*)shm_;
    const int tid = threadIdx.x, wid = __builtin_amdgcn_readfirstlane(tid >> 6), lane = tid & 63, c = lane & 15, g = lane >> 4;
    const int xcd = blockIdx.x & 7, bi = blockIdx.x >> 3;
    const int mt = (xcd >> 1) * 8 + (bi & 7), nt = (xcd & 1) * 4 + (bi >> 3);
    constexpr int NSL = KIND == 0 ? 5 : 18, RD = 4;
    constexpr int SK = KIND == 0 ? 1 : 3;
    STAMP(layer * 4 + SK, 0);
    const int sl0 = KIND == 0 ? (wid < 2 ? 5 * wid : 10 + 4 * (wid - 2)) : 18 * wid;
    const auto rs = __builtin_amdgcn_make_buffer_rsrc(ws, 0, WS_END, 0x00020000);
    const u32 ab = KIND == 0 ? WS_O + (4 * mt * 18 + sl0) * 1024 : WS_H + (4 * mt * 8 + 2 * wid) * 9216;
    constexpr int AST = KIND == 0 ? 18432 : 73728;
    const u32 wb = KIND == 0 ? WS_WO + (nt * 18 + sl0) * 4608 : WS_WOUT + (nt * 72 + sl0) * 4608;
    const u32 vo = lane * 16, vo8 = lane * 8, vo4 = 4096 + g * 128 + (lane & 7) * 16;
    bf16x8 fa[RD][4], fw[RD][5];
    f32x4 acc[4][5];
#pragma unroll
    for (int m = 0; m < 4; ++m)
#pragma unroll
        for (int n = 0; n < 5; ++n) acc[m][n] = (f32x4){0.f, 0.f, 0.f, 0.f};
#define K2_B128(v, sof) __builtin_bit_cast(bf16x8, __builtin_amdgcn_raw_buffer_load_b128(rs, v, sof, 0))
#define K2_LOAD(s) do {                                                                                              \
        _Pragma("unroll") for (int n = 0; n < 4; ++n) fw[(s) % RD][n] = K2_B128(vo + n * 1024, wb + (s) * 4608);     \
        fw[(s) % RD][4] = K2_B128(vo4, wb + (s) * 4608);                                                             \
        _Pragma("unroll") for (int m = 0; m < 4; ++m) {                                                              \
            if (KIND != 0 && (s) % 9 == 8) {                                                                         \
                const u32x2 h0 = __builtin_amdgcn_raw_buffer_load_b64(rs, vo8, ab + m * AST + (s) * 1024, 0);        \
                const u32x2 h1 = __builtin_amdgcn_raw_buffer_load_b64(rs, vo8 + 512, ab + m * AST + (s) * 1024, 0);  \
                fa[(s) % RD][m] = mk8(h0[0], h0[1], h1[0], h1[1]);                                                   \
            } else fa[(s) % RD][m] = K2_B128(vo + ((s) & 3) * 1024, ab + m * AST + ((s) & ~3) * 1024);               \
        } SB(); } while (0)
#define K2_MM(s) do { _Pragma("unroll") for (int m = 0; m < 4; ++m) _Pragma("unroll") for (int n = 0; n < 5; ++n)    \
        acc[m][n] = MFMAB(fw[(s) % RD][n], fa[(s) % RD][m], acc[m][n]); SB(); } while (0)
#pragma unroll
    for (int s = 0; s < RD; ++s) K2_LOAD(s);
    u32x4 res[3];
    const u32 xoff = WS_X + (4 * mt + g) * 18432 + c * 16;
#define K2_XA(i) (xoff + ((9 * nt + wid + 4 * (i)) >> 2) * 1024 + ((9 * nt + wid + 4 * (i)) & 3) * 256)
#define K2_RES() do { res[0] = *(const u32x4*)(ws + K2_XA(0)); res[1] = *(const u32x4*)(ws + K2_XA(1)); res[2] = *(const u32x4*)(ws + K2_XA(wid == 0 ? 2 : 1)); SB(); } while (0)
    if (KIND == 0) {
        K2_MM(0);
        if (wid < 2) K2_LOAD(4);
        K2_RES();
        K2_MM(1); K2_MM(2); K2_MM(3);
        if (wid < 2) K2_MM(4);
    } else {
#pragma unroll
        for (int s = 0; s < NSL; ++s) {
            K2_MM(s);
            if (s + RD < NSL) K2_LOAD(s + RD);
            if (s + RD == NSL) K2_RES();
        }
    }
    STAMP(layer * 4 + SK, 1);
#pragma unroll
    for (int m = 0; m < 4; ++m)
#pragma unroll
        for (int n = 0; n < 5; ++n)
            if (n < 4 || g < 2) *(LAS f32x4*)(size_t)(lds + wid * 19456 + ((16 * m + c) * 76 + 16 * n + 4 * g) * 4) = acc[m][n];
    __syncthreads();
    float s1 = 0.f, s2 = 0.f;
#pragma unroll
    for (int i = 0; i < 3; ++i) {
        if (i < 2 || wid == 0) {
            const int ch = wid + 4 * i;
            f32x4 a = {bflo(res[i][0]), bfhi(res[i][0]), bflo(res[i][1]), bfhi(res[i][1])}, b = {bflo(res[i][2]), bfhi(res[i][2]), bflo(res[i][3]), bfhi(res[i][3])};
            f32x4 sa = {0.f, 0.f, 0.f, 0.f}, sb = sa;
#pragma unroll
            for (int k = 0; k < 4; ++k) {
                sa += *(LAS f32x4*)(size_t)(lds + k * 19456 + (lane * 76 + 8 * ch) * 4);
                sb += *(LAS f32x4*)(size_t)(lds + k * 19456 + (lane * 76 + 8 * ch) * 4 + 16);
            }
            a += sa; b += sb;
            u32x4 o = {pk2(a[0], a[1]), pk2(a[2], a[3]), pk2(b[0], b[1]), pk2(b[2], b[3])};
            if (KIND == 2) *(u32x4*)(out + (size_t)(64 * mt + lane) * 576 + 72 * nt + 8 * ch) = o;
            else {
                *(u32x4*)(ws + K2_XA(i)) = o;
#pragma unroll
                for (int k = 0; k < 4; ++k) { const float u = bflo(o[k]), v = bfhi(o[k]); s1 += u + v; s2 += u * u + v * v; }
            }
        }
    }
    if (KIND != 2) {
        f32x2 st = {s1, s2};
        *(LAS f32x2*)(size_t)(lds + K2_ST + (wid * 64 + lane) * 8) = st;
        __syncthreads();
        if (wid == 0) {
#pragma unroll
            for (int k = 1; k < 4; ++k) st += *(LAS f32x2*)(size_t)(lds + K2_ST + (k * 64 + lane) * 8);
            *(f32x2*)(ws + WS_XS + (64 * mt + lane) * 64 + nt * 8) = st;
        }
    }
    STAMP(layer * 4 + SK, 2);
}

constexpr u32 STAGE = 18432, NS = 6;
constexpr u32 K3_PK = NS * STAGE, K3_PP = 304, K3_WS = K3_PK + 128 * K3_PP, K3_S1 = K3_WS + 4096, K3_S2 = K3_S1 + 2048, K3_FL = K3_S2 + 1024, K3_LDS = K3_FL + 128;

__global__ __launch_bounds__(512, 2) void k34_mlp(char* __restrict__ ws, const u16* __restrict__ wsp, const u16* __restrict__ bsp, int layer) {
    extern __shared__ __attribute__((aligned(16))) char shm_[];
    const u32 lds = (u32)(size_t)(LAS char*)shm_;
    const int tid = threadIdx.x, wid = __builtin_amdgcn_readfirstlane(tid >> 6), lane = tid & 63, c = lane & 15, g = lane >> 4;
    const int xcd = blockIdx.x & 7, bi = blockIdx.x >> 3;
    const int pm = (xcd & 1) * 8 + (bi & 7), pn = (xcd >> 1) * 4 + (bi >> 3);
    const u32 FL = lds + K3_FL;
    const auto rs = __builtin_amdgcn_make_buffer_rsrc(ws, 0, WS_END, 0x00020000);
    if (wid == 4) {
        u32 ao[16];
        const u32 vo = lane * 16;
#pragma unroll
        for (int q = 0; q < 16; ++q) { ao[q] = vo + (q >> 1) * 18432 - (q & 2) * 1024 + 4096; PINV(ao[q]); }
        const char* ap = ws + WS_X + 8 * pm * 18432 - 4096;
        const char* xp = ws + WS_WIN + (144 + 9 * pn + 8) * 18432;
        u32 slot = 0;
        for (int s = 0; s < 53; ++s) {
            if (s >= 3) {
                if (s <= 18) WAIT_V(36); else if (s == 19) WAIT_V(34); else if (s <= 50) WAIT_V(32); else if (s == 51) WAIT_V(16); else WAIT_V(0);
                stsw(FL, s - 2);
                FENCE();
            }
            if (s == 2) BAR();
            if (s < 50) {
                if (s >= (int)NS) lds_wait_sleep(FL + 16 + (lane & 3) * 4, s - NS + 1);
                if (s == 9) { ap -= 9 * 2048; xp = ws + WS_WIN + (9 * pn + 8) * 18432; }
                if (s == 18) {
#pragma unroll
                    for (int q = 0; q < 16; ++q) { ao[q] = (q * 8 + (lane >> 3)) * 4096 + (((lane & 7) ^ ((q & 1) * 4 + g)) * 16) - (q & 3) * 1024 + 4096; PINV(ao[q]); }
                    ap = (const char*)wsp + (size_t)pm * 128 * 4096 - 4096;
                    asm volatile("s_nop 4");
                }
                const u32 dst = lds + slot * STAGE;
                DMA4(ao[0], ao[1], ao[2], ao[3], ap, dst);
                DMA4(ao[4], ao[5], ao[6], ao[7], ap, dst + 4096);
                DMA4(ao[8], ao[9], ao[10], ao[11], ap, dst + 8192);
                DMA4(ao[12], ao[13], ao[14], ao[15], ap, dst + 12288);
                if (s < 18) { DMA2(vo, vo, xp, dst + 16384); xp += 2048; ap += 2048; } else ap += 128;
                slot = slot + 1 == NS ? 0 : slot + 1;
            }
        }
        return;
    }
    if (wid > 4) {
        const int ht = tid - 320;
        if (ht >= 128 && ht < 160) stsw(FL + (ht - 128) * 4, ht == 129 ? 18u : 0u);
        float mean = 0.f, var = 0.f;
        if (ht < 128) x_stats(ws, 128 * pm + ht, mean, var);
        WAIT_L0();
        BAR();
        if (ht < 128) {
            const float r = __builtin_amdgcn_rsqf(var + 1e-5f);
            f32x4 o = {-mean, -2.302208198f * r, -0.1029432397f * r * r * r, (var + 1e-5f) * r};
            *(LAS f32x4*)(size_t)(lds + K3_S1 + ht * 16) = o;
            WAIT_L0();
            stsw(FL + 32 + (wid - 5) * 4, 1u);
        }
        lds_wait_sleep(FL + 48 + (lane & 3) * 4, 1u);
        STAMP(layer * 4 + 2, 8);
        if (ht < 128) {
            f32x2 st = *(LAS f32x2*)(size_t)(lds + K3_WS + ht * 8);
#pragma unroll
            for (int k = 1; k < 4; ++k) st += *(LAS f32x2*)(size_t)(lds + K3_WS + (k * 128 + ht) * 8);
            u32 b0 = __float_as_uint(st[0]), b1 = __float_as_uint(st[1]);
            b0 = b0 == SENT ? 0x7fc00000u : b0; b1 = b1 == SENT ? 0x7fc00000u : b1;
            GAS u64* zs = (GAS u64*)(ws + WS_ZST + (layer * 16 + pm) * 16384) + ht;
            __hip_atomic_store(zs + pn * 128, ((u64)b1 << 32) | b0, RLX_AGENT);
            u64 t[16];
            for (u32 spins = 0; spins < SPIN_CAP; ++spins) {
                bool bad = false;
#pragma unroll
                for (int k = 0; k < 16; ++k) t[k] = __hip_atomic_load(zs + k * 128, RLX_AGENT);
#pragma unroll
                for (int k = 0; k < 16; ++k) bad = bad || (u32)t[k] == SENT;
                if (!__any(bad)) break;
                __builtin_amdgcn_s_sleep(2);
            }
            float s1 = 0.f, s2 = 0.f;
#pragma unroll
            for (int k = 0; k < 16; ++k) { s1 += __uint_as_float((u32)t[k]); s2 += __uint_as_float((u32)(t[k] >> 32)); }
            const float m2 = s1 * (1.f / 2304.f), v2 = fmaxf(s2 * (1.f / 2304.f) - m2 * m2, 0.f);
            const float r2 = __builtin_amdgcn_rsqf(v2 + 1e-5f);
            f32x2 o = {r2, -m2 * r2};
            *(LAS f32x2*)(size_t)(lds + K3_S2 + ht * 8) = o;
            WAIT_L0();
            stsw(FL + 64 + (wid - 5) * 4, 1u);
        }
        lds_wait(FL + 64 + (lane & 1) * 4, 1u);
        STAMP(layer * 4 + 2, 9);
        {
            const int h = wid - 5;
#pragma unroll
            for (int q = 0; q < 4; ++q) {
                f32x4 sv[4];
#pragma unroll
                for (int k = 0; k < 4; ++k) sv[k] = *(LAS f32x4*)(size_t)(lds + K3_S2 + (32 * q + 8 * g) * 8 + k * 16);
#pragma unroll
                for (int k = 0; k < 3; ++k) {
                    const int fi = 3 * h + k;
                    const u32 pos = fi < 8 ? 32 * (fi >> 1) + 8 * (c & 3) + 4 * (fi & 1) : 128 + 4 * (c & 3);
                    const u32 a = lds + K3_PK + (32 * q + 8 * g + (c >> 2)) * K3_PP + pos * 2;
                    u32x2 t0, t1;
                    asm volatile("ds_read_b64_tr_b16 %0, %2\n\tds_read_b64_tr_b16 %1, %2 offset:%3\n\ts_waitcnt lgkmcnt(0)" : "=&v"(t0), "=&v"(t1) : "v"(a), "i"(4 * K3_PP) : "memory");
                    u32x4 o;
                    o[0] = pk2(bflo(t0[0]) * sv[0][0] + sv[0][1], bfhi(t0[0]) * sv[0][2] + sv[0][3]);
                    o[1] = pk2(bflo(t0[1]) * sv[1][0] + sv[1][1], bfhi(t0[1]) * sv[1][2] + sv[1][3]);
                    o[2] = pk2(bflo(t1[0]) * sv[2][0] + sv[2][1], bfhi(t1[0]) * sv[2][2] + sv[2][3]);
                    o[3] = pk2(bflo(t1[1]) * sv[3][0] + sv[3][1], bfhi(t1[1]) * sv[3][2] + sv[3][3]);
                    __builtin_amdgcn_raw_buffer_store_b128(o, rs, lane * 16, WS_Z + ((9 * pn + fi) * 64 + 4 * pm + q) * 1024, 16);
                }
            }
        }
        WAIT_V(0);
        STAMP(layer * 4 + 2, 10);
        GAS u32* cnt = (GAS u32*)(ws + WS_CNT + (layer * 16 + pn) * 256);
        if (lane == 0) {
            const u32 old = __hip_atomic_fetch_add((LAS u32*)(size_t)(FL + 80), 1u, __ATOMIC_RELAXED, __HIP_MEMORY_SCOPE_WORKGROUP);
            if (old == 2) __hip_atomic_fetch_add(cnt, 1u, RLX_AGENT);
        }
        if (wid != 5) return;
        for (u32 spins = 0; spins < SPIN_CAP; ++spins) {
            if (__builtin_amdgcn_readfirstlane(__hip_atomic_load(cnt, RLX_AGENT)) >= 16u) break;
            __builtin_amdgcn_s_sleep(1);
        }
        FENCE();
        stsw(FL + 84, 1u);
        FENCE();
        STAMP(layer * 4 + 2, 11);
        const u32 zx = WS_Z + (9 * pn + 8) * 65536;
        u32x4 xr[4][2];
#pragma unroll
        for (int k = 0; k < 4; ++k) {
            xr[k][0] = __builtin_amdgcn_raw_buffer_load_b128(rs, lane * 16, zx + k * 2048, 16);
            xr[k][1] = __builtin_amdgcn_raw_buffer_load_b128(rs, lane * 16 + 1024, zx + k * 2048, 16);
            SB();
        }
        u32 slot = 0;
        for (int s0 = 0; s0 < 32; s0 += 4) {
#pragma unroll
            for (int k = 0; k < 4; ++k) {
                const int s = s0 + k;
                lds_wait_sleep(FL + 16 + (lane & 3) * 4, 18 + s - NS + 1);
                *(LAS u32x4*)(size_t)(lds + slot * STAGE + 16384 + lane * 16) = xr[k][0];
                *(LAS u32x4*)(size_t)(lds + slot * STAGE + 17408 + lane * 16) = xr[k][1];
                const u32 sn = s + 4 < 32 ? s + 4 : 31;
                xr[k][0] = __builtin_amdgcn_raw_buffer_load_b128(rs, lane * 16, zx + sn * 2048, 16);
                xr[k][1] = __builtin_amdgcn_raw_buffer_load_b128(rs, lane * 16 + 1024, zx + sn * 2048, 16);
                WAIT_L0();
                stsw(FL + 4, 18 + s + 1);
                FENCE();
                slot = slot + 1 == NS ? 0 : slot + 1;
            }
        }
        return;
    }
    STAMP(layer * 4 + 2, 0);
    u32 vA = lane * 16, vB = lane * 16 + 18432;
    u32 so = WS_WIN + (144 + 9 * pn + 2 * wid) * 18432;
    bf16x8 bb[6][2];
#define BLD(voff, imm, aux) __builtin_bit_cast(bf16x8, __builtin_amdgcn_raw_buffer_load_b128(rs, (voff) + (imm), so, aux))
#define BPRO(aux) do { _Pragma("unroll") for (int r = 0; r < 5; ++r) { if (r == 3) so += 3072;                       \
        bb[r][0] = BLD(vA, (r % 3) * 1024, aux); SB(); bb[r][1] = BLD(vB, (r % 3) * 1024, aux); SB(); } } while (0)
    const float* csin = (const float*)(ws + WS_CSIN) + 144 * pn + 4 * g;
    f32x4 cs[2][3];
#pragma unroll
    for (int ph = 0; ph < 2; ++ph) {
        cs[ph][0] = *(const f32x4*)(csin + (1 - ph) * 2304 + 32 * wid); cs[ph][1] = *(const f32x4*)(csin + (1 - ph) * 2304 + 32 * wid + 16);
        cs[ph][2] = *(const f32x4*)(csin + (1 - ph) * 2304 + 128);
    }
    SB();
    BPRO(0);
    u32 pa[2][4];
#pragma unroll
    for (int q = 0; q < 4; ++q) { pa[0][q] = lds + lane * 16 + ((q ^ wid) * 4096); pa[1][q] = pa[0][q] + 1024; }
    u32 px = lds + 16384 + lane * 16;
    u32 myfree = FL + 16 + wid * 4, fladdr = FL + (lane & 1) * 4, cnt = 0;
    PINV(cnt); PINV(myfree); PINV(fladdr);
    int dstep = 3 * STAGE;
    f32x4 acc[8][2], accx[2];
    bf16x8 a[8], xf;
#define ALOAD() do { a[0] = LDSR(pa[0][0], 0); SB(); xf = LDSR(px, 0); SB();                                         \
        _Pragma("unroll") for (int m = 1; m < 8; ++m) { a[m] = LDSR(pa[0][m >> 1], (m & 1) * 2048); SB(); } } while (0)
#define HALF(r, RD, kn, sf, rl, imm, aux, E0, E1, E2, E3, E4, E5) do {                                               \
        WCNT(8, 5); MFMA(acc[0][0], bb[r][0], a[0]); E0; SB();                                                       \
        MFMA(acc[0][1], bb[r][1], a[0]); E1; SB();                                                                   \
        MFMA(accx[0], xf, a[0]); if (RD) a[0] = LDSR(pa[kn][0], (sf)); SB();                                         \
        MFMA(accx[1], xf, a[1]); if (RD) xf = LDSR(px, (sf) + (kn) * 1024); SB();                                    \
        MFMA(acc[1][1], bb[r][1], a[1]); bb[rl][0] = BLD(vA, imm, aux); SB();                                        \
        MFMA(acc[1][0], bb[r][0], a[1]); if (RD) a[1] = LDSR(pa[kn][0], (sf) + 2048); SB();                          \
        MFMA(acc[2][0], bb[r][0], a[2]); E2; SB();                                                                   \
        MFMA(acc[2][1], bb[r][1], a[2]); if (RD) a[2] = LDSR(pa[kn][1], (sf)); SB();                                 \
        WCNT(63, 5); MFMA(acc[3][1], bb[r][1], a[3]); SB();                                                          \
        MFMA(acc[3][0], bb[r][0], a[3]); if (RD) a[3] = LDSR(pa[kn][1], (sf) + 2048); SB();                          \
        MFMA(acc[4][0], bb[r][0], a[4]); bb[rl][1] = BLD(vB, imm, aux); SB();                                        \
        MFMA(acc[4][1], bb[r][1], a[4]); if (RD) a[4] = LDSR(pa[kn][2], (sf)); SB();                                 \
        MFMA(acc[5][1], bb[r][1], a[5]); E3; SB();                                                                   \
        MFMA(acc[5][0], bb[r][0], a[5]); if (RD) a[5] = LDSR(pa[kn][2], (sf) + 2048); SB();                          \
        MFMA(acc[6][0], bb[r][0], a[6]); E4; SB();                                                                   \
        MFMA(acc[6][1], bb[r][1], a[6]); if (RD) a[6] = LDSR(pa[kn][3], (sf)); SB();                                 \
        MFMA(acc[7][1], bb[r][1], a[7]); E5; SB();                                                                   \
        MFMA(acc[7][0], bb[r][0], a[7]); if (RD) a[7] = LDSR(pa[kn][3], (sf) + 2048); SB(); } while (0)
#define STEP(u, chk, nso, aux) do { u32 fl_; u64 msk_;                                                               \
        HALF(2 * (u), 1, 1, (u) * STAGE, (2 * (u) + 5) % 6, ((2 * (u) + 5) % 3) * 1024, aux,                         \
             { if ((u) == 2) so += 3072; }, { cnt += 1; PINV(cnt); }, { FENCE(); fl_ = ldsw(fladdr); FENCE(); },     \
             { if ((u) == 2) { pa[0][0] += dstep; pa[0][1] += dstep; PINV(pa[0][0]); PINV(pa[0][1]); } },            \
             { if ((u) == 2) { pa[0][2] += dstep; pa[0][3] += dstep; PINV(pa[0][2]); PINV(pa[0][3]); } },            \
             { asm volatile("v_cmp_le_u32 %0, %1, %2" : "=s"(msk_) : "v"(fl_), "v"(cnt)); if ((u) == 2) { px += dstep; PINV(px); } }); \
        if (__builtin_expect(msk_ != 0, 0)) { if (chk) lds_wait(fladdr, cnt + 1); } \
        FENCE();                                                                                                     \
        HALF(2 * (u) + 1, 1, 0, (((u) + 1) % 3) * STAGE, (2 * (u) + 6) % 6, ((2 * (u) + 6) % 3) * 1024, aux,         \
             { FENCE(); stsw(myfree, cnt); FENCE(); }, { if ((u) == 0) so = (nso); },                                \
             { if ((u) == 2) { pa[1][0] += dstep; pa[1][1] += dstep; PINV(pa[1][0]); PINV(pa[1][1]); } },            \
             { if ((u) == 2) { pa[1][2] += dstep; pa[1][3] += dstep; PINV(pa[1][2]); PINV(pa[1][3]); } },            \
             { }, { if ((u) == 2) dstep = -dstep; }); } while (0)
#define PINACC() do { _Pragma("unroll") for (int m = 0; m < 8; ++m) { PINV(acc[m][0]); PINV(acc[m][1]); } PINV(accx[0]); PINV(accx[1]); asm volatile("s_nop 1"); } while (0)
    BAR();
    lds_wait(FL + 32 + (lane & 1) * 4, 1u);
    lds_wait(fladdr, 1u);
    ALOAD();
    STAMP(layer * 4 + 2, 1);
    u32 yp[18][2];
    const u32 s1a = lds + K3_S1 + c * 16;
#pragma unroll
    for (int ph = 0; ph < 2; ++ph) {
#pragma unroll
        for (int m = 0; m < 8; ++m) {
            const float nm = *(LAS float*)(size_t)(s1a + ((((m >> 1) ^ wid) << 1) + (m & 1)) * 256);
            acc[m][0] = cs[ph][0] * nm; acc[m][1] = cs[ph][1] * nm;
            if (m < 2) accx[m] = cs[ph][2] * nm;
        }
        PINACC();
        const u32 nxt = WS_WIN + (9 * pn + 2 * wid) * 18432;
        for (int j = 0; j < 3; ++j) {
            STEP(0, true, j == 2 ? nxt : so + 3072, 0);
            STEP(1, true, 0, 0);
            STEP(2, ph == 0 || j < 2, 0, 0);
        }
        asm volatile("s_nop 15" ::: "memory");
        STAMP(layer * 4 + 2, 2 + 2 * ph);
        float t1[8], t2[8];
#pragma unroll
        for (int m = 0; m < 8; ++m) {
            const f32x4 rc = *(LAS f32x4*)(size_t)(s1a + ((((m >> 1) ^ wid) << 1) + (m & 1)) * 256);
            float u1 = 0.f, u2 = 0.f;
#pragma unroll
            for (int n = 0; n < 3; ++n) {
                if (n == 2 && m >= 2) continue;
                const f32x4 av = n < 2 ? acc[m][n] : accx[m & 1];
                float y[4];
#pragma unroll
                for (int r = 0; r < 4; ++r) {
                    const float v = av[r];
                    const float e = __builtin_amdgcn_exp2f(v * (v * v * rc[2] + rc[1]));
                    y[r] = v * __builtin_amdgcn_rcpf(e * rc[3] + rc[3]);
                    if (ph == 0) { u1 += y[r]; u2 += y[r] * y[r]; }
                }
                const int id = n < 2 ? 2 * m + n : 16 + m;
                yp[id][0] = pk2(y[0], y[1]); yp[id][1] = pk2(y[2], y[3]);
            }
            t1[m] = u1; t2[m] = u2;
            PINV(yp[2 * m][0]); PINV(yp[2 * m][1]); PINV(yp[2 * m + 1][0]); PINV(yp[2 * m + 1][1]);
            if (m < 2) { PINV(yp[16 + m][0]); PINV(yp[16 + m][1]); }
            if (ph == 0) {
                const u32 row = (((((m >> 1) ^ wid) << 1) + (m & 1)) << 4) + c;
                u32x4 o = {yp[2 * m][0], yp[2 * m][1], yp[2 * m + 1][0], yp[2 * m + 1][1]};
                *(LAS u32x4*)(size_t)(lds + K3_PK + row * K3_PP + 64 * wid + 16 * g) = o;
                if (m < 2) { u32x2 ox = {yp[16 + m][0], yp[16 + m][1]}; *(LAS u32x2*)(size_t)(lds + K3_PK + row * K3_PP + 256 + 8 * g) = ox; }
            }
        }
        if (ph == 0) {
#pragma unroll
            for (int m = 0; m < 4; ++m) {
                auto r1 = __builtin_amdgcn_permlane32_swap(__float_as_uint(t1[m]), __float_as_uint(t1[m + 4]), false, false);
                auto r2 = __builtin_amdgcn_permlane32_swap(__float_as_uint(t2[m]), __float_as_uint(t2[m + 4]), false, false);
                t1[m] = __uint_as_float(r1[0]) + __uint_as_float(r1[1]); t2[m] = __uint_as_float(r2[0]) + __uint_as_float(r2[1]);
            }
#pragma unroll
            for (int m = 0; m < 2; ++m) {
                auto r1 = __builtin_amdgcn_permlane16_swap(__float_as_uint(t1[m]), __float_as_uint(t1[m + 2]), false, false);
                auto r2 = __builtin_amdgcn_permlane16_swap(__float_as_uint(t2[m]), __float_as_uint(t2[m + 2]), false, false);
                f32x2 o = {__uint_as_float(r1[0]) + __uint_as_float(r1[1]), __uint_as_float(r2[0]) + __uint_as_float(r2[1])};
                *(LAS f32x2*)(size_t)(lds + K3_WS + (wid * 128 + 32 * (g ^ wid) + 16 * m + c) * 8) = o;
            }
            WAIT_L0();
            stsw(FL + 48 + wid * 4, 1u);
            FENCE();
        }
        STAMP(layer * 4 + 2, 3 + 2 * ph);
    }
    {
        float bv[8];
#pragma unroll
        for (int m = 0; m < 8; ++m) bv[m] = bflo(bsp[128 * pm + (((((m >> 1) ^ wid) << 1) + (m & 1)) << 4) + c]);
#pragma unroll
        for (int m = 0; m < 8; ++m) {
            acc[m][0] = acc[m][1] = (f32x4){bv[m], bv[m], bv[m], bv[m]};
            if (m < 2) accx[m] = acc[m][0];
        }
    }
#pragma unroll
    for (int q = 0; q < 4; ++q) { pa[0][q] = lds + (c >> 3) * 1024 + (c & 7) * 128 + ((g ^ ((c >> 1) & 7)) * 16) + ((q ^ wid) * 4096); pa[1][q] = pa[0][q] ^ 64; }
    vB = lane * 16 + 65536;
    so = WS_Z + (9 * pn + 2 * wid) * 65536;
    lds_wait(FL + 84, 1u);
    STAMP(layer * 4 + 2, 6);
    BPRO(16);
    lds_wait(fladdr, 19u);
    ALOAD();
    PINACC();
    for (int j = 0; j < 10; ++j) {
        STEP(0, true, so + 3072, 16);
        STEP(1, true, 0, 16);
        STEP(2, true, 0, 16);
    }
    so -= 8192;
    STEP(0, true, so, 16);
    STEP(1, false, 0, 16);
    asm volatile("s_nop 15" ::: "memory");
    STAMP(layer * 4 + 2, 7);
    {
        char* hb = ws + WS_H + (8 * pm * 8 + (pn >> 1)) * 9216;
#pragma unroll
        for (int m = 0; m < 8; ++m) {
            const int mt = (((m >> 1) ^ wid) << 1) + (m & 1);
            u32x4 o;
            o[0] = pk2(bflo(yp[2 * m][0]) * acc[m][0][0], bfhi(yp[2 * m][0]) * acc[m][0][1]);
            o[1] = pk2(bflo(yp[2 * m][1]) * acc[m][0][2], bfhi(yp[2 * m][1]) * acc[m][0][3]);
            o[2] = pk2(bflo(yp[2 * m + 1][0]) * acc[m][1][0], bfhi(yp[2 * m + 1][0]) * acc[m][1][1]);
            o[3] = pk2(bflo(yp[2 * m + 1][1]) * acc[m][1][2], bfhi(yp[2 * m + 1][1]) * acc[m][1][3]);
            *(u32x4*)(hb + mt * 73728 + ((pn & 1) * 4 + wid) * 1024 + lane * 16) = o;
            if (m < 2) {
                u32x2 ox = {pk2(bflo(yp[16 + m][0]) * accx[m][0], bfhi(yp[16 + m][0]) * accx[m][1]), pk2(bflo(yp[16 + m][1]) * accx[m][2], bfhi(yp[16 + m][1]) * accx[m][3])};
                *(u32x2*)(hb + mt * 73728 + 8192 + (pn & 1) * 512 + lane * 8) = ox;
            }
        }
    }
    STAMP(layer * 4 + 2, 12);
}

extern "C" void kernel_launch(void* const* d_in, const int* in_sizes, int n_in, void* d_out, int out_size, void* d_ws, size_t ws_size,
                              hipStream_t stream) {
    const u16* x = (const u16*)d_in[0];
    const u16* wqkv = (const u16*)d_in[1];
    const u16* wo = (const u16*)d_in[2];
    const u16* win = (const u16*)d_in[4];
    const u16* wsp = (const u16*)d_in[5];
    const u16* bsp = (const u16*)d_in[6];
    const u16* wout = (const u16*)d_in[7];
    char* ws = (char*)d_ws;
    u16* out = (u16*)d_out;
    (void)hipFuncSetAttribute((const void*)k1_attn, hipFuncAttributeMaxDynamicSharedMemorySize, K1_LDS);
    (void)hipFuncSetAttribute((const void*)k34_mlp, hipFuncAttributeMaxDynamicSharedMemorySize, K3_LDS);
    (void)hipFuncSetAttribute((const void*)k2_proj<0>, hipFuncAttributeMaxDynamicSharedMemorySize, K2_LDS);
    (void)hipFuncSetAttribute((const void*)k2_proj<1>, hipFuncAttributeMaxDynamicSharedMemorySize, K2_LDS);
    (void)hipFuncSetAttribute((const void*)k2_proj<2>, hipFuncAttributeMaxDynamicSharedMemorySize, K2_LDS);
    k0_prep<<<256, 576, 0, stream>>>(x, wqkv, wo, win, wout, ws);
    for (int layer = 0; layer < 2; ++layer) {
        k1_attn<<<256, 512, K1_LDS, stream>>>(ws, layer);
        k2_proj<0><<<256, 256, K2_LDS, stream>>>(ws, out, layer);
        k34_mlp<<<256, 512, K3_LDS, stream>>>(ws, wsp, bsp, layer);
        if (layer == 0) k2_proj<1><<<256, 256, K2_LDS, stream>>>(ws, out, layer);
        else k2_proj<2><<<256, 256, K2_LDS, stream>>>(ws, out, layer);
    }
}
```

```cpp
#include <hip/hip_runtime.h>
#include <hip/hip_bf16.h>
#include <stdint.h>

#define LAS __attribute__((address_space(3)))
#define GAS __attribute__((address_space(1)))
typedef unsigned short u16;
typedef unsigned u32;
typedef unsigned long long u64;
typedef short bf16x8 __attribute__((ext_vector_type(8)));
typedef float f32x2 __attribute__((ext_vector_type(2)));
typedef float f32x4 __attribute__((ext_vector_type(4)));
typedef unsigned u32x2 __attribute__((ext_vector_type(2)));
typedef unsigned u32x4 __attribute__((ext_vector_type(4)));
typedef __bf16 bf2_t __attribute__((ext_vector_type(2)));

#define ANY(p) (__builtin_amdgcn_ballot_w64(p) != 0)
#define U2F(x) __builtin_bit_cast(float, (u32)(x))
#define F2U(x) __builtin_bit_cast(u32, (float)(x))
#define WAIT_V(n) asm volatile("s_waitcnt vmcnt(%0)" ::"i"(n) : "memory")
#define WAIT_L0() asm volatile("s_waitcnt lgkmcnt(0)" ::: "memory")
#define FENCE() asm volatile("" ::: "memory")
#define SB() __builtin_amdgcn_sched_barrier(0)
#define BAR() do { FENCE(); __builtin_amdgcn_s_barrier(); FENCE(); } while (0)
#define WCNT(vm, lg) __builtin_amdgcn_s_waitcnt(((vm) & 15) | (7 << 4) | ((lg) << 8) | (((vm) >> 4) << 14))
#define MFMA(acc, x, y) asm volatile("v_mfma_f32_16x16x32_bf16 %0, %1, %2, %0" : "+v"(acc) : "v"(x), "v"(y))
#define MFMAB(x, y, c) __builtin_amdgcn_mfma_f32_16x16x32_bf16(x, y, c, 0, 0, 0)
#define BST128(o, rs, vo, so, aux) do { __builtin_amdgcn_raw_buffer_store_b128(o, rs, vo, so, aux); SB(); asm volatile("s_nop 1"); SB(); } while (0)
#define PINV(x) asm volatile("" : "+v"(x))
#define RLX_AGENT __ATOMIC_RELAXED, __HIP_MEMORY_SCOPE_AGENT

constexpr int S_ = 2048, D_ = 576, FH_ = 2304;
#define XDELAY1 10
#define XDELAY3 22
#define LGATE 12
#define LGATE2 14
#define POLLD 40
#define STAG 3
#define HPRIO 3
#define FDELAY 24
#define STAG2 60
#define MAP1 1
#define MAP3 1
constexpr u32 SENT = 0xFFFFFFFFu;
constexpr u32 SPIN_CAP = 1u << 16;

constexpr u32 WS_WQ = 0;
constexpr u32 WS_CSQ = WS_WQ + 16 * 129024;
constexpr u32 WS_WO = WS_CSQ + 16384;
constexpr u32 WS_WIN = WS_WO + 8 * 18 * 4608;
constexpr u32 WS_CSIN = WS_WIN + 288 * 18 * 1024;
constexpr u32 WS_WOUT = WS_CSIN + 2 * 4608 * 4;
constexpr u32 WS_X = WS_WOUT + 8 * 72 * 4608;
constexpr u32 XB = 128 * 18 * 1024, XSB = 2048 * 64, ZB = 144 * 64 * 1024;
constexpr u32 WS_XS = WS_X + 4 * XB;
constexpr u32 WS_O = WS_XS + 4 * XSB;
constexpr u32 WS_Z = WS_O + 2 * XB;
constexpr u32 WS_H = WS_Z + 2 * ZB;
constexpr u32 WS_KVP = WS_H + 2 * ZB;
constexpr u32 WS_ZST = WS_KVP + 2 * 256 * 2816;
constexpr u32 WS_CNT = WS_ZST + 2 * 256 * 1024;
constexpr u32 WS_END = WS_CNT + 2 * 16 * 32 * 128;
constexpr u32 WS_DBG = WS_END;

#define STAMP_DECL
#define STAMP(k, i) do { } while (0)
#define HST(i) do { } while (0)
#define HST_DECL
#define HST_OUT(k) do { } while (0)
#define STAMPV(k, i, v) do { } while (0)
#define STAMP_OUT(k) do { } while (0)

__device__ __forceinline__ u32 pk2(float lo, float hi) {
    f32x2 v = {lo, hi};
    bf2_t b = __builtin_convertvector(v, bf2_t);
    return __builtin_bit_cast(u32, b);
}
__device__ __forceinline__ float bflo(u32 w) { return U2F(w << 16); }
__device__ __forceinline__ float bfhi(u32 w) { return U2F(w & 0xffff0000u); }
__device__ __forceinline__ bf16x8 mk8(u32 a, u32 b, u32 c, u32 d) { u32x4 v = {a, b, c, d}; return __builtin_bit_cast(bf16x8, v); }
__device__ __forceinline__ float sfma(float a, float b, float c) { float r; asm("v_fma_f32 %0, %1, %2, %3" : "=v"(r) : "v"(a), "v"(b), "v"(c)); return r; }
__device__ __forceinline__ f32x4 sadd4(f32x4 a, f32x4 b) {
    float r0 = a[0], r1 = a[1], r2 = a[2], r3 = a[3];
    asm("v_add_f32 %0, %0, %4\n\tv_add_f32 %1, %1, %5\n\tv_add_f32 %2, %2, %6\n\tv_add_f32 %3, %3, %7" : "+v"(r0), "+v"(r1), "+v"(r2), "+v"(r3) : "v"(b[0]), "v"(b[1]), "v"(b[2]), "v"(b[3]));
    f32x4 r = {r0, r1, r2, r3};
    return r;
}
__device__ __forceinline__ float phi(float t) {
    float e = __builtin_amdgcn_exp2f(t * 1.44269504089f);
    return (t > 0.f ? t + 1.f : e) + 1e-6f;
}
__device__ __forceinline__ u32 ldsw(u32 a) { return *(volatile LAS u32*)(size_t)a; }
__device__ __forceinline__ void stsw(u32 a, u32 v) { *(volatile LAS u32*)(size_t)a = v; }
#define LDSR(addr, off) (*(LAS bf16x8*)((LAS char*)(size_t)(addr) + (off)))
__device__ __forceinline__ void lds_wait(u32 a, u32 target) {
    FENCE();
#pragma nounroll
    for (u32 n = 0; n < SPIN_CAP; ++n) { const u32 v = ldsw(a); if (!ANY(v < target)) break; }
    FENCE();
}
__device__ __forceinline__ void lds_wait_sleep(u32 a, u32 target) {
    FENCE();
#pragma nounroll
    for (u32 n = 0; n < SPIN_CAP; ++n) { const u32 v = ldsw(a); if (!ANY(v < target)) break; __builtin_amdgcn_s_sleep(1); }
    FENCE();
}
#define DMA4(v0, v1, v2, v3, sbase, m0v)                                                                            \
    asm volatile("s_mov_b32 m0, %5\n\ts_nop 0\n\tglobal_load_lds_dwordx4 %0, %4\n\t"                                \
                 "global_load_lds_dwordx4 %1, %4 offset:1024\n\tglobal_load_lds_dwordx4 %2, %4 offset:2048\n\t"     \
                 "global_load_lds_dwordx4 %3, %4 offset:3072" ::"v"(v0), "v"(v1), "v"(v2), "v"(v3), "s"(sbase), "s"(m0v) : "memory")
#define DMA2(v0, v1, sbase, m0v)                                                                                    \
    asm volatile("s_mov_b32 m0, %3\n\ts_nop 0\n\tglobal_load_lds_dwordx4 %0, %2\n\t"                                \
                 "global_load_lds_dwordx4 %1, %2 offset:1024" ::"v"(v0), "v"(v1), "s"(sbase), "s"(m0v) : "memory")

__device__ __forceinline__ u32x4 tcol(const u32x2 (&ld)[8], int i) {
    const u32 sel = (i & 1) ? 0x07060302u : 0x05040100u;
    u32x4 o;
    o.x = __builtin_amdgcn_perm(ld[1][i >> 1], ld[0][i >> 1], sel);
    o.y = __builtin_amdgcn_perm(ld[3][i >> 1], ld[2][i >> 1], sel);
    o.z = __builtin_amdgcn_perm(ld[5][i >> 1], ld[4][i >> 1], sel);
    o.w = __builtin_amdgcn_perm(ld[7][i >> 1], ld[6][i >> 1], sel);
    return o;
}
__device__ __forceinline__ float sum8(u32x4 o) {
    return ((bflo(o.x) + bfhi(o.x)) + (bflo(o.y) + bfhi(o.y))) + ((bflo(o.z) + bfhi(o.z)) + (bflo(o.w) + bfhi(o.w)));
}

constexpr int K0_QKV = 128, K0_X = 64, K0_WIN = 144, K0_WO = 90, K0_INIT = 54, K0_GRID = K0_QKV + K0_X + K0_WIN + K0_WO + K0_INIT;
__device__ __forceinline__ void k0_body(const u16* __restrict__ x, const u16* __restrict__ wqkv, const u16* __restrict__ wo,
                                        const u16* __restrict__ win, const u16* __restrict__ wout, char* __restrict__ ws) {
    __shared__ __attribute__((aligned(16))) char img[36864 + 576 * 8];
    float (*red)[64] = (float (*)[64])(img + 36864);
    const int tid = threadIdx.x, w = __builtin_amdgcn_readfirstlane(tid >> 6), lane = tid & 63, c = lane & 15, g = lane >> 4;
    int b = blockIdx.x;
    if (b < K0_QKV) {
        const int hf = b & 1, fp = (b >> 1) & 3, p = b >> 3, kt = 9 * hf + w;
        const int fi = 2 * fp + (c >> 3), cc = 2 * (c & 7);
        int col; bool ok = true;
        if (fi == 4) { ok = cc < 12; const int tt = (cc >> 2) == 0 ? 1 : ((cc >> 2) == 1 ? 2 : 0); col = 576 * tt + 18 * (2 * p + ((cc >> 1) & 1)) + 16; }
        else if (fi < 4) col = 576 * (1 + (fi >> 1)) + 18 * (2 * p + (fi & 1)) + cc;
        else col = 18 * (2 * p + ((fi - 5) & 1)) + cc;
        u32 ld[8];
#pragma unroll
        for (int j = 0; j < 8; ++j) ld[j] = *(const u32*)(wqkv + (size_t)(32 * kt + 8 * g + j) * 1728 + col);
        SB();
        const u32 msk = ok ? 0xffffffffu : 0u;
#pragma unroll
        for (int j = 0; j < 8; ++j) ld[j] &= msk;
#pragma unroll
        for (int i = 0; i < 2; ++i) {
            const u32 sel = i ? 0x07060302u : 0x05040100u;
            u32x4 o;
            o.x = __builtin_amdgcn_perm(ld[1], ld[0], sel); o.y = __builtin_amdgcn_perm(ld[3], ld[2], sel);
            o.z = __builtin_amdgcn_perm(ld[5], ld[4], sel); o.w = __builtin_amdgcn_perm(ld[7], ld[6], sel);
            *(u32x4*)(img + w * 2048 + (c >> 3) * 1024 + (cc + i + 16 * g) * 16) = o;
            float cs = sum8(o);
            cs += __shfl_xor(cs, 16); cs += __shfl_xor(cs, 32);
            if (g == 0) red[w][2 * c + i] = cs;
        }
        FENCE();
#pragma unroll
        for (int t = 0; t < 2; ++t) {
            const int f2 = 2 * fp + t;
            const u32 tile = f2 < 5 ? (kt * 5 + f2) * 1024 : 92160 + (kt * 2 + f2 - 5) * 1024;
            if (f2 < 7) *(u32x4*)(ws + WS_WQ + p * 129024 + tile + lane * 16) = *(const u32x4*)(img + w * 2048 + t * 1024 + lane * 16);
        }
        __syncthreads();
        if (tid < 32) {
            float s = 0.f;
#pragma unroll
            for (int k = 0; k < 9; ++k) s += red[k][tid];
            ((float*)(ws + WS_CSQ))[(hf * 16 + p) * 128 + fp * 32 + tid] = s;
        }
        return;
    }
    b -= K0_QKV;
    if (b < K0_X) {
        const int rl = tid / 18, t18 = tid - 18 * rl;
        u32x4 v[4];
#pragma unroll
        for (int i = 0; i < 4; ++i) v[i] = *(const u32x4*)(x + (size_t)(32 * b + rl) * 576 + (18 * i + t18) * 8);
        float s1 = 0.f, s2 = 0.f;
#pragma unroll
        for (int i = 0; i < 4; ++i) {
            const int ch = 18 * i + t18;
            *(u32x4*)(img + ((rl >> 4) * 18 + (ch >> 2)) * 1024 + ((rl & 15) + 16 * (ch & 3)) * 16) = v[i];
#pragma unroll
            for (int k = 0; k < 4; ++k) { const float a = bflo(v[i][k]), bb = bfhi(v[i][k]); s1 += a + bb; s2 += a * a + bb * bb; }
        }
        f32x2 o = {s1, s2};
        ((f32x2*)(img + 36864))[tid] = o;
        __syncthreads();
#pragma unroll
        for (int t = 0; t < 4; ++t) *(u32x4*)(ws + WS_X + b * 36864 + (w * 4 + t) * 1024 + lane * 16) = *(const u32x4*)(img + (w * 4 + t) * 1024 + lane * 16);
        if (tid < 256) {
            const int r = tid & 31, q = tid >> 5;
            const f32x2* pp = (const f32x2*)(img + 36864) + r * 18;
            o = pp[2 * q] + pp[2 * q + 1];
            if (q < 2) o += pp[16 + q];
            *(f32x2*)(ws + WS_XS + (q * 2048 + 32 * b + r) * 8) = o;
        }
        return;
    }
    b -= K0_X;
    if (b < K0_WIN) {
        const int hf = b & 1, cb = b >> 1, kt = 9 * hf + w;
        u32x2 ld[8];
#pragma unroll
        for (int j = 0; j < 8; ++j) ld[j] = *(const u32x2*)(win + (size_t)(32 * kt + 8 * g + j) * 4608 + 64 * cb + 4 * c);
#pragma unroll
        for (int i = 0; i < 4; ++i) {
            const u32x4 o = tcol(ld, i);
            *(u32x4*)(img + w * 4096 + (c >> 2) * 1024 + (4 * (c & 3) + i + 16 * g) * 16) = o;
            float cs = sum8(o);
            cs += __shfl_xor(cs, 16); cs += __shfl_xor(cs, 32);
            if (g == 0) red[w][4 * c + i] = cs;
        }
        FENCE();
#pragma unroll
        for (int t = 0; t < 4; ++t) *(u32x4*)(ws + WS_WIN + ((4 * cb + t) * 18 + kt) * 1024 + lane * 16) = *(const u32x4*)(img + w * 4096 + t * 1024 + lane * 16);
        __syncthreads();
        if (tid < 64) {
            float s = 0.f;
#pragma unroll
            for (int k = 0; k < 9; ++k) s += red[k][tid];
            ((float*)(ws + WS_CSIN))[hf * 4608 + 64 * cb + tid] = s;
        }
        return;
    }
    b -= K0_WIN;
    if (b < K0_WO) {
        const bool iso = b < 18;
        const int sl = iso ? b : b - 18, G = sl / 9, t = sl - 9 * G;
        const u16* src = iso ? wo : wout;
        const int col = 64 * w + 4 * c, nt = col / 72, cl = col - 72 * nt;
        const int r0 = iso ? (sl < 16 ? 36 * sl : 288 * (sl - 16) + 16) : (t < 8 ? 288 * G + 144 * (t >> 2) + 32 * (t & 3) : 288 * G + 128);
        const int gs = (iso && sl >= 16) ? 72 : 4, s4 = iso ? (sl < 16 ? 18 : 36) : (t < 8 ? 16 : 144), s2 = (iso && sl >= 16) ? 18 : 2;
        u32x2 ld[8];
#pragma unroll
        for (int j = 0; j < 8; ++j) ld[j] = *(const u32x2*)(src + (size_t)(r0 + gs * g + s4 * (j >> 2) + s2 * ((j >> 1) & 1) + (j & 1)) * 576 + col);
        const int n = cl < 64 ? 2 * (cl >> 5) + ((cl >> 2) & 1) : 4, ii = cl < 64 ? 4 * ((cl >> 3) & 3) : cl - 64;
#pragma unroll
        for (int i = 0; i < 4; ++i) {
            const u32 off = n < 4 ? n * 1024 + (ii + i + 16 * g) * 16 : 4096 + g * 128 + (ii + i) * 16;
            *(u32x4*)(img + nt * 4608 + off) = tcol(ld, i);
        }
        __syncthreads();
#pragma unroll
        for (int k = 0; k < 4; ++k) {
            const u32 i = (w * 4 + k) * 1024 + lane * 16, n2 = i / 4608, off = i - 4608 * n2;
            *(u32x4*)(ws + (iso ? WS_WO + (n2 * 18 + sl) * 4608 : WS_WOUT + (n2 * 72 + sl) * 4608) + off) = *(const u32x4*)(img + i);
        }
        return;
    }
    b -= K0_WO;
    {
        const auto rs = __builtin_amdgcn_make_buffer_rsrc(ws, 0, WS_END, 0x00020000);
        const u32 n16 = (WS_END - WS_KVP) / 16, nz = (WS_CNT - WS_KVP) / 16;
        for (u32 i = b * 576 + tid; i < n16; i += K0_INIT * 576) {
            const u32 v = i < nz ? SENT : 0u;
            u32x4 o = {v, v, v, v};
            BST128(o, rs, i * 16, WS_KVP, 16);
        }
    }
}

__global__ __launch_bounds__(576) void k0_prep(const u16* __restrict__ x, const u16* __restrict__ wqkv, const u16* __restrict__ wo,
                                               const u16* __restrict__ win, const u16* __restrict__ wout, char* __restrict__ ws) {
    STAMP_DECL;
    STAMP(8, 0);
    k0_body(x, wqkv, wo, win, wout, ws);
    STAMP(8, 1);
    STAMP_OUT(11);
}

__device__ __forceinline__ void x_stats(const char* ws, int ver, int row, float& mean, float& var) {
    const f32x2* p = (const f32x2*)(ws + WS_XS + ver * XSB) + row;
    f32x2 v[8];
#pragma unroll
    for (int k = 0; k < 8; ++k) v[k] = p[k * 2048];
    const f32x2 sm = ((v[0] + v[1]) + (v[2] + v[3])) + ((v[4] + v[5]) + (v[6] + v[7]));
    const float s1 = sm[0], s2 = sm[1];
    mean = s1 * (1.f / 576.f);
    var = fmaxf(s2 * (1.f / 576.f) - mean * mean, 0.f);
}

constexpr u32 K1_PW = 129024, K1_OP = K1_PW + 4 * 176 * 16, K1_ST = K1_OP + 4096, K1_FL = K1_ST + 1024, K1_LDS = K1_FL + 64;

__global__ __launch_bounds__(512, 2) void k1_attn(char* __restrict__ ws, int layer) {
    extern __shared__ __attribute__((aligned(16))) char shm_[];
    const u32 lds = (u32)(size_t)(LAS char*)shm_;
    const int tid = threadIdx.x, wid = __builtin_amdgcn_readfirstlane(tid >> 6), lane = tid & 63, c = lane & 15, g = lane >> 4;
    const int xcd = blockIdx.x & 7, bi = blockIdx.x >> 3;
    const int pm = MAP1 ? (xcd & 3) * 4 + (bi & 3) : (xcd & 1) * 8 + (bi & 7), p = MAP1 ? (xcd >> 2) * 8 + (bi >> 2) : (xcd >> 1) * 4 + (bi >> 3);
    const u32 FL = lds + K1_FL;
    STAMP_DECL;
    if (wid == 4) {
        const char* src = ws + WS_WQ + p * 129024;
        const u32 vo = lane * 16;
#define K1_U10(u) do { DMA4(vo, vo, vo, vo, src + (u) * 10240, lds + (u) * 10240); DMA4(vo, vo, vo, vo, src + (u) * 10240 + 4096, lds + (u) * 10240 + 4096); \
                       DMA2(vo, vo, src + (u) * 10240 + 8192, lds + (u) * 10240 + 8192); } while (0)
#define K1_U12(u) do { DMA4(vo, vo, vo, vo, src + 92160 + (u) * 12288, lds + 92160 + (u) * 12288); DMA4(vo, vo, vo, vo, src + 92160 + (u) * 12288 + 4096, lds + 92160 + (u) * 12288 + 4096); \
                       DMA4(vo, vo, vo, vo, src + 92160 + (u) * 12288 + 8192, lds + 92160 + (u) * 12288 + 8192); } while (0)
        K1_U10(0);
        BAR();
        K1_U10(1); K1_U10(2);
#pragma unroll
        for (int u = 3; u < 9; ++u) { WAIT_V(20); stsw(FL, u - 2); FENCE(); K1_U10(u); }
        WAIT_V(20); stsw(FL, 7); FENCE(); K1_U12(0);
        WAIT_V(22); stsw(FL, 8); FENCE(); K1_U12(1);
        WAIT_V(24); stsw(FL, 9); FENCE(); K1_U12(2);
        WAIT_V(24); stsw(FL, 10); FENCE();
        WAIT_V(12); stsw(FL, 11); FENCE();
        WAIT_V(0); stsw(FL, 12);
        return;
    }
    if (wid > 4) {
        const int ht = tid - 320;
        {
            u32x4 z = {0u, 0u, 0u, 0u};
            *(LAS u32x4*)(size_t)(lds + K1_OP + ht * 16) = z;
            if (ht < 64) *(LAS u32x4*)(size_t)(lds + K1_OP + 3072 + ht * 16) = z;
            if (ht >= 128 && ht < 144) stsw(FL + (ht - 128) * 4, 0u);
        }
        WAIT_L0();
        BAR();
        if (wid != 7) {
            float mean, var;
            x_stats(ws, 2 * layer, 128 * pm + ht, mean, var);
            const float r = __builtin_amdgcn_rsqf(var + 1e-5f);
            f32x2 o = {r, -mean * r};
            *(LAS f32x2*)(size_t)(lds + K1_ST + ht * 8) = o;
            WAIT_L0();
            stsw(FL + 4 + (wid - 5) * 4, 1u);
        }
        __builtin_amdgcn_s_setprio(3);
        lds_wait_sleep(FL + 16 + (lane & 3) * 4, 1u);
        STAMP(layer * 4 + 0, 7);
        const int it = ht < 173 ? ht : 172;
        f32x4 v = *(LAS f32x4*)(size_t)(lds + K1_PW + it * 16);
#pragma unroll
        for (int k = 1; k < 4; ++k) v = sadd4(v, *(LAS f32x4*)(size_t)(lds + K1_PW + k * 2816 + it * 16));
        const auto rs = __builtin_amdgcn_make_buffer_rsrc(ws, 0, WS_END, 0x00020000);
        const u32 gbase = WS_KVP + (layer * 16 + p) * 16 * 2816;
        {
            u32x4 o = __builtin_bit_cast(u32x4, v);
#pragma unroll
            for (int k = 0; k < 4; ++k) o[k] = o[k] == SENT ? 0x7fc00000u : o[k];
            if (ht < 173) BST128(o, rs, it * 16, gbase + pm * 2816, 16);
        }
        u32x4 t[16];
        STAMP(layer * 4 + 0, 8); STAMP(layer * 4 + 0, 9);
        __builtin_amdgcn_s_sleep(XDELAY1);
        u32 spins = 0, miss = 0xffffu;
        for (; spins < SPIN_CAP; ++spins) {
            FENCE();
#pragma unroll
            for (int k = 0; k < 16; ++k) if (miss >> k & 1) t[k] = __builtin_amdgcn_raw_buffer_load_b128(rs, it * 16, gbase + k * 2816, 16);
            u32 nm = 0;
#pragma unroll
            for (int k = 0; k < 16; ++k) if (miss >> k & 1) { const u32 mx = max(max(t[k][0], t[k][1]), max(t[k][2], t[k][3])); nm |= ANY(mx == SENT) ? 1u << k : 0u; }
            miss = nm;
            if (spins == 0) STAMP(layer * 4 + 0, 9);
            if (!miss) break;
        }
        STAMP(layer * 4 + 0, 10); STAMPV(layer * 4 + 0, 15, spins);
        v = __builtin_bit_cast(f32x4, t[0]);
#pragma unroll
        for (int k = 1; k < 16; ++k) v = sadd4(v, __builtin_bit_cast(f32x4, t[k]));
        const u32 lo = pk2(v[0], v[1]), hi = pk2(v[2], v[3]);
        const u32x2 lh = {lo, hi};
        const u32 OP = lds + K1_OP;
        if (ht < 128) *(LAS u32x2*)(size_t)(OP + ht * 16) = lh;
        else if (ht < 144) { stsw(OP + (ht - 128 + 32) * 16 + 8, lo); stsw(OP + 1024 + (ht - 128 + 32) * 16 + 12, hi); }
        else if (ht < 168) {
            const int q = ht - 144, hb = q >= 12, qq = q - 12 * hb, tt = qq >> 2, gg = qq & 3;
            const u32 fb = OP + 2048 + hb * 1024 + gg * 256;
            if (tt < 2) *(LAS u32x2*)(size_t)(fb + (4 * hb + tt) * 16) = lh;
            else {
#pragma unroll
                for (int k = 0; k < 4; ++k) *(LAS u32x2*)(size_t)(fb + (4 * k + 2 + hb) * 16) = lh;
            }
        } else if (ht < 173) {
            const int q = ht - 168;
            if (q < 2) stsw(OP + 2048 + (32 + q) * 16 + 8, lo);
            else if (q < 4) stsw(OP + 3072 + (32 + 2 + q) * 16 + 12, hi);
            else {
#pragma unroll
                for (int k = 0; k < 4; ++k) { stsw(OP + 2048 + (32 + 4 * k + 2) * 16 + 8, lo); stsw(OP + 3072 + (32 + 4 * k + 3) * 16 + 12, hi); }
            }
        }
        WAIT_L0();
        stsw(FL + 32 + (wid - 5) * 4, 1u);
        if (wid == 5) STAMP_OUT(layer * 4 + 0);
        return;
    }
    STAMP(layer * 4 + 0, 0);
    const float* csq = (const float*)(ws + WS_CSQ) + p * 128;
    float cs1[5], cs1b[5];
    const auto rs = __builtin_amdgcn_make_buffer_rsrc(ws, 0, WS_END, 0x00020000);
    const u32 xb = WS_X + 2 * layer * XB + (8 * pm + 2 * wid) * 18432, vo = lane * 16;
    bf16x8 xa[2][18];
#define K1_LX(m, kt) xa[m][kt] = __builtin_bit_cast(bf16x8, __builtin_amdgcn_raw_buffer_load_b128(rs, vo, xb + ((m) * 18 + (kt)) * 1024, 0))
#pragma unroll
    for (int kt = 0; kt < 6; ++kt) { K1_LX(0, kt); SB(); K1_LX(1, kt); SB(); }
    f32x4 acc[2][5];
#pragma unroll
    for (int f = 0; f < 5; ++f) { acc[0][f] = acc[1][f] = (f32x4){0.f, 0.f, 0.f, 0.f}; PINV(acc[0][f]); PINV(acc[1][f]); }
    u32 wb0 = lds + lane * 16, wb1 = wb0 + 61440, wb2 = wb0 + 92160;
    PINV(wb0); PINV(wb1); PINV(wb2);
#define K1_W1(kt, f) ((kt) < 12 ? LDSR(wb0, ((kt) * 5 + (f)) * 1024) : LDSR(wb1, (((kt) - 12) * 5 + (f)) * 1024))
    BAR();
    lds_wait(FL, 1u);
    STAMP(layer * 4 + 0, 1);
    bf16x8 wf[2][5];
#pragma unroll
    for (int f = 0; f < 5; ++f) { wf[0][f] = K1_W1(0, f); SB(); }
#pragma unroll
    for (int f = 0; f < 5; ++f) { wf[1][f] = K1_W1(1, f); SB(); }
    u32 fl = 0;
#pragma unroll
    for (int kt = 0; kt < 18; ++kt) {
        if ((kt & 1) == 0 && kt + 2 < 18) { if (__builtin_expect(ANY(fl < (u32)(kt / 2 + 2)), 0)) lds_wait(FL, kt / 2 + 2); FENCE(); }
#pragma unroll
        for (int f = 0; f < 5; ++f) {
            MFMA(acc[0][f], xa[0][kt], wf[kt & 1][f]);
            if (f == 1 && kt + 6 < 18) K1_LX(0, kt + 6);
            if (f == 3 && kt + 6 < 18) K1_LX(1, kt + 6);
            if (kt == 16) cs1[f] = csq[f * 16 + c];
            if (kt == 17) cs1b[f] = csq[2048 + f * 16 + c];
            if (f == 0 && (kt & 1) && kt < 16) { FENCE(); fl = ldsw(FL); FENCE(); }
            SB();
            MFMA(acc[1][f], xa[1][kt], wf[kt & 1][f]);
            if (kt + 2 < 18) wf[kt & 1][f] = K1_W1(kt + 2, f);
            SB();
        }
    }
    asm volatile("s_nop 15" ::: "memory");
    STAMP(layer * 4 + 0, 2);
    lds_wait(FL + 4 + (lane & 1) * 4, 1u);
    {
        u32 op[5][4], rk[4];
#pragma unroll
        for (int f = 0; f < 5; ++f) cs1[f] += cs1b[f];
#pragma unroll
        for (int m = 0; m < 2; ++m) {
            const f32x4 sa = *(LAS f32x4*)(size_t)(lds + K1_ST + (32 * wid + 16 * m + 4 * g) * 8);
            const f32x4 sb = *(LAS f32x4*)(size_t)(lds + K1_ST + (32 * wid + 16 * m + 4 * g) * 8 + 16);
            const float rr[4] = {sa[0], sa[2], sb[0], sb[2]}, mm[4] = {sa[1], sa[3], sb[1], sb[3]};
#pragma unroll
            for (int f = 0; f < 5; ++f) {
                float v[4];
#pragma unroll
                for (int r = 0; r < 4; ++r) v[r] = rr[r] * acc[m][f][r] + mm[r] * cs1[f];
                if (f < 2) {
#pragma unroll
                    for (int r = 0; r < 4; ++r) v[r] = phi(v[r]);
                }
                if (f == 4) {
                    rk[2 * m] = pk2(phi(v[0]), phi(v[1])); rk[2 * m + 1] = pk2(phi(v[2]), phi(v[3]));
#pragma unroll
                    for (int r = 0; r < 4; ++r) v[r] = c >= 12 ? 1.f : v[r];
                }
                op[f][2 * m] = pk2(v[0], v[1]); op[f][2 * m + 1] = pk2(v[2], v[3]);
            }
        }
        const bf16x8 KAa = mk8(op[0][0], op[0][1], op[0][2], op[0][3]), KAb = mk8(op[1][0], op[1][1], op[1][2], op[1][3]);
        const bf16x8 VBa = mk8(op[2][0], op[2][1], op[2][2], op[2][3]), VBb = mk8(op[3][0], op[3][1], op[3][2], op[3][3]);
        const bf16x8 RV = mk8(op[4][0], op[4][1], op[4][2], op[4][3]), RK = mk8(rk[0], rk[1], rk[2], rk[3]);
        const f32x4 z4 = {0.f, 0.f, 0.f, 0.f};
        const f32x4 T1a = MFMAB(KAa, VBa, z4), T1b = MFMAB(KAb, VBb, z4), T2a = MFMAB(KAa, RV, z4), T2b = MFMAB(KAb, RV, z4);
        const f32x4 T3a = MFMAB(RK, VBa, z4), T3b = MFMAB(RK, VBb, z4), T4 = MFMAB(RK, RV, z4);
        const u32 pw = lds + K1_PW + wid * 2816;
        *(LAS f32x4*)(size_t)(pw + lane * 16) = T1a;
        *(LAS f32x4*)(size_t)(pw + 1024 + lane * 16) = T1b;
        if (g == 0) { f32x4 u = {T3a[0], T3a[1], T3b[2], T3b[3]}; *(LAS f32x4*)(size_t)(pw + (128 + c) * 16) = u; }
        if (c == 4 || c == 5 || c == 12) *(LAS f32x4*)(size_t)(pw + (144 + (c == 12 ? 2 : c - 4) * 4 + g) * 16) = T2a;
        if (c == 6 || c == 7 || c == 12) *(LAS f32x4*)(size_t)(pw + (156 + (c == 12 ? 2 : c - 6) * 4 + g) * 16) = T2b;
        if (g == 0 && ((c >= 4 && c < 8) || c == 12)) *(LAS f32x4*)(size_t)(pw + (168 + (c == 12 ? 4 : c - 4)) * 16) = T4;
        WAIT_L0();
        stsw(FL + 16 + wid * 4, 1u);
    }
    STAMP(layer * 4 + 0, 3);
    f32x4 cs2[3], cs2b[3];
    cs2[0] = *(const f32x4*)(csq + 5 * 16 + 4 * g); cs2[1] = *(const f32x4*)(csq + 6 * 16 + 4 * g); cs2[2] = *(const f32x4*)(csq + 4 * 16 + 4 * g);
    cs2b[0] = *(const f32x4*)(csq + 2048 + 5 * 16 + 4 * g); cs2b[1] = *(const f32x4*)(csq + 2048 + 6 * 16 + 4 * g); cs2b[2] = *(const f32x4*)(csq + 2048 + 4 * 16 + 4 * g);
    SB();
    f32x4 qa[2][3];
#pragma unroll
    for (int f = 0; f < 3; ++f) { qa[0][f] = qa[1][f] = (f32x4){0.f, 0.f, 0.f, 0.f}; PINV(qa[0][f]); PINV(qa[1][f]); }
#define K1_W2(kt, f) ((f) < 2 ? LDSR(wb2, ((kt) * 2 + (f)) * 1024) : K1_W1(kt, 4))
    lds_wait(FL, 10u);
    bf16x8 wq[3][3];
#pragma unroll
    for (int k = 0; k < 3; ++k)
#pragma unroll
        for (int f = 0; f < 3; ++f) { wq[k][f] = K1_W2(k, f); SB(); }
#pragma unroll
    for (int kt = 0; kt < 18; ++kt) {
        if (kt == 3) lds_wait(FL, 11u);
        if (kt == 9) lds_wait(FL, 12u);
#pragma unroll
        for (int f = 0; f < 3; ++f) {
            MFMA(qa[0][f], wq[kt % 3][f], xa[0][kt]);
            SB();
            MFMA(qa[1][f], wq[kt % 3][f], xa[1][kt]);
            if (kt + 3 < 18) wq[kt % 3][f] = K1_W2(kt + 3, f);
            SB();
        }
    }
    asm volatile("s_nop 15" ::: "memory");
    STAMP(layer * 4 + 0, 4);
#pragma unroll
    for (int f = 0; f < 3; ++f) cs2[f] += cs2b[f];
    bf16x8 PQ[2][2];
#pragma unroll
    for (int m = 0; m < 2; ++m) {
        const f32x2 st = *(LAS f32x2*)(size_t)(lds + K1_ST + (32 * wid + 16 * m + c) * 8);
        u32 w2[3][2];
#pragma unroll
        for (int f = 0; f < 3; ++f) {
            float v[4];
#pragma unroll
            for (int r = 0; r < 4; ++r) v[r] = phi(st[0] * qa[m][f][r] + st[1] * cs2[f][r]);
            w2[f][0] = pk2(v[0], v[1]); w2[f][1] = pk2(v[2], v[3]);
        }
        if (g != 2) { w2[2][0] = 0u; w2[2][1] = 0u; }
        PQ[m][0] = mk8(w2[0][0], w2[0][1], w2[2][0], w2[2][1]);
        PQ[m][1] = mk8(w2[1][0], w2[1][1], w2[2][0], w2[2][1]);
    }
    lds_wait(FL + 32 + (lane % 3) * 4, 1u);
    STAMP(layer * 4 + 0, 5);
    {
        const u32 OP = lds + K1_OP + lane * 16;
        const bf16x8 A1a = LDSR(OP, 0), A1b = LDSR(OP, 1024), A2a = LDSR(OP, 2048), A2b = LDSR(OP, 3072);
        const f32x4 z4 = {0.f, 0.f, 0.f, 0.f};
#pragma unroll
        for (int m = 0; m < 2; ++m) {
            const f32x4 O1a = MFMAB(A1a, PQ[m][0], z4), O1b = MFMAB(A1b, PQ[m][1], z4);
            f32x4 O2 = MFMAB(A2a, PQ[m][0], z4);
            O2 = MFMAB(A2b, PQ[m][1], O2);
            const float ia = __builtin_amdgcn_rcpf(O2[2] + 1e-6f), ib = __builtin_amdgcn_rcpf(O2[3] + 1e-6f);
            u32x4 o = {pk2(O1a[0] * ia, O1a[1] * ia), pk2(O1a[2] * ia, O1a[3] * ia), pk2(O1b[0] * ib, O1b[1] * ib), pk2(O1b[2] * ib, O1b[3] * ib)};
            char* ob = ws + WS_O + layer * XB;
            *(u32x4*)(ob + ((8 * pm + 2 * wid + m) * 16 + p) * 1024 + lane * 16) = o;
            const float ir = g == 0 ? ia : ib;
            if (g < 2) *(u32*)(ob + 2097152 + (p * 2048 + 128 * pm + 32 * wid + 16 * m + c) * 8 + g * 4) = pk2(O2[0] * ir, O2[1] * ir);
        }
    }
    STAMP(layer * 4 + 0, 6);
    STAMP_OUT(layer * 4 + 0);
}

constexpr u32 K2_LDS = 96 * 1024;
template <int KIND>
__global__ __launch_bounds__(256, 2) void k2_proj(char* __restrict__ ws, u16* __restrict__ out, int layer) {
    extern __shared__ __attribute__((aligned(16))) char shm_[];
    const u32 lds = (u32)(size_t)(LAS char*)shm_;
    const int tid = threadIdx.x, wid = __builtin_amdgcn_readfirstlane(tid >> 6), lane = tid & 63, c = lane & 15, g = lane >> 4;
    const int xcd = blockIdx.x & 7, bi = blockIdx.x >> 3;
    const int mt = (xcd & 3) * 8 + (bi & 7), nt = (xcd >> 2) * 4 + (bi >> 3);
    constexpr int NSL = KIND == 0 ? 5 : 18, RD = 4;
    constexpr int SK = KIND == 0 ? 1 : 3;
    STAMP_DECL;
    STAMP(layer * 4 + SK, 0);
    const auto rs = __builtin_amdgcn_make_buffer_rsrc(ws, 0, WS_END, 0x00020000);
    const u32 ab = KIND == 0 ? WS_O + layer * XB + (4 * mt * 16 + 4 * wid) * 1024 : WS_H + layer * ZB + (4 * mt * 8 + 2 * wid) * 9216;
    constexpr int AST = KIND == 0 ? 16384 : 73728;
    const u32 wb = KIND == 0 ? WS_WO + (nt * 18 + 4 * wid) * 4608 : WS_WOUT + (nt * 72 + 18 * wid) * 4608;
    const u32 vo = lane * 16, vo8 = KIND == 0 ? g * 32768 + c * 8 : lane * 8, vo4 = 4096 + g * 128 + (lane & 7) * 16;
    bf16x8 fa[RD][4], fw[RD][5];
    f32x4 acc[4][5];
#pragma unroll
    for (int m = 0; m < 4; ++m)
#pragma unroll
        for (int n = 0; n < 5; ++n) acc[m][n] = (f32x4){0.f, 0.f, 0.f, 0.f};
    u32 abm[4];
#pragma unroll
    for (int m = 0; m < 4; ++m) abm[m] = ab + (m ^ wid) * AST;
#define K2_B128(v, sof) __builtin_bit_cast(bf16x8, __builtin_amdgcn_raw_buffer_load_b128(rs, v, sof, 0))
#define K2_LOADW(s, wo_) do { _Pragma("unroll") for (int n = 0; n < 4; ++n) fw[(s) % RD][n] = K2_B128(vo + n * 1024, wo_); \
        fw[(s) % RD][4] = K2_B128(vo4, wo_); } while (0)
#define K2_SPLIT(s, m, so_, d_) do { const u32x2 h0 = __builtin_amdgcn_raw_buffer_load_b64(rs, vo8, so_, 0);          \
        const u32x2 h1 = __builtin_amdgcn_raw_buffer_load_b64(rs, vo8, (so_) + (d_), 0);                             \
        fa[(s) % RD][m] = mk8(h0[0], h0[1], h1[0], h1[1]); } while (0)
#define K2_LOAD(s) do { K2_LOADW(s, wb + (s) * 4608);                                                                \
        _Pragma("unroll") for (int m = 0; m < 4; ++m) {                                                              \
            if (KIND != 0 && (s) % 9 == 8) K2_SPLIT(s, m, abm[m] + (s) * 1024, 512);                                 \
            else fa[(s) % RD][m] = K2_B128(vo + ((s) & 3) * 1024, abm[m] + ((s) & ~3) * 1024);                       \
        } SB(); } while (0)
#define K2_MM(s) do { _Pragma("unroll") for (int m = 0; m < 4; ++m) _Pragma("unroll") for (int n = 0; n < 5; ++n)    \
        acc[m][n] = MFMAB(fw[(s) % RD][n], fa[(s) % RD][m], acc[m][n]); SB(); } while (0)
#pragma unroll
    for (int s = 0; s < RD; ++s) K2_LOAD(s);
    u32x4 res[2]; u32x2 res2;
    const int ver = 2 * layer + (KIND != 0);
    const u32 xoff = WS_X + ver * XB + (4 * mt + wid) * 18432;
    const u32 xa0 = (9 * nt + g) * 256 + c * 16, xa2 = (9 * nt + 8) * 256 + c * 16 + (g & 1) * 8;
#define K2_RES() do { res[0] = __builtin_amdgcn_raw_buffer_load_b128(rs, xa0, xoff, 0); res[1] = __builtin_amdgcn_raw_buffer_load_b128(rs, xa0 + 1024, xoff, 0); \
        res2 = __builtin_amdgcn_raw_buffer_load_b64(rs, xa2, xoff, 0); SB(); } while (0)
    if (KIND == 0) {
        K2_MM(0);
        if (wid < 2) {
            K2_LOADW(4, WS_WO + (nt * 18 + 16 + wid) * 4608);
#pragma unroll
            for (int m = 0; m < 4; ++m) K2_SPLIT(4, m, WS_O + layer * XB + 2097152 + (wid * 8 * 2048 + 64 * mt + 16 * (m ^ wid)) * 8, 16384);
            SB();
        }
        K2_RES();
        K2_MM(1); K2_MM(2); K2_MM(3);
        if (wid < 2) K2_MM(4);
    } else {
#pragma unroll
        for (int s = 0; s < NSL; ++s) {
            K2_MM(s);
            if (s + RD < NSL) K2_LOAD(s + RD);
            if (s + RD == NSL) K2_RES();
        }
    }
    STAMP(layer * 4 + SK, 1);
#pragma unroll
    for (int m = 1; m < 4; ++m)
#pragma unroll
        for (int n = 0; n < 5; ++n) *(LAS f32x4*)(size_t)(lds + (((m ^ wid) * 3 + m - 1) * 5 + n) * 1024 + vo) = acc[m][n];
    __syncthreads();
#pragma unroll
    for (int n = 0; n < 5; ++n)
        acc[0][n] = (acc[0][n] + *(LAS f32x4*)(size_t)(lds + (wid * 15 + n) * 1024 + vo)) +
                    (*(LAS f32x4*)(size_t)(lds + (wid * 15 + 5 + n) * 1024 + vo) + *(LAS f32x4*)(size_t)(lds + (wid * 15 + 10 + n) * 1024 + vo));
    u32x4 o[2]; u32x2 o2;
#pragma unroll
    for (int i = 0; i < 2; ++i) {
        const f32x4 a = acc[0][2 * i], b = acc[0][2 * i + 1];
        o[i] = (u32x4){pk2(a[0] + bflo(res[i][0]), a[1] + bfhi(res[i][0])), pk2(a[2] + bflo(res[i][1]), a[3] + bfhi(res[i][1])),
                       pk2(b[0] + bflo(res[i][2]), b[1] + bfhi(res[i][2])), pk2(b[2] + bflo(res[i][3]), b[3] + bfhi(res[i][3]))};
    }
    o2 = (u32x2){pk2(acc[0][4][0] + bflo(res2[0]), acc[0][4][1] + bfhi(res2[0])), pk2(acc[0][4][2] + bflo(res2[1]), acc[0][4][3] + bfhi(res2[1]))};
    if (KIND == 2) {
        u16* op = out + (size_t)(64 * mt + 16 * wid + c) * 576 + 72 * nt;
        *(u32x4*)(op + 8 * g) = o[0];
        *(u32x4*)(op + 32 + 8 * g) = o[1];
        if (g < 2) *(u32x2*)(op + 64 + 4 * g) = o2;
    } else {
        char* xp = ws + xoff + XB;
        *(u32x4*)(xp + xa0) = o[0];
        *(u32x4*)(xp + xa0 + 1024) = o[1];
        if (g < 2) *(u32x2*)(xp + xa2) = o2; else o2 = (u32x2){0u, 0u};
        float s1 = 0.f, s2 = 0.f;
#pragma unroll
        for (int k = 0; k < 10; ++k) { const u32 wv = k < 8 ? o[k >> 2][k & 3] : o2[k & 1]; const float u = bflo(wv), v = bfhi(wv); s1 += u + v; s2 += u * u + v * v; }
        auto r1 = __builtin_amdgcn_permlane32_swap(F2U(s1), F2U(s2), false, false);
        const float t = U2F(r1[0]) + U2F(r1[1]);
        auto r2 = __builtin_amdgcn_permlane16_swap(F2U(t), F2U(t), false, false);
        const float tt = U2F(r2[0]) + U2F(r2[1]);
        if ((g & 1) == 0) *(float*)(ws + WS_XS + (ver + 1) * XSB + (nt * 2048 + 64 * mt + 16 * wid + c) * 8 + (g >> 1) * 4) = tt;
    }
    STAMP(layer * 4 + SK, 2);
    STAMP_OUT(layer * 4 + SK);
}

constexpr u32 STAGE = 18432, NS = 6;
constexpr u32 K3_PK = NS * STAGE, K3_PP = 304, K3_WS = K3_PK + 128 * K3_PP, K3_S1 = K3_WS + 4096, K3_S2 = K3_S1 + 2048, K3_FL = K3_S2 + 1024, K3_LDS = K3_FL + 128;

__global__ __launch_bounds__(512, 2) void k34_mlp(char* __restrict__ ws, const u16* __restrict__ wsp, const u16* __restrict__ bsp, int layer) {
    extern __shared__ __attribute__((aligned(16))) char shm_[];
    const u32 lds = (u32)(size_t)(LAS char*)shm_;
    const int tid = threadIdx.x, wid = __builtin_amdgcn_readfirstlane(tid >> 6), lane = tid & 63, c = lane & 15, g = lane >> 4;
    const int xcd = blockIdx.x & 7, bi = blockIdx.x >> 3;
    const int pm = MAP3 ? (xcd & 3) * 4 + (bi & 3) : (xcd & 1) * 8 + (bi & 7), pn = MAP3 ? (xcd >> 2) * 8 + (bi >> 2) : (xcd >> 1) * 4 + (bi >> 3);
    const u32 FL = lds + K3_FL;
    STAMP_DECL;
    const auto rs = __builtin_amdgcn_make_buffer_rsrc(ws, 0, WS_END, 0x00020000);
    if (wid == 4) {
        u32 ao[16];
        const u32 vo = lane * 16;
#pragma unroll
        for (int q = 0; q < 16; ++q) { ao[q] = vo + (q >> 1) * 18432 - (q & 2) * 1024 + 4096; PINV(ao[q]); }
        const char* ap = ws + WS_X + (2 * layer + 1) * XB + 8 * pm * 18432 - 4096;
        const char* xp = ws + WS_WIN + (144 + 9 * pn + 8) * 18432;
        u32 slot = 0;
#define K3_ISSUE() do { const u32 dst = lds + slot * STAGE;                                                          \
            DMA4(ao[0], ao[1], ao[2], ao[3], ap, dst); DMA4(ao[4], ao[5], ao[6], ao[7], ap, dst + 4096);             \
            DMA4(ao[8], ao[9], ao[10], ao[11], ap, dst + 8192); DMA4(ao[12], ao[13], ao[14], ao[15], ap, dst + 12288); } while (0)
        for (int s = 0; s < 21; ++s) {
            if (s >= 3) {
                if (s <= 18) WAIT_V(36); else if (s == 19) WAIT_V(18); else WAIT_V(0);
                stsw(FL, s - 2);
                FENCE();
            }
            if (s == 1) BAR();
            if (s < 18) {
                if (s >= (int)NS) lds_wait_sleep(FL + 16 + (lane & 3) * 4, s >= 15 ? LGATE : s - NS + 1);
                if (s == 9) { ap -= 9 * 2048; xp = ws + WS_WIN + (9 * pn + 8) * 18432; }
                K3_ISSUE();
                DMA2(vo, vo, xp, lds + slot * STAGE + 16384);
                xp += 2048; ap += 2048;
                slot = slot + 1 == NS ? 0 : slot + 1;
            }
        }
#pragma unroll
        for (int q = 0; q < 16; ++q) { ao[q] = (q * 8 + (lane >> 3)) * 4096 + (((lane & 7) ^ ((q & 1) * 4 + g)) * 16) - (q & 3) * 1024 + 4096; PINV(ao[q]); }
        ap = (const char*)wsp + (size_t)pm * 128 * 4096 - 4096;
        lds_wait_sleep(FL + 16 + (lane & 3) * 4, LGATE2);
        for (int s = 18; s < 53; ++s) {
            if (s >= 21) {
                if (s <= 50) WAIT_V(32); else if (s == 51) WAIT_V(16); else WAIT_V(0);
                stsw(FL, s - 2);
                FENCE();
            }
            if (s < 50) {
                lds_wait_sleep(FL + 16 + (lane & 3) * 4, s - NS + 1);
                K3_ISSUE();
                ap += s == 33 ? 128 - 15 * 256 : 256;
                slot = slot + 1 == NS ? 0 : slot + 1;
            }
        }
        return;
    }
    if (wid > 4) {
        const int ht = tid - 320;
        if (ht >= 128 && ht < 160) stsw(FL + (ht - 128) * 4, ht == 129 ? 18u : 0u);
        WAIT_L0();
        BAR();
        if (wid != 7) {
            float mean, var;
            x_stats(ws, 2 * layer + 1, 128 * pm + ht, mean, var);
            const float r = __builtin_amdgcn_rsqf(var + 1e-5f);
            f32x4 o = {-mean, -2.302208198f * r, -0.1029432397f * r * r * r, (var + 1e-5f) * r};
            *(LAS f32x4*)(size_t)(lds + K3_S1 + ht * 16) = o;
            WAIT_L0();
            stsw(FL + 32 + (wid - 5) * 4, 1u);
        }
        u32 vo = lane * 16;
        PINV(vo);
        GAS u32* flg = (GAS u32*)(ws + WS_CNT + (layer * 16 + pn) * 4096);
        HST_DECL;
        if (wid != 5) {
            const int h = wid - 6;
            u32 ta = lds + K3_PK + (32 * h + 8 * g + (c >> 2)) * K3_PP + 16 * (c & 3), tx = ta + 256 - 8 * (c & 3), s2r = lds + K3_S2 + 128 * h + 32 * g;
            PINV(ta); PINV(tx); PINV(s2r);
            const u32 zob = WS_Z + layer * ZB + (9 * pn * 64 + 2 * pm + h) * 1024;
            lds_wait_sleep(FL + 48 + (lane & 3) * 4, 1u);
            HST(0);
            if (pm < HPRIO) __builtin_amdgcn_s_setprio(3);
            float zf[18][8];
#define K3_TR(fi, A, O) do { u32x2 t0_, t1_, t2_, t3_;                                                               \
                asm volatile("ds_read_b64_tr_b16 %0, %4 offset:%5\n\tds_read_b64_tr_b16 %1, %4 offset:%6\n\tds_read_b64_tr_b16 %2, %4 offset:%7\n\t" \
                             "ds_read_b64_tr_b16 %3, %4 offset:%8\n\ts_waitcnt lgkmcnt(0)" : "=&v"(t0_), "=&v"(t1_), "=&v"(t2_), "=&v"(t3_) \
                             : "v"(A), "i"(O), "i"((O) + 4 * K3_PP), "i"((O) + 64 * K3_PP), "i"((O) + 68 * K3_PP) : "memory"); \
                const u32 w_[8] = {t0_[0], t0_[1], t1_[0], t1_[1], t2_[0], t2_[1], t3_[0], t3_[1]};                  \
                _Pragma("unroll") for (int i_ = 0; i_ < 8; ++i_) { zf[2 * (fi) + (i_ >> 2)][2 * (i_ & 3)] = bflo(w_[i_]); zf[2 * (fi) + (i_ >> 2)][2 * (i_ & 3) + 1] = bfhi(w_[i_]); \
                    PINV(zf[2 * (fi) + (i_ >> 2)][2 * (i_ & 3)]); PINV(zf[2 * (fi) + (i_ >> 2)][2 * (i_ & 3) + 1]); } } while (0)
            K3_TR(0, ta, 0); K3_TR(1, ta, 8); K3_TR(2, ta, 64); K3_TR(3, ta, 72); K3_TR(4, ta, 128); K3_TR(5, ta, 136); K3_TR(6, ta, 192); K3_TR(7, ta, 200);
            K3_TR(8, tx, 0);
            HST(1);
            lds_wait_sleep(FL + 64, 1u);
            for (int i = 2; i < pm; ++i) __builtin_amdgcn_s_sleep(STAG);
            HST(2);
            f32x4 sv[2][4];
#pragma unroll
            for (int q = 0; q < 2; ++q)
#pragma unroll
                for (int k = 0; k < 4; ++k) sv[q][k] = *(LAS f32x4*)(size_t)(s2r + (k >> 1) * 512 + 256 * q + (k & 1) * 16);
#pragma unroll
            for (int ii = 0; ii < 18; ++ii) {
                const int q = ii / 9, i = 2 * (ii % 9) + q;
                u32x4 o;
                asm("v_fma_f32 %4, %4, %12, %20\n\tv_fma_f32 %5, %5, %13, %21\n\tv_fma_f32 %6, %6, %14, %22\n\tv_fma_f32 %7, %7, %15, %23\n\t"
                    "v_fma_f32 %8, %8, %16, %24\n\tv_fma_f32 %9, %9, %17, %25\n\tv_fma_f32 %10, %10, %18, %26\n\tv_fma_f32 %11, %11, %19, %27\n\t"
                    "v_cvt_pk_bf16_f32 %0, %4, %5\n\tv_cvt_pk_bf16_f32 %1, %6, %7\n\tv_cvt_pk_bf16_f32 %2, %8, %9\n\tv_cvt_pk_bf16_f32 %3, %10, %11"
                    : "=&v"(o[0]), "=&v"(o[1]), "=&v"(o[2]), "=&v"(o[3]), "+v"(zf[i][0]), "+v"(zf[i][1]), "+v"(zf[i][2]), "+v"(zf[i][3]), "+v"(zf[i][4]), "+v"(zf[i][5]), "+v"(zf[i][6]), "+v"(zf[i][7])
                    : "v"(sv[q][0][0]), "v"(sv[q][0][1]), "v"(sv[q][0][2]), "v"(sv[q][0][3]), "v"(sv[q][1][0]), "v"(sv[q][1][1]), "v"(sv[q][1][2]), "v"(sv[q][1][3]),
                      "v"(sv[q][2][0]), "v"(sv[q][2][1]), "v"(sv[q][2][2]), "v"(sv[q][2][3]), "v"(sv[q][3][0]), "v"(sv[q][3][1]), "v"(sv[q][3][2]), "v"(sv[q][3][3]));
                __builtin_amdgcn_raw_buffer_store_b128(o, rs, vo, zob + q * 32768 + (i >> 1) * 65536, 16);
                SB();
                if (ii % 9 == 8) {
                    if (q == 0) HST(3);
                    WAIT_V(0);
                    if (q == 0) HST(4);
                    u32 old = 0;
                    if (lane == 0) old = __hip_atomic_fetch_add((LAS u32*)(size_t)(FL + 76 + 4 * q), 1u, __ATOMIC_RELAXED, __HIP_MEMORY_SCOPE_WORKGROUP);
                    if (__builtin_amdgcn_readfirstlane(old) == 1 && lane < 16)
                        __hip_atomic_fetch_add(flg + lane * 64, 1u << (16 * q + pm), RLX_AGENT);
                    if (q == 0) { __builtin_amdgcn_s_setprio(0); __builtin_amdgcn_s_sleep(STAG2); }
                    SB();
                }
            }
            HST(5);
            if (wid == 6) HST_OUT(9 + layer);
            return;
        }
        u32 wsa = lds + K3_WS + lane * 16, s2w = lds + K3_S2 + lane * 8;
        PINV(wsa); PINV(s2w);
        const u32 zsb = WS_ZST + (layer * 16 + pm) * 16384;
        lds_wait_sleep(FL + 16 + (lane & 3) * 4, 9u);
        __builtin_amdgcn_s_sleep(POLLD);
        __builtin_amdgcn_s_setprio(3);
        lds_wait(FL + 48 + (lane & 3) * 4, 1u);
        STAMP(layer * 4 + 2, 8);
        {
            const f32x4 st = sadd4(sadd4(*(LAS f32x4*)(size_t)wsa, *(LAS f32x4*)(size_t)(wsa + 1024)), sadd4(*(LAS f32x4*)(size_t)(wsa + 2048), *(LAS f32x4*)(size_t)(wsa + 3072)));
            u32x4 o = __builtin_bit_cast(u32x4, st);
#pragma unroll
            for (int k = 0; k < 4; ++k) o[k] = o[k] == SENT ? 0x7fc00000u : o[k];
            BST128(o, rs, vo, zsb + pn * 1024, 16);
            __builtin_amdgcn_s_sleep(XDELAY3);
            u32x4 t[16];
#pragma unroll
            for (int k = 0; k < 16; ++k) t[k] = __builtin_amdgcn_raw_buffer_load_b128(rs, vo + (k & 3) * 1024, zsb + (k >> 2) * 4096, 16);
            u32 mx = 0;
#pragma unroll
            for (int k = 0; k < 16; ++k) mx = max(max(mx, max(t[k][0], t[k][1])), max(t[k][2], t[k][3]));
            u32 spins = 0;
            if (__builtin_expect(ANY(mx == SENT), 0)) {
#pragma nounroll
                for (spins = 1; spins < SPIN_CAP; ++spins) {
                    FENCE();
                    u32 miss = 0;
#pragma unroll
                    for (int k = 0; k < 16; ++k) miss |= ANY(max(max(t[k][0], t[k][1]), max(t[k][2], t[k][3])) == SENT) ? 1u << k : 0u;
                    if (!miss) break;
#pragma unroll
                    for (int k = 0; k < 16; ++k) if (miss >> k & 1) t[k] = __builtin_amdgcn_raw_buffer_load_b128(rs, vo + (k & 3) * 1024, zsb + (k >> 2) * 4096, 16);
                }
            }
            STAMPV(layer * 4 + 2, 15, spins);
            f32x4 sa[4];
#pragma unroll
            for (int k = 0; k < 4; ++k)
                sa[k] = sadd4(sadd4(__builtin_bit_cast(f32x4, t[4 * k]), __builtin_bit_cast(f32x4, t[4 * k + 1])), sadd4(__builtin_bit_cast(f32x4, t[4 * k + 2]), __builtin_bit_cast(f32x4, t[4 * k + 3])));
            const f32x4 ss = sadd4(sadd4(sa[0], sa[1]), sadd4(sa[2], sa[3]));
            f32x2 rr, mr;
#pragma unroll
            for (int k = 0; k < 2; ++k) {
                const float m2 = sfma(ss[2 * k], 1.f / 2304.f, 0.f);
                float v2 = fmaxf(sfma(-m2, m2, sfma(ss[2 * k + 1], 1.f / 2304.f, 0.f)), 0.f) + 1e-5f;
                PINV(v2);
                rr[k] = __builtin_amdgcn_rsqf(v2);
                float t2 = -m2 * rr[k];
                PINV(t2);
                mr[k] = t2;
            }
            *(LAS f32x2*)(size_t)s2w = rr;
            *(LAS f32x2*)(size_t)(s2w + 512) = mr;
            FENCE();
            stsw(FL + 64, 1u);
            FENCE();
        }
        STAMP(layer * 4 + 2, 9);
        STAMP(layer * 4 + 2, 10);
        u32 ready = 0;
#define K3_NEED(n) do { _Pragma("nounroll") for (u32 spins_ = 0; ready < (u32)(n) && spins_ < SPIN_CAP; ++spins_) {   \
            const u32 v_ = __builtin_amdgcn_readfirstlane(__hip_atomic_load(flg + pm * 64, RLX_AGENT));                                               \
            ready = __builtin_ctzll(~(u64)v_); } FENCE(); } while (0)
        __builtin_amdgcn_s_sleep(FDELAY);
        K3_NEED(3);
        stsw(FL + 84, 1u);
        FENCE();
        STAMP(layer * 4 + 2, 11);
        STAMP_OUT(layer * 4 + 2);
        __builtin_amdgcn_s_setprio(0);
        const u32 zx = WS_Z + layer * ZB + (9 * pn + 8) * 65536;
        u32x4 xr[4][2];
#pragma unroll
        for (int k = 0; k < 3; ++k) {
            xr[k][0] = __builtin_amdgcn_raw_buffer_load_b128(rs, vo, zx + k * 2048, 16);
            xr[k][1] = __builtin_amdgcn_raw_buffer_load_b128(rs, vo + 1024, zx + k * 2048, 16);
            SB();
        }
        u32 slot = 0;
        for (int s0 = 0; s0 < 32; s0 += 4) {
#pragma unroll
            for (int k = 0; k < 4; ++k) {
                const int s = s0 + k;
                lds_wait_sleep(FL + 16 + (lane & 3) * 4, 18 + s - NS + 1);
                *(LAS u32x4*)(size_t)(lds + slot * STAGE + 16384 + lane * 16) = xr[k][0];
                *(LAS u32x4*)(size_t)(lds + slot * STAGE + 17408 + lane * 16) = xr[k][1];
                FENCE();
                stsw(FL + 4, 18 + s + 1);
                FENCE();
                const u32 sn = s + 3 < 32 ? s + 3 : 31;
                K3_NEED(sn + 1);
                xr[(k + 3) & 3][0] = __builtin_amdgcn_raw_buffer_load_b128(rs, vo, zx + sn * 2048, 16);
                xr[(k + 3) & 3][1] = __builtin_amdgcn_raw_buffer_load_b128(rs, vo + 1024, zx + sn * 2048, 16);
                slot = slot + 1 == NS ? 0 : slot + 1;
            }
        }
        return;
    }
    STAMP(layer * 4 + 2, 0);
    u32 vA = lane * 16, vB = lane * 16 + 18432;
    u32 so = WS_WIN + (144 + 9 * pn + 2 * wid) * 18432;
    bf16x8 bb[6][2];
#define BLD(voff, imm, aux) __builtin_bit_cast(bf16x8, __builtin_amdgcn_raw_buffer_load_b128(rs, (voff) + (imm), so, aux))
#define BPRO(aux) do { _Pragma("unroll") for (int r = 0; r < 5; ++r) { if (r == 3) so += 3072;                       \
        bb[r][0] = BLD(vA, (r % 3) * 1024, aux); SB(); bb[r][1] = BLD(vB, (r % 3) * 1024, aux); SB(); } } while (0)
    const float* csin = (const float*)(ws + WS_CSIN) + 144 * pn + 4 * g;
    f32x4 cs[2][3], csb[2][3];
#pragma unroll
    for (int ph = 0; ph < 2; ++ph) {
        cs[ph][0] = *(const f32x4*)(csin + (1 - ph) * 2304 + 32 * wid); cs[ph][1] = *(const f32x4*)(csin + (1 - ph) * 2304 + 32 * wid + 16);
        cs[ph][2] = *(const f32x4*)(csin + (1 - ph) * 2304 + 128);
        csb[ph][0] = *(const f32x4*)(csin + 4608 + (1 - ph) * 2304 + 32 * wid); csb[ph][1] = *(const f32x4*)(csin + 4608 + (1 - ph) * 2304 + 32 * wid + 16);
        csb[ph][2] = *(const f32x4*)(csin + 4608 + (1 - ph) * 2304 + 128);
    }
    SB();
    BPRO(0);
    u32 pa[2][4];
#pragma unroll
    for (int q = 0; q < 4; ++q) { pa[0][q] = lds + lane * 16 + ((q ^ wid) * 4096); pa[1][q] = pa[0][q] + 1024; }
    u32 px = lds + 16384 + lane * 16;
    u32 myfree = FL + 16 + wid * 4, fladdr = FL + (lane & 1) * 4, cnt = 0;
    PINV(cnt); PINV(myfree); PINV(fladdr);
    int dstep = 3 * STAGE;
    f32x4 acc[8][2], accx[2];
    bf16x8 a[8], xf;
#define ALOAD() do { a[0] = LDSR(pa[0][0], 0); SB(); xf = LDSR(px, 0); SB();                                         \
        _Pragma("unroll") for (int m = 1; m < 8; ++m) { a[m] = LDSR(pa[0][m >> 1], (m & 1) * 2048); SB(); } } while (0)
#define HALF(r, RD, kn, sf, rl, imm, aux, E0, E1, E2, E3, E4, E5) do {                                               \
        WCNT(8, 5); MFMA(acc[0][0], bb[r][0], a[0]); E0; SB();                                                       \
        MFMA(acc[0][1], bb[r][1], a[0]); E1; SB();                                                                   \
        MFMA(accx[0], xf, a[0]); if (RD) a[0] = LDSR(pa[kn][0], (sf)); SB();                                         \
        MFMA(accx[1], xf, a[1]); if (RD) xf = LDSR(px, (sf) + (kn) * 1024); SB();                                    \
        MFMA(acc[1][1], bb[r][1], a[1]); bb[rl][0] = BLD(vA, imm, aux); SB();                                        \
        MFMA(acc[1][0], bb[r][0], a[1]); if (RD) a[1] = LDSR(pa[kn][0], (sf) + 2048); SB();                          \
        MFMA(acc[2][0], bb[r][0], a[2]); E2; SB();                                                                   \
        MFMA(acc[2][1], bb[r][1], a[2]); if (RD) a[2] = LDSR(pa[kn][1], (sf)); SB();                                 \
        WCNT(63, 5); MFMA(acc[3][1], bb[r][1], a[3]); SB();                                                          \
        MFMA(acc[3][0], bb[r][0], a[3]); if (RD) a[3] = LDSR(pa[kn][1], (sf) + 2048); SB();                          \
        MFMA(acc[4][0], bb[r][0], a[4]); bb[rl][1] = BLD(vB, imm, aux); SB();                                        \
        MFMA(acc[4][1], bb[r][1], a[4]); if (RD) a[4] = LDSR(pa[kn][2], (sf)); SB();                                 \
        MFMA(acc[5][1], bb[r][1], a[5]); E3; SB();                                                                   \
        MFMA(acc[5][0], bb[r][0], a[5]); if (RD) a[5] = LDSR(pa[kn][2], (sf) + 2048); SB();                          \
        MFMA(acc[6][0], bb[r][0], a[6]); E4; SB();                                                                   \
        MFMA(acc[6][1], bb[r][1], a[6]); if (RD) a[6] = LDSR(pa[kn][3], (sf)); SB();                                 \
        MFMA(acc[7][1], bb[r][1], a[7]); E5; SB();                                                                   \
        MFMA(acc[7][0], bb[r][0], a[7]); if (RD) a[7] = LDSR(pa[kn][3], (sf) + 2048); SB(); } while (0)
#define STEP(u, chk, nso, aux) do { u32 fl_; u64 msk_;                                                               \
        HALF(2 * (u), 1, 1, (u) * STAGE, (2 * (u) + 5) % 6, ((2 * (u) + 5) % 3) * 1024, aux,                         \
             { if ((u) == 2) so += 3072; }, { cnt += 1; PINV(cnt); }, { FENCE(); fl_ = ldsw(fladdr); FENCE(); },     \
             { if ((u) == 2) { pa[0][0] += dstep; pa[0][1] += dstep; PINV(pa[0][0]); PINV(pa[0][1]); } },            \
             { if ((u) == 2) { pa[0][2] += dstep; pa[0][3] += dstep; PINV(pa[0][2]); PINV(pa[0][3]); } },            \
             { asm volatile("v_cmp_le_u32 %0, %1, %2" : "=s"(msk_) : "v"(fl_), "v"(cnt)); if ((u) == 2) { px += dstep; PINV(px); } }); \
        if (__builtin_expect(msk_ != 0, 0)) { if (chk) lds_wait(fladdr, cnt + 1); } \
        FENCE();                                                                                                     \
        HALF(2 * (u) + 1, 1, 0, (((u) + 1) % 3) * STAGE, (2 * (u) + 6) % 6, ((2 * (u) + 6) % 3) * 1024, aux,         \
             { FENCE(); stsw(myfree, cnt); FENCE(); }, { if ((u) == 0) so = (nso); },                                \
             { if ((u) == 2) { pa[1][0] += dstep; pa[1][1] += dstep; PINV(pa[1][0]); PINV(pa[1][1]); } },            \
             { if ((u) == 2) { pa[1][2] += dstep; pa[1][3] += dstep; PINV(pa[1][2]); PINV(pa[1][3]); } },            \
             { }, { if ((u) == 2) dstep = -dstep; }); } while (0)
#define PINACC() do { _Pragma("unroll") for (int m = 0; m < 8; ++m) { PINV(acc[m][0]); PINV(acc[m][1]); } PINV(accx[0]); PINV(accx[1]); asm volatile("s_nop 1"); } while (0)
    BAR();
#pragma unroll
    for (int i = 0; i < 6; ++i) { cs[i / 3][i % 3] += csb[i / 3][i % 3]; PINV(cs[i / 3][i % 3]); }
    lds_wait(FL + 32 + (lane & 1) * 4, 1u);
    lds_wait(fladdr, 1u);
    ALOAD();
    STAMP(layer * 4 + 2, 1);
    u32 yp[18][2], early = 0;
    const u32 s1a = lds + K3_S1 + c * 16;
#pragma unroll
    for (int ph = 0; ph < 2; ++ph) {
#pragma unroll
        for (int m = 0; m < 8; ++m) {
            const float nm = *(LAS float*)(size_t)(s1a + ((((m >> 1) ^ wid) << 1) + (m & 1)) * 256);
            acc[m][0] = cs[ph][0] * nm; acc[m][1] = cs[ph][1] * nm;
            if (m < 2) accx[m] = cs[ph][2] * nm;
        }
        PINACC();
        const u32 nxt = WS_WIN + (9 * pn + 2 * wid) * 18432;
        for (int j = 0; j < 3; ++j) {
            STEP(0, true, j == 2 ? nxt : so + 3072, 0);
            STEP(1, true, 0, 0);
            STEP(2, ph == 0 || j < 2, 0, 0);
        }
        asm volatile("s_nop 15" ::: "memory");
        STAMP(layer * 4 + 2, 2 + 2 * ph);
        if (ph == 1) {
            vB = lane * 16 + 65536;
            so = WS_Z + layer * ZB + (9 * pn + 2 * wid) * 65536;
            FENCE();
            early = __builtin_amdgcn_readfirstlane(ldsw(FL + 84));
            FENCE();
            if (early) BPRO(16);
        }
        float t1[8], t2[8];
        f32x4 rcs[8];
#pragma unroll
        for (int m = 0; m < 8; ++m) rcs[m] = *(LAS f32x4*)(size_t)(s1a + ((((m >> 1) ^ wid) << 1) + (m & 1)) * 256);
#pragma unroll
        for (int m = 0; m < 8; ++m) {
            constexpr int NP = 6;
            const int np = m < 2 ? 6 : 4;
            const f32x2 k1 = {rcs[m][1], rcs[m][1]}, k3 = {rcs[m][2], rcs[m][2]}, sg = {rcs[m][3], rcs[m][3]};
            f32x2 v[NP], e[NP];
#pragma unroll
            for (int i = 0; i < NP; ++i) if (i < np) { const f32x4 av = i < 4 ? acc[m][i >> 1] : accx[m & 1]; v[i] = (f32x2){av[2 * (i & 1)], av[2 * (i & 1) + 1]}; }
#pragma unroll
            for (int i = 0; i < NP; ++i) if (i < np) e[i] = v[i] * v[i];
#pragma unroll
            for (int i = 0; i < NP; ++i) if (i < np) e[i] = e[i] * k3 + k1;
#pragma unroll
            for (int i = 0; i < NP; ++i) if (i < np) e[i] = e[i] * v[i];
            SB();
#pragma unroll
            for (int i = 0; i < NP; ++i) if (i < np) { e[i][0] = __builtin_amdgcn_exp2f(e[i][0]); e[i][1] = __builtin_amdgcn_exp2f(e[i][1]); }
            SB();
#pragma unroll
            for (int i = 0; i < NP; ++i) if (i < np) e[i] = e[i] * sg + sg;
            SB();
#pragma unroll
            for (int i = 0; i < NP; ++i) if (i < np) { e[i][0] = __builtin_amdgcn_rcpf(e[i][0]); e[i][1] = __builtin_amdgcn_rcpf(e[i][1]); }
            SB();
#pragma unroll
            for (int i = 0; i < NP; ++i) if (i < np) v[i] = v[i] * e[i];
#pragma unroll
            for (int i = 0; i < NP; ++i) if (i < np) yp[i < 4 ? 2 * m + (i >> 1) : 16 + m][i & 1] = pk2(v[i][0], v[i][1]);
            if (ph == 0) {
                f32x2 u1 = (v[0] + v[1]) + (v[2] + v[3]), u2 = v[0] * v[0], u3 = v[1] * v[1];
                u2 = v[2] * v[2] + u2; u3 = v[3] * v[3] + u3;
                if (m < 2) { u1 += v[4] + v[5]; u2 = v[4] * v[4] + u2; u3 = v[5] * v[5] + u3; }
                u2 += u3;
                t1[m] = u1[0] + u1[1]; t2[m] = u2[0] + u2[1];
            }
            PINV(yp[2 * m][0]); PINV(yp[2 * m][1]); PINV(yp[2 * m + 1][0]); PINV(yp[2 * m + 1][1]);
            if (m < 2) { PINV(yp[16 + m][0]); PINV(yp[16 + m][1]); }
            if (ph == 0) {
                const u32 row = (((((m >> 1) ^ wid) << 1) + (m & 1)) << 4) + c;
                u32x4 o = {yp[2 * m][0], yp[2 * m][1], yp[2 * m + 1][0], yp[2 * m + 1][1]};
                *(LAS u32x4*)(size_t)(lds + K3_PK + row * K3_PP + 64 * wid + 16 * g) = o;
                if (m < 2) { u32x2 ox = {yp[16 + m][0], yp[16 + m][1]}; *(LAS u32x2*)(size_t)(lds + K3_PK + row * K3_PP + 256 + 8 * g) = ox; }
            }
        }
        if (ph == 0) {
#pragma unroll
            for (int m = 0; m < 4; ++m) {
                auto r1 = __builtin_amdgcn_permlane32_swap(F2U(t1[m]), F2U(t1[m + 4]), false, false);
                auto r2 = __builtin_amdgcn_permlane32_swap(F2U(t2[m]), F2U(t2[m + 4]), false, false);
                t1[m] = U2F(r1[0]) + U2F(r1[1]); t2[m] = U2F(r2[0]) + U2F(r2[1]);
            }
#pragma unroll
            for (int m = 0; m < 2; ++m) {
                auto r1 = __builtin_amdgcn_permlane16_swap(F2U(t1[m]), F2U(t1[m + 2]), false, false);
                auto r2 = __builtin_amdgcn_permlane16_swap(F2U(t2[m]), F2U(t2[m + 2]), false, false);
                f32x2 o = {U2F(r1[0]) + U2F(r1[1]), U2F(r2[0]) + U2F(r2[1])};
                *(LAS f32x2*)(size_t)(lds + K3_WS + (wid * 128 + 32 * (g ^ wid) + 16 * m + c) * 8) = o;
            }
            WAIT_L0();
            stsw(FL + 48 + wid * 4, 1u);
            FENCE();
        }
        STAMP(layer * 4 + 2, 3 + 2 * ph);
    }
    {
        float bv[8];
#pragma unroll
        for (int m = 0; m < 8; ++m) bv[m] = bflo(bsp[128 * pm + (((((m >> 1) ^ wid) << 1) + (m & 1)) << 4) + c]);
#pragma unroll
        for (int m = 0; m < 8; ++m) {
            acc[m][0] = acc[m][1] = (f32x4){bv[m], bv[m], bv[m], bv[m]};
            if (m < 2) accx[m] = acc[m][0];
        }
    }
#pragma unroll
    for (int q = 0; q < 4; ++q) { pa[0][q] = lds + (c >> 3) * 1024 + (c & 7) * 128 + ((g ^ ((c >> 1) & 7)) * 16) + ((q ^ wid) * 4096); pa[1][q] = pa[0][q] ^ 64; }
    if (!early) { lds_wait(FL + 84, 1u); BPRO(16); }
    lds_wait(fladdr, 19u);
    STAMP(layer * 4 + 2, 6);
    ALOAD();
    PINACC();
    for (int j = 0; j < 10; ++j) {
        STEP(0, true, so + 3072, 16);
        STEP(1, true, 0, 16);
        STEP(2, true, 0, 16);
    }
    so -= 8192;
    STEP(0, true, so, 16);
    STEP(1, false, 0, 16);
    asm volatile("s_nop 15" ::: "memory");
    STAMP(layer * 4 + 2, 7);
    {
        char* hb = ws + WS_H + layer * ZB + (8 * pm * 8 + (pn >> 1)) * 9216;
#pragma unroll
        for (int m = 0; m < 8; ++m) {
            const int mt = (((m >> 1) ^ wid) << 1) + (m & 1);
            u32x4 o;
            o[0] = pk2(bflo(yp[2 * m][0]) * acc[m][0][0], bfhi(yp[2 * m][0]) * acc[m][0][1]);
            o[1] = pk2(bflo(yp[2 * m][1]) * acc[m][0][2], bfhi(yp[2 * m][1]) * acc[m][0][3]);
            o[2] = pk2(bflo(yp[2 * m + 1][0]) * acc[m][1][0], bfhi(yp[2 * m + 1][0]) * acc[m][1][1]);
            o[3] = pk2(bflo(yp[2 * m + 1][1]) * acc[m][1][2], bfhi(yp[2 * m + 1][1]) * acc[m][1][3]);
            *(u32x4*)(hb + mt * 73728 + ((pn & 1) * 4 + wid) * 1024 + lane * 16) = o;
            if (m < 2) {
                u32x2 ox = {pk2(bflo(yp[16 + m][0]) * accx[m][0], bfhi(yp[16 + m][0]) * accx[m][1]), pk2(bflo(yp[16 + m][1]) * accx[m][2], bfhi(yp[16 + m][1]) * accx[m][3])};
                *(u32x2*)(hb + mt * 73728 + 8192 + (pn & 1) * 512 + lane * 8) = ox;
            }
        }
    }
    STAMP(layer * 4 + 2, 12);
    STAMP_OUT(layer * 4 + 2);
}

extern "C" void kernel_launch(void* const* d_in, const int* in_sizes, int n_in, void* d_out, int out_size, void* d_ws, size_t ws_size,
                              hipStream_t stream) {
    const u16* x = (const u16*)d_in[0];
    const u16* wqkv = (const u16*)d_in[1];
    const u16* wo = (const u16*)d_in[2];
    const u16* win = (const u16*)d_in[4];
    const u16* wsp = (const u16*)d_in[5];
    const u16* bsp = (const u16*)d_in[6];
    const u16* wout = (const u16*)d_in[7];
    char* ws = (char*)d_ws;
    u16* out = (u16*)d_out;
    (void)hipFuncSetAttribute((const void*)k1_attn, hipFuncAttributeMaxDynamicSharedMemorySize, K1_LDS);
    (void)hipFuncSetAttribute((const void*)k34_mlp, hipFuncAttributeMaxDynamicSharedMemorySize, K3_LDS);
    (void)hipFuncSetAttribute((const void*)k2_proj<0>, hipFuncAttributeMaxDynamicSharedMemorySize, K2_LDS);
    (void)hipFuncSetAttribute((const void*)k2_proj<1>, hipFuncAttributeMaxDynamicSharedMemorySize, K2_LDS);
    (void)hipFuncSetAttribute((const void*)k2_proj<2>, hipFuncAttributeMaxDynamicSharedMemorySize, K2_LDS);
    k0_prep<<<K0_GRID, 576, 0, stream>>>(x, wqkv, wo, win, wout, ws);
    for (int layer = 0; layer < 2; ++layer) {
        k1_attn<<<256, 512, K1_LDS, stream>>>(ws, layer);
        k2_proj<0><<<256, 256, K2_LDS, stream>>>(ws, out, layer);
        k34_mlp<<<256, 512, K3_LDS, stream>>>(ws, wsp, bsp, layer);
        if (layer == 0) k2_proj<1><<<256, 256, K2_LDS, stream>>>(ws, out, layer);
        else k2_proj<2><<<256, 256, K2_LDS, stream>>>(ws, out, layer);
    }
}
```

```cpp
#include <hip/hip_runtime.h>
#include <hip/hip_bf16.h>
#include <stdint.h>

#define LAS __attribute__((address_space(3)))
#define GAS __attribute__((address_space(1)))
typedef unsigned short u16;
typedef unsigned u32;
typedef unsigned long long u64;
typedef short bf16x8 __attribute__((ext_vector_type(8)));
typedef float f32x2 __attribute__((ext_vector_type(2)));
typedef float f32x4 __attribute__((ext_vector_type(4)));
typedef unsigned u32x2 __attribute__((ext_vector_type(2)));
typedef unsigned u32x4 __attribute__((ext_vector_type(4)));
typedef __bf16 bf2_t __attribute__((ext_vector_type(2)));

#define ANY(p) (__builtin_amdgcn_ballot_w64(p) != 0)
#define U2F(x) __builtin_bit_cast(float, (u32)(x))
#define F2U(x) __builtin_bit_cast(u32, (float)(x))
#define WAIT_V(n) asm volatile("s_waitcnt vmcnt(%0)" ::"i"(n) : "memory")
#define WAIT_L0() asm volatile("s_waitcnt lgkmcnt(0)" ::: "memory")
#define FENCE() asm volatile("" ::: "memory")
#define SB() __builtin_amdgcn_sched_barrier(0)
#define BAR() do { FENCE(); __builtin_amdgcn_s_barrier(); FENCE(); } while (0)
#define WCNT(vm, lg) __builtin_amdgcn_s_waitcnt(((vm) & 15) | (7 << 4) | ((lg) << 8) | (((vm) >> 4) << 14))
#define MFMA(acc, x, y) asm volatile("v_mfma_f32_16x16x32_bf16 %0, %1, %2, %0" : "+v"(acc) : "v"(x), "v"(y))
#define MFMAB(x, y, c) __builtin_amdgcn_mfma_f32_16x16x32_bf16(x, y, c, 0, 0, 0)
#define BST128(o, rs, vo, so, aux) do { __builtin_amdgcn_raw_buffer_store_b128(o, rs, vo, so, aux); SB(); asm volatile("s_nop 1"); SB(); } while (0)
#define PINV(x) asm volatile("" : "+v"(x))
#define RLX_AGENT __ATOMIC_RELAXED, __HIP_MEMORY_SCOPE_AGENT

constexpr int S_ = 2048, D_ = 576, FH_ = 2304;
#define XDELAY1 10
#define XDELAY3 20
#define LGATE 12
#define LGATE2 14
#define POLLD 40
#define STAG 3
#define HPRIO 3
#define FDELAY 45
#define STAG2 60
#define MAP1 1
#define MAP3 1
#define CODETOUCH 1
#define WSPTOUCH 1
#define LSH 0
#define L2PF 0
#define ZSPLIT 10
#define ZGATE 8
#define QLATE 0
#define BADV 3072
#define AFAKE 0
#define BAUX 16
#define HDELAY 10
#define MAP5 1
#define WGATE 0
#define WGI 7
#define WEARLY 0
constexpr u32 SENT = 0xFFFFFFFFu;
constexpr u32 SPIN_CAP = 1u << 16;

constexpr u32 WS_WQ = 0;
constexpr u32 WS_CSQ = WS_WQ + 16 * 129024;
constexpr u32 WS_WO = WS_CSQ + 16384;
constexpr u32 WS_WIN = WS_WO + 8 * 18 * 4608;
constexpr u32 WS_CSIN = WS_WIN + 288 * 18 * 1024;
constexpr u32 WS_WOUT = WS_CSIN + 2 * 4608 * 4;
constexpr u32 WS_X = WS_WOUT + 8 * 72 * 4608;
constexpr u32 XB = 128 * 18 * 1024, XSB = 2048 * 64, ZB = 144 * 64 * 1024;
constexpr u32 WS_XS = WS_X + 4 * XB;
constexpr u32 WS_O = WS_XS + 4 * XSB;
constexpr u32 WS_Z = WS_O + (1 + LSH) * XB;
constexpr u32 WS_H = WS_Z + (1 + LSH) * ZB;
constexpr u32 WS_KVP = WS_H + (1 + LSH) * ZB;
constexpr u32 WS_ZST = WS_KVP + 2 * 256 * 2816;
constexpr u32 WS_CNT = WS_ZST + 2 * 256 * 1024;
constexpr u32 WS_END = WS_CNT + 2 * 16 * 32 * 128;
constexpr u32 WS_DBG = WS_END;

#define STAMP_DECL
#define STAMP(k, i) do { } while (0)
#define HST(i) do { } while (0)
#define HST_DECL
#define HST_OUT(k) do { } while (0)
#define STAMPV(k, i, v) do { } while (0)
#define STAMP_OUT(k) do { } while (0)

__device__ __forceinline__ u32 pk2(float lo, float hi) {
    f32x2 v = {lo, hi};
    bf2_t b = __builtin_convertvector(v, bf2_t);
    return __builtin_bit_cast(u32, b);
}
__device__ __forceinline__ float bflo(u32 w) { return U2F(w << 16); }
__device__ __forceinline__ float bfhi(u32 w) { return U2F(w & 0xffff0000u); }
__device__ __forceinline__ bf16x8 mk8(u32 a, u32 b, u32 c, u32 d) { u32x4 v = {a, b, c, d}; return __builtin_bit_cast(bf16x8, v); }
__device__ __forceinline__ float sfma(float a, float b, float c) { float r; asm("v_fma_f32 %0, %1, %2, %3" : "=v"(r) : "v"(a), "v"(b), "v"(c)); return r; }
__device__ __forceinline__ f32x4 sadd4(f32x4 a, f32x4 b) {
    float r0 = a[0], r1 = a[1], r2 = a[2], r3 = a[3];
    asm("v_add_f32 %0, %0, %4\n\tv_add_f32 %1, %1, %5\n\tv_add_f32 %2, %2, %6\n\tv_add_f32 %3, %3, %7" : "+v"(r0), "+v"(r1), "+v"(r2), "+v"(r3) : "v"(b[0]), "v"(b[1]), "v"(b[2]), "v"(b[3]));
    f32x4 r = {r0, r1, r2, r3};
    return r;
}
__device__ __forceinline__ float phi(float t) {
    float e = __builtin_amdgcn_exp2f(t * 1.44269504089f);
    return (t > 0.f ? t + 1.f : e) + 1e-6f;
}
__device__ __forceinline__ u32 ldsw(u32 a) { return *(volatile LAS u32*)(size_t)a; }
__device__ __forceinline__ void stsw(u32 a, u32 v) { *(volatile LAS u32*)(size_t)a = v; }
#define LDSR(addr, off) (*(LAS bf16x8*)((LAS char*)(size_t)(addr) + (off)))
__device__ __forceinline__ void lds_wait(u32 a, u32 target) {
    FENCE();
#pragma nounroll
    for (u32 n = 0; n < SPIN_CAP; ++n) { const u32 v = ldsw(a); if (!ANY(v < target)) break; }
    FENCE();
}
__device__ __forceinline__ void lds_wait_gt(u32 a, u32 low) {
    FENCE();
#pragma nounroll
    for (u32 n = 0; n < SPIN_CAP; ++n) { const u32 v = ldsw(a); if (!ANY(v <= low)) break; }
    FENCE();
}
__device__ __forceinline__ void lds_wait_sleep(u32 a, u32 target) {
    FENCE();
#pragma nounroll
    for (u32 n = 0; n < SPIN_CAP; ++n) { const u32 v = ldsw(a); if (!ANY(v < target)) break; __builtin_amdgcn_s_sleep(1); }
    FENCE();
}
#define DMA4(v0, v1, v2, v3, sbase, m0v)                                                                            \
    asm volatile("s_mov_b32 m0, %5\n\ts_nop 0\n\tglobal_load_lds_dwordx4 %0, %4\n\t"                                \
                 "global_load_lds_dwordx4 %1, %4 offset:1024\n\tglobal_load_lds_dwordx4 %2, %4 offset:2048\n\t"     \
                 "global_load_lds_dwordx4 %3, %4 offset:3072" ::"v"(v0), "v"(v1), "v"(v2), "v"(v3), "s"(sbase), "s"(m0v) : "memory")
#define DMA2(v0, v1, sbase, m0v)                                                                                    \
    asm volatile("s_mov_b32 m0, %3\n\ts_nop 0\n\tglobal_load_lds_dwordx4 %0, %2\n\t"                                \
                 "global_load_lds_dwordx4 %1, %2 offset:1024" ::"v"(v0), "v"(v1), "s"(sbase), "s"(m0v) : "memory")

__device__ __forceinline__ u32x4 tcol(const u32x2 (&ld)[8], int i) {
    const u32 sel = (i & 1) ? 0x07060302u : 0x05040100u;
    u32x4 o;
    o.x = __builtin_amdgcn_perm(ld[1][i >> 1], ld[0][i >> 1], sel);
    o.y = __builtin_amdgcn_perm(ld[3][i >> 1], ld[2][i >> 1], sel);
    o.z = __builtin_amdgcn_perm(ld[5][i >> 1], ld[4][i >> 1], sel);
    o.w = __builtin_amdgcn_perm(ld[7][i >> 1], ld[6][i >> 1], sel);
    return o;
}
__device__ __forceinline__ float sum8(u32x4 o) {
    return ((bflo(o.x) + bfhi(o.x)) + (bflo(o.y) + bfhi(o.y))) + ((bflo(o.z) + bfhi(o.z)) + (bflo(o.w) + bfhi(o.w)));
}

constexpr int K0_QKV = 128, K0_X = 64, K0_WIN = 144, K0_WO = 90, K0_INIT = 54, K0_TOUCH = WSPTOUCH ? 114 : 0, K0_GRID = K0_QKV + K0_X + K0_WIN + K0_WO + K0_INIT + K0_TOUCH;
__device__ __forceinline__ void k0_body(const u16* __restrict__ x, const u16* __restrict__ wqkv, const u16* __restrict__ wo,
                                        const u16* __restrict__ win, const u16* __restrict__ wout, const u16* __restrict__ wsp, char* __restrict__ ws) {
    __shared__ __attribute__((aligned(16))) char img[36864 + 576 * 8];
    float (*red)[64] = (float (*)[64])(img + 36864);
    const int tid = threadIdx.x, w = __builtin_amdgcn_readfirstlane(tid >> 6), lane = tid & 63, c = lane & 15, g = lane >> 4;
    int b = blockIdx.x;
    if (b < K0_QKV) {
        const int hf = b & 1, fp = (b >> 1) & 3, p = b >> 3, kt = 9 * hf + w;
        const int fi = 2 * fp + (c >> 3), cc = 2 * (c & 7);
        int col; bool ok = true;
        if (fi == 4) { ok = cc < 12; const int tt = (cc >> 2) == 0 ? 1 : ((cc >> 2) == 1 ? 2 : 0); col = 576 * tt + 18 * (2 * p + ((cc >> 1) & 1)) + 16; }
        else if (fi < 4) col = 576 * (1 + (fi >> 1)) + 18 * (2 * p + (fi & 1)) + cc;
        else col = 18 * (2 * p + ((fi - 5) & 1)) + cc;
        u32 ld[8];
#pragma unroll
        for (int j = 0; j < 8; ++j) ld[j] = *(const u32*)(wqkv + (size_t)(32 * kt + 8 * g + j) * 1728 + col);
        SB();
        const u32 msk = ok ? 0xffffffffu : 0u;
#pragma unroll
        for (int j = 0; j < 8; ++j) ld[j] &= msk;
#pragma unroll
        for (int i = 0; i < 2; ++i) {
            const u32 sel = i ? 0x07060302u : 0x05040100u;
            u32x4 o;
            o.x = __builtin_amdgcn_perm(ld[1], ld[0], sel); o.y = __builtin_amdgcn_perm(ld[3], ld[2], sel);
            o.z = __builtin_amdgcn_perm(ld[5], ld[4], sel); o.w = __builtin_amdgcn_perm(ld[7], ld[6], sel);
            *(u32x4*)(img + w * 2048 + (c >> 3) * 1024 + (cc + i + 16 * g) * 16) = o;
            float cs = sum8(o);
            cs += __shfl_xor(cs, 16); cs += __shfl_xor(cs, 32);
            if (g == 0) red[w][2 * c + i] = cs;
        }
        FENCE();
#pragma unroll
        for (int t = 0; t < 2; ++t) {
            const int f2 = 2 * fp + t;
            const u32 tile = f2 < 5 ? (kt * 5 + f2) * 1024 : 92160 + (kt * 2 + f2 - 5) * 1024;
            if (f2 < 7) *(u32x4*)(ws + WS_WQ + p * 129024 + tile + lane * 16) = *(const u32x4*)(img + w * 2048 + t * 1024 + lane * 16);
        }
        __syncthreads();
        if (tid < 32) {
            float s = 0.f;
#pragma unroll
            for (int k = 0; k < 9; ++k) s += red[k][tid];
            ((float*)(ws + WS_CSQ))[(hf * 16 + p) * 128 + fp * 32 + tid] = s;
        }
        return;
    }
    b -= K0_QKV;
    if (b < K0_X) {
        const int rl = tid / 18, t18 = tid - 18 * rl;
        u32x4 v[4];
#pragma unroll
        for (int i = 0; i < 4; ++i) v[i] = *(const u32x4*)(x + (size_t)(32 * b + rl) * 576 + (18 * i + t18) * 8);
        float s1 = 0.f, s2 = 0.f;
#pragma unroll
        for (int i = 0; i < 4; ++i) {
            const int ch = 18 * i + t18;
            *(u32x4*)(img + ((rl >> 4) * 18 + (ch >> 2)) * 1024 + ((rl & 15) + 16 * (ch & 3)) * 16) = v[i];
#pragma unroll
            for (int k = 0; k < 4; ++k) { const float a = bflo(v[i][k]), bb = bfhi(v[i][k]); s1 += a + bb; s2 += a * a + bb * bb; }
        }
        f32x2 o = {s1, s2};
        ((f32x2*)(img + 36864))[tid] = o;
        __syncthreads();
#pragma unroll
        for (int t = 0; t < 4; ++t) *(u32x4*)(ws + WS_X + b * 36864 + (w * 4 + t) * 1024 + lane * 16) = *(const u32x4*)(img + (w * 4 + t) * 1024 + lane * 16);
        if (tid < 256) {
            const int r = tid & 31, q = tid >> 5;
            const f32x2* pp = (const f32x2*)(img + 36864) + r * 18;
            o = pp[2 * q] + pp[2 * q + 1];
            if (q < 2) o += pp[16 + q];
            *(f32x2*)(ws + WS_XS + (q * 2048 + 32 * b + r) * 8) = o;
        }
        return;
    }
    b -= K0_X;
    if (b < K0_WIN) {
        const int hf = b & 1, cb = b >> 1, kt = 9 * hf + w;
        u32x2 ld[8];
#pragma unroll
        for (int j = 0; j < 8; ++j) ld[j] = *(const u32x2*)(win + (size_t)(32 * kt + 8 * g + j) * 4608 + 64 * cb + 4 * c);
#pragma unroll
        for (int i = 0; i < 4; ++i) {
            const u32x4 o = tcol(ld, i);
            *(u32x4*)(img + w * 4096 + (c >> 2) * 1024 + (4 * (c & 3) + i + 16 * g) * 16) = o;
            float cs = sum8(o);
            cs += __shfl_xor(cs, 16); cs += __shfl_xor(cs, 32);
            if (g == 0) red[w][4 * c + i] = cs;
        }
        FENCE();
#pragma unroll
        for (int t = 0; t < 4; ++t) *(u32x4*)(ws + WS_WIN + ((4 * cb + t) * 18 + kt) * 1024 + lane * 16) = *(const u32x4*)(img + w * 4096 + t * 1024 + lane * 16);
        __syncthreads();
        if (tid < 64) {
            float s = 0.f;
#pragma unroll
            for (int k = 0; k < 9; ++k) s += red[k][tid];
            ((float*)(ws + WS_CSIN))[hf * 4608 + 64 * cb + tid] = s;
        }
        return;
    }
    b -= K0_WIN;
    if (b < K0_WO) {
        const bool iso = b < 18;
        const int sl = iso ? b : b - 18, G = sl / 9, t = sl - 9 * G;
        const u16* src = iso ? wo : wout;
        const int col = 64 * w + 4 * c, nt = col / 72, cl = col - 72 * nt;
        const int r0 = iso ? (sl < 16 ? 36 * sl : 288 * (sl - 16) + 16) : (t < 8 ? 288 * G + 144 * (t >> 2) + 32 * (t & 3) : 288 * G + 128);
        const int gs = (iso && sl >= 16) ? 72 : 4, s4 = iso ? (sl < 16 ? 18 : 36) : (t < 8 ? 16 : 144), s2 = (iso && sl >= 16) ? 18 : 2;
        u32x2 ld[8];
#pragma unroll
        for (int j = 0; j < 8; ++j) ld[j] = *(const u32x2*)(src + (size_t)(r0 + gs * g + s4 * (j >> 2) + s2 * ((j >> 1) & 1) + (j & 1)) * 576 + col);
        const int n = cl < 64 ? 2 * (cl >> 5) + ((cl >> 2) & 1) : 4, ii = cl < 64 ? 4 * ((cl >> 3) & 3) : cl - 64;
#pragma unroll
        for (int i = 0; i < 4; ++i) {
            const u32 off = n < 4 ? n * 1024 + (ii + i + 16 * g) * 16 : 4096 + g * 128 + (ii + i) * 16;
            *(u32x4*)(img + nt * 4608 + off) = tcol(ld, i);
        }
        __syncthreads();
#pragma unroll
        for (int k = 0; k < 4; ++k) {
            const u32 i = (w * 4 + k) * 1024 + lane * 16, n2 = i / 4608, off = i - 4608 * n2;
            *(u32x4*)(ws + (iso ? WS_WO + (n2 * 18 + sl) * 4608 : WS_WOUT + (n2 * 72 + sl) * 4608) + off) = *(const u32x4*)(img + i);
        }
        return;
    }
    b -= K0_WO;
    if (b >= K0_INIT) {
        __builtin_amdgcn_s_sleep(WSPTOUCH);
        const u32 ln = (b - K0_INIT) * 576 + tid;
        if (ln < 65536) { const u32 v = *(const u32*)(wsp + (size_t)ln * 64); asm volatile("" :: "v"(v)); }
        return;
    }
    if (b < 16) {
        u64 pc; int dist;
        asm volatile("s_getpc_b64 %0\n\ts_mov_b32 %1, _Z7k2_projILi0EEvPcPti@rel32@lo+4" : "=&s"(pc), "=&s"(dist));
        if (tid * 128 < dist) { u32 v = *(const GAS u32*)(pc + tid * 128); asm volatile("" :: "v"(v)); }
    }
    {
        const auto rs = __builtin_amdgcn_make_buffer_rsrc(ws, 0, WS_END, 0x00020000);
        const u32 n16 = (WS_END - WS_KVP) / 16, nz = (WS_CNT - WS_KVP) / 16;
        for (u32 i = b * 576 + tid; i < n16; i += K0_INIT * 576) {
            const u32 v = i < nz ? SENT : 0u;
            u32x4 o = {v, v, v, v};
            BST128(o, rs, i * 16, WS_KVP, 16);
        }
    }
}

__global__ __launch_bounds__(576) void k0_prep(const u16* __restrict__ x, const u16* __restrict__ wqkv, const u16* __restrict__ wo,
                                               const u16* __restrict__ win, const u16* __restrict__ wout, const u16* __restrict__ wsp, char* __restrict__ ws) {
    STAMP_DECL;
    STAMP(8, 0);
    k0_body(x, wqkv, wo, win, wout, wsp, ws);
    STAMP(8, 1);
    STAMP_OUT(11);
}

__device__ __forceinline__ void x_stats(const char* ws, int ver, int row, float& mean, float& var) {
    const f32x2* p = (const f32x2*)(ws + WS_XS + ver * XSB) + row;
    f32x2 v[8];
#pragma unroll
    for (int k = 0; k < 8; ++k) v[k] = p[k * 2048];
    const f32x2 sm = ((v[0] + v[1]) + (v[2] + v[3])) + ((v[4] + v[5]) + (v[6] + v[7]));
    const float s1 = sm[0], s2 = sm[1];
    mean = s1 * (1.f / 576.f);
    var = fmaxf(s2 * (1.f / 576.f) - mean * mean, 0.f);
}

constexpr u32 K1_PW = 129024, K1_OP = K1_PW + 4 * 176 * 16, K1_ST = K1_OP + 4096, K1_CS = K1_ST + 1024, K1_FL = K1_CS + 512, K1_LDS = K1_FL + 64;

__global__ __launch_bounds__(512, 2) void k1_attn(char* __restrict__ ws, int layer) {
    extern __shared__ __attribute__((aligned(16))) char shm_[];
    const u32 lds = (u32)(size_t)(LAS char*)shm_;
    const int tid = threadIdx.x, wid = __builtin_amdgcn_readfirstlane(tid >> 6), lane = tid & 63, c = lane & 15, g = lane >> 4;
    const int xcd = blockIdx.x & 7, bi = blockIdx.x >> 3;
    const int pm = MAP1 ? (xcd & 3) * 4 + (bi & 3) : (xcd & 1) * 8 + (bi & 7), p = MAP1 ? (xcd >> 2) * 8 + (bi >> 2) : (xcd >> 1) * 4 + (bi >> 3);
    const u32 FL = lds + K1_FL;
    STAMP_DECL;
    if (wid == 4) {
        const char* src = ws + WS_WQ + p * 129024;
        const u32 vo = lane * 16;
#define K1_U10(u) do { DMA4(vo, vo, vo, vo, src + (u) * 10240, lds + (u) * 10240); DMA4(vo, vo, vo, vo, src + (u) * 10240 + 4096, lds + (u) * 10240 + 4096); \
                       DMA2(vo, vo, src + (u) * 10240 + 8192, lds + (u) * 10240 + 8192); } while (0)
#define K1_U12(u) do { DMA4(vo, vo, vo, vo, src + 92160 + (u) * 12288, lds + 92160 + (u) * 12288); DMA4(vo, vo, vo, vo, src + 92160 + (u) * 12288 + 4096, lds + 92160 + (u) * 12288 + 4096); \
                       DMA4(vo, vo, vo, vo, src + 92160 + (u) * 12288 + 8192, lds + 92160 + (u) * 12288 + 8192); } while (0)
        K1_U10(0);
        BAR();
        K1_U10(1); K1_U10(2);
#pragma unroll
        for (int u = 3; u < 9; ++u) { WAIT_V(20); stsw(FL, u - 2); FENCE(); K1_U10(u); }
        WAIT_V(20); stsw(FL, 7); FENCE(); K1_U12(0);
        WAIT_V(22); stsw(FL, 8); FENCE(); K1_U12(1);
        WAIT_V(24); stsw(FL, 9); FENCE(); K1_U12(2);
        WAIT_V(24); stsw(FL, 10); FENCE();
        WAIT_V(12); stsw(FL, 11); FENCE();
        WAIT_V(0); stsw(FL, 12);
        return;
    }
    if (wid > 4) {
        const int ht = tid - 320;
        {
            u32x4 z = {0u, 0u, 0u, 0u};
            *(LAS u32x4*)(size_t)(lds + K1_OP + ht * 16) = z;
            if (ht < 64) *(LAS u32x4*)(size_t)(lds + K1_OP + 3072 + ht * 16) = z;
            if (ht >= 128 && ht < 144) stsw(FL + (ht - 128) * 4, 0u);
        }
        WAIT_L0();
        BAR();
        if (wid != 7) {
            float mean, var;
            x_stats(ws, 2 * layer, 128 * pm + ht, mean, var);
            const float r = __builtin_amdgcn_rsqf(var + 1e-5f);
            f32x2 o = {r, -mean * r};
            *(LAS f32x2*)(size_t)(lds + K1_ST + ht * 8) = o;
        } else {
            const f32x2* cp = (const f32x2*)(ws + WS_CSQ) + p * 64 + lane;
            *(LAS f32x2*)(size_t)(lds + K1_CS + lane * 8) = cp[0] + cp[1024];
        }
        FENCE();
        stsw(FL + 4 + (wid - 5) * 4, 1u);
        __builtin_amdgcn_s_setprio(3);
        lds_wait_sleep(FL + 16 + (lane & 3) * 4, 1u);
        STAMP(layer * 4 + 0, 7);
        const int it = ht < 173 ? ht : 172;
        f32x4 v = *(LAS f32x4*)(size_t)(lds + K1_PW + it * 16);
#pragma unroll
        for (int k = 1; k < 4; ++k) v = sadd4(v, *(LAS f32x4*)(size_t)(lds + K1_PW + k * 2816 + it * 16));
        const auto rs = __builtin_amdgcn_make_buffer_rsrc(ws, 0, WS_END, 0x00020000);
        const u32 gbase = WS_KVP + (layer * 16 + p) * 16 * 2816;
        {
            u32x4 o = __builtin_bit_cast(u32x4, v);
#pragma unroll
            for (int k = 0; k < 4; ++k) o[k] = o[k] == SENT ? 0x7fc00000u : o[k];
            if (ht < 173) BST128(o, rs, it * 16, gbase + pm * 2816, 16);
        }
        u32x4 t[16];
        STAMP(layer * 4 + 0, 8); STAMP(layer * 4 + 0, 9);
        __builtin_amdgcn_s_sleep(XDELAY1);
        u32 spins = 0, miss = 0xffffu;
        for (; spins < SPIN_CAP; ++spins) {
            FENCE();
#pragma unroll
            for (int k = 0; k < 16; ++k) if (miss >> k & 1) t[k] = __builtin_amdgcn_raw_buffer_load_b128(rs, it * 16, gbase + k * 2816, 16);
            u32 nm = 0;
#pragma unroll
            for (int k = 0; k < 16; ++k) if (miss >> k & 1) { const u32 mx = max(max(t[k][0], t[k][1]), max(t[k][2], t[k][3])); nm |= ANY(mx == SENT) ? 1u << k : 0u; }
            miss = nm;
            if (spins == 0) STAMP(layer * 4 + 0, 9);
            if (!miss) break;
        }
        STAMP(layer * 4 + 0, 10); STAMPV(layer * 4 + 0, 15, spins);
        v = __builtin_bit_cast(f32x4, t[0]);
#pragma unroll
        for (int k = 1; k < 16; ++k) v = sadd4(v, __builtin_bit_cast(f32x4, t[k]));
        const u32 lo = pk2(v[0], v[1]), hi = pk2(v[2], v[3]);
        const u32x2 lh = {lo, hi};
        const u32 OP = lds + K1_OP;
        if (ht < 128) *(LAS u32x2*)(size_t)(OP + ht * 16) = lh;
        else if (ht < 144) { stsw(OP + (ht - 128 + 32) * 16 + 8, lo); stsw(OP + 1024 + (ht - 128 + 32) * 16 + 12, hi); }
        else if (ht < 168) {
            const int q = ht - 144, hb = q >= 12, qq = q - 12 * hb, tt = qq >> 2, gg = qq & 3;
            const u32 fb = OP + 2048 + hb * 1024 + gg * 256;
            if (tt < 2) *(LAS u32x2*)(size_t)(fb + (4 * hb + tt) * 16) = lh;
            else {
#pragma unroll
                for (int k = 0; k < 4; ++k) *(LAS u32x2*)(size_t)(fb + (4 * k + 2 + hb) * 16) = lh;
            }
        } else if (ht < 173) {
            const int q = ht - 168;
            if (q < 2) stsw(OP + 2048 + (32 + q) * 16 + 8, lo);
            else if (q < 4) stsw(OP + 3072 + (32 + 2 + q) * 16 + 12, hi);
            else {
#pragma unroll
                for (int k = 0; k < 4; ++k) { stsw(OP + 2048 + (32 + 4 * k + 2) * 16 + 8, lo); stsw(OP + 3072 + (32 + 4 * k + 3) * 16 + 12, hi); }
            }
        }
        WAIT_L0();
        stsw(FL + 32 + (wid - 5) * 4, 1u);
        if (wid == 5) STAMP_OUT(layer * 4 + 0);
        return;
    }
    STAMP(layer * 4 + 0, 0);

    const auto rs = __builtin_amdgcn_make_buffer_rsrc(ws, 0, WS_END, 0x00020000);
    const u32 xb = WS_X + 2 * layer * XB + (8 * pm + 2 * wid) * 18432, vo = lane * 16;
    bf16x8 xa[2][18];
#define K1_LX(m, kt) xa[m][kt] = __builtin_bit_cast(bf16x8, __builtin_amdgcn_raw_buffer_load_b128(rs, vo, xb + ((m) * 18 + (kt)) * 1024, 0))
#pragma unroll
    for (int kt = 0; kt < 6; ++kt) { K1_LX(0, kt); SB(); K1_LX(1, kt); SB(); }
    f32x4 acc[2][5];
#pragma unroll
    for (int f = 0; f < 5; ++f) { acc[0][f] = acc[1][f] = (f32x4){0.f, 0.f, 0.f, 0.f}; PINV(acc[0][f]); PINV(acc[1][f]); }
    u32 wb0 = lds + lane * 16, wb1 = wb0 + 61440, wb2 = wb0 + 92160;
    PINV(wb0); PINV(wb1); PINV(wb2);
#define K1_W1(kt, f) ((kt) < 12 ? LDSR(wb0, ((kt) * 5 + (f)) * 1024) : LDSR(wb1, (((kt) - 12) * 5 + (f)) * 1024))
    BAR();
    lds_wait(FL, 1u);
    STAMP(layer * 4 + 0, 1);
    bf16x8 wf[2][5];
#pragma unroll
    for (int f = 0; f < 5; ++f) { wf[0][f] = K1_W1(0, f); SB(); }
#pragma unroll
    for (int f = 0; f < 5; ++f) { wf[1][f] = K1_W1(1, f); SB(); }
    u32 fl = 0;
#pragma unroll
    for (int kt = 0; kt < 18; ++kt) {
        if ((kt & 1) == 0 && kt + 2 < 18) { if (__builtin_expect(ANY(fl < (u32)(kt / 2 + 2)), 0)) lds_wait(FL, kt / 2 + 2); FENCE(); }
#pragma unroll
        for (int f = 0; f < 5; ++f) {
            MFMA(acc[0][f], xa[0][kt], wf[kt & 1][f]);
            if (f == 1 && kt + 6 < 18) K1_LX(0, kt + 6);
            if (f == 3 && kt + 6 < 18) K1_LX(1, kt + 6);
            if (f == 0 && (kt & 1) && kt < 16) { FENCE(); fl = ldsw(FL); FENCE(); }
            SB();
            MFMA(acc[1][f], xa[1][kt], wf[kt & 1][f]);
            if (kt + 2 < 18) wf[kt & 1][f] = K1_W1(kt + 2, f);
            SB();
        }
    }
    asm volatile("s_nop 15" ::: "memory");
    STAMP(layer * 4 + 0, 2);
    lds_wait(FL + 4 + (lane % 3) * 4, 1u);
    {
        u32 op[5][4], rk[4];
        float cs1[5];
#pragma unroll
        for (int f = 0; f < 5; ++f) cs1[f] = *(LAS float*)(size_t)(lds + K1_CS + f * 64 + c * 4);
#pragma unroll
        for (int m = 0; m < 2; ++m) {
            const f32x4 sa = *(LAS f32x4*)(size_t)(lds + K1_ST + (32 * wid + 16 * m + 4 * g) * 8);
            const f32x4 sb = *(LAS f32x4*)(size_t)(lds + K1_ST + (32 * wid + 16 * m + 4 * g) * 8 + 16);
            const float rr[4] = {sa[0], sa[2], sb[0], sb[2]}, mm[4] = {sa[1], sa[3], sb[1], sb[3]};
#pragma unroll
            for (int f = 0; f < 5; ++f) {
                float v[4];
#pragma unroll
                for (int r = 0; r < 4; ++r) v[r] = rr[r] * acc[m][f][r] + mm[r] * cs1[f];
                if (f < 2) {
#pragma unroll
                    for (int r = 0; r < 4; ++r) v[r] = phi(v[r]);
                }
                if (f == 4) {
                    rk[2 * m] = pk2(phi(v[0]), phi(v[1])); rk[2 * m + 1] = pk2(phi(v[2]), phi(v[3]));
#pragma unroll
                    for (int r = 0; r < 4; ++r) v[r] = c >= 12 ? 1.f : v[r];
                }
                op[f][2 * m] = pk2(v[0], v[1]); op[f][2 * m + 1] = pk2(v[2], v[3]);
            }
        }
        const bf16x8 KAa = mk8(op[0][0], op[0][1], op[0][2], op[0][3]), KAb = mk8(op[1][0], op[1][1], op[1][2], op[1][3]);
        const bf16x8 VBa = mk8(op[2][0], op[2][1], op[2][2], op[2][3]), VBb = mk8(op[3][0], op[3][1], op[3][2], op[3][3]);
        const bf16x8 RV = mk8(op[4][0], op[4][1], op[4][2], op[4][3]), RK = mk8(rk[0], rk[1], rk[2], rk[3]);
        const f32x4 z4 = {0.f, 0.f, 0.f, 0.f};
        const f32x4 T1a = MFMAB(KAa, VBa, z4), T1b = MFMAB(KAb, VBb, z4), T2a = MFMAB(KAa, RV, z4), T2b = MFMAB(KAb, RV, z4);
        const f32x4 T3a = MFMAB(RK, VBa, z4), T3b = MFMAB(RK, VBb, z4), T4 = MFMAB(RK, RV, z4);
        const u32 pw = lds + K1_PW + wid * 2816;
        *(LAS f32x4*)(size_t)(pw + lane * 16) = T1a;
        *(LAS f32x4*)(size_t)(pw + 1024 + lane * 16) = T1b;
        if (g == 0) { f32x4 u = {T3a[0], T3a[1], T3b[2], T3b[3]}; *(LAS f32x4*)(size_t)(pw + (128 + c) * 16) = u; }
        if (c == 4 || c == 5 || c == 12) *(LAS f32x4*)(size_t)(pw + (144 + (c == 12 ? 2 : c - 4) * 4 + g) * 16) = T2a;
        if (c == 6 || c == 7 || c == 12) *(LAS f32x4*)(size_t)(pw + (156 + (c == 12 ? 2 : c - 6) * 4 + g) * 16) = T2b;
        if (g == 0 && ((c >= 4 && c < 8) || c == 12)) *(LAS f32x4*)(size_t)(pw + (168 + (c == 12 ? 4 : c - 4)) * 16) = T4;
        WAIT_L0();
        stsw(FL + 16 + wid * 4, 1u);
    }
    STAMP(layer * 4 + 0, 3);
    f32x4 qa[2][3];
#pragma unroll
    for (int f = 0; f < 3; ++f) { qa[0][f] = qa[1][f] = (f32x4){0.f, 0.f, 0.f, 0.f}; PINV(qa[0][f]); PINV(qa[1][f]); }
#define K1_W2(kt, f) ((f) < 2 ? LDSR(wb2, ((kt) * 2 + (f)) * 1024) : K1_W1(kt, 4))
    lds_wait(FL, 10u);
    bf16x8 wq[3][3];
#pragma unroll
    for (int k = 0; k < 3; ++k)
#pragma unroll
        for (int f = 0; f < 3; ++f) { wq[k][f] = K1_W2(k, f); SB(); }
#pragma unroll
    for (int kt = 0; kt < 18; ++kt) {
        if (kt == 3) lds_wait(FL, 11u);
        if (kt == 9) lds_wait(FL, 12u);
#pragma unroll
        for (int f = 0; f < 3; ++f) {
            MFMA(qa[0][f], wq[kt % 3][f], xa[0][kt]);
            SB();
            MFMA(qa[1][f], wq[kt % 3][f], xa[1][kt]);
            if (kt + 3 < 18) wq[kt % 3][f] = K1_W2(kt + 3, f);
            SB();
        }
    }
    asm volatile("s_nop 15" ::: "memory");
    STAMP(layer * 4 + 0, 4);
    f32x4 cs2[3];
#pragma unroll
    for (int f = 0; f < 3; ++f) cs2[f] = *(LAS f32x4*)(size_t)(lds + K1_CS + (f < 2 ? 5 + f : 4) * 64 + g * 16);
    bf16x8 PQ[2][2];
#pragma unroll
    for (int m = 0; m < 2; ++m) {
        const f32x2 st = *(LAS f32x2*)(size_t)(lds + K1_ST + (32 * wid + 16 * m + c) * 8);
        u32 w2[3][2];
#pragma unroll
        for (int f = 0; f < 3; ++f) {
            float v[4];
#pragma unroll
            for (int r = 0; r < 4; ++r) v[r] = phi(st[0] * qa[m][f][r] + st[1] * cs2[f][r]);
            w2[f][0] = pk2(v[0], v[1]); w2[f][1] = pk2(v[2], v[3]);
        }
        if (g != 2) { w2[2][0] = 0u; w2[2][1] = 0u; }
        PQ[m][0] = mk8(w2[0][0], w2[0][1], w2[2][0], w2[2][1]);
        PQ[m][1] = mk8(w2[1][0], w2[1][1], w2[2][0], w2[2][1]);
    }
    lds_wait(FL + 32 + (lane % 3) * 4, 1u);
    STAMP(layer * 4 + 0, 5);
    {
        const u32 OP = lds + K1_OP + lane * 16;
        const bf16x8 A1a = LDSR(OP, 0), A1b = LDSR(OP, 1024), A2a = LDSR(OP, 2048), A2b = LDSR(OP, 3072);
        const f32x4 z4 = {0.f, 0.f, 0.f, 0.f};
#pragma unroll
        for (int m = 0; m < 2; ++m) {
            const f32x4 O1a = MFMAB(A1a, PQ[m][0], z4), O1b = MFMAB(A1b, PQ[m][1], z4);
            f32x4 O2 = MFMAB(A2a, PQ[m][0], z4);
            O2 = MFMAB(A2b, PQ[m][1], O2);
            const float ia = __builtin_amdgcn_rcpf(O2[2] + 1e-6f), ib = __builtin_amdgcn_rcpf(O2[3] + 1e-6f);
            u32x4 o = {pk2(O1a[0] * ia, O1a[1] * ia), pk2(O1a[2] * ia, O1a[3] * ia), pk2(O1b[0] * ib, O1b[1] * ib), pk2(O1b[2] * ib, O1b[3] * ib)};
            char* ob = ws + WS_O + LSH * layer * XB;
            *(u32x4*)(ob + ((8 * pm + 2 * wid + m) * 16 + p) * 1024 + lane * 16) = o;
            const float ir = g == 0 ? ia : ib;
            if (g < 2) *(u32*)(ob + 2097152 + (p * 2048 + 128 * pm + 32 * wid + 16 * m + c) * 8 + g * 4) = pk2(O2[0] * ir, O2[1] * ir);
        }
    }
    STAMP(layer * 4 + 0, 6);
    STAMP_OUT(layer * 4 + 0);
}

constexpr u32 K2_LDS = 96 * 1024;
template <int KIND>
__global__ __launch_bounds__(256, 2) void k2_proj(char* __restrict__ ws, u16* __restrict__ out, int layer) {
    extern __shared__ __attribute__((aligned(16))) char shm_[];
    const u32 lds = (u32)(size_t)(LAS char*)shm_;
    const int tid = threadIdx.x, wid = __builtin_amdgcn_readfirstlane(tid >> 6), lane = tid & 63, c = lane & 15, g = lane >> 4;
    const int xcd = blockIdx.x & 7, bi = blockIdx.x >> 3;
    const int mt = (xcd & 3) * 8 + (bi & 7), nt = (xcd >> 2) * 4 + (bi >> 3);
    constexpr int NSL = KIND == 0 ? 5 : 18, RD = 4;
    constexpr int SK = KIND == 0 ? 1 : 3;
    STAMP_DECL;
    STAMP(layer * 4 + SK, 0);
    const auto rs = __builtin_amdgcn_make_buffer_rsrc(ws, 0, WS_END, 0x00020000);
    const u32 ab = KIND == 0 ? WS_O + LSH * layer * XB + (4 * mt * 16 + 4 * wid) * 1024 : WS_H + LSH * layer * ZB + (4 * mt * 8 + 2 * wid) * 9216;
    constexpr int AST = KIND == 0 ? 16384 : 73728;
    const u32 wb = KIND == 0 ? WS_WO + (nt * 18 + 4 * wid) * 4608 : WS_WOUT + (nt * 72 + 18 * wid) * 4608;
    const u32 vo = lane * 16, vo8 = KIND == 0 ? g * 32768 + c * 8 : lane * 8, vo4 = 4096 + g * 128 + (lane & 7) * 16;
    bf16x8 fa[RD][4], fw[RD][5];
    f32x4 acc[4][5];
#pragma unroll
    for (int m = 0; m < 4; ++m)
#pragma unroll
        for (int n = 0; n < 5; ++n) acc[m][n] = (f32x4){0.f, 0.f, 0.f, 0.f};
    u32 abm[4];
#pragma unroll
    for (int m = 0; m < 4; ++m) abm[m] = ab + (m ^ wid) * AST;
#define K2_B128(v, sof) __builtin_bit_cast(bf16x8, __builtin_amdgcn_raw_buffer_load_b128(rs, v, sof, 0))
#define K2_LOADW(s, wo_) do { _Pragma("unroll") for (int n = 0; n < 4; ++n) fw[(s) % RD][n] = K2_B128(vo + n * 1024, wo_); \
        fw[(s) % RD][4] = K2_B128(vo4, wo_); } while (0)
#define K2_SPLIT(s, m, so_, d_) do { const u32x2 h0 = __builtin_amdgcn_raw_buffer_load_b64(rs, vo8, so_, 0);          \
        const u32x2 h1 = __builtin_amdgcn_raw_buffer_load_b64(rs, vo8, (so_) + (d_), 0);                             \
        fa[(s) % RD][m] = mk8(h0[0], h0[1], h1[0], h1[1]); } while (0)
#define K2_LOAD(s) do { K2_LOADW(s, wb + (s) * 4608);                                                                \
        _Pragma("unroll") for (int m = 0; m < 4; ++m) {                                                              \
            if (KIND != 0 && (s) % 9 == 8) K2_SPLIT(s, m, abm[m] + (s) * 1024, 512);                                 \
            else fa[(s) % RD][m] = K2_B128(vo + ((s) & 3) * 1024, abm[m] + ((s) & ~3) * 1024);                       \
        } SB(); } while (0)
#define K2_MM(s) do { _Pragma("unroll") for (int m = 0; m < 4; ++m) _Pragma("unroll") for (int n = 0; n < 5; ++n)    \
        acc[m][n] = MFMAB(fw[(s) % RD][n], fa[(s) % RD][m], acc[m][n]); SB(); } while (0)
#pragma unroll
    for (int s = 0; s < RD; ++s) K2_LOAD(s);
    u32x4 res[2]; u32x2 res2;
    const int ver = 2 * layer + (KIND != 0);
    const u32 xoff = WS_X + ver * XB + (4 * mt + wid) * 18432;
    const u32 xa0 = (9 * nt + g) * 256 + c * 16, xa2 = (9 * nt + 8) * 256 + c * 16 + (g & 1) * 8;
#define K2_RES() do { res[0] = __builtin_amdgcn_raw_buffer_load_b128(rs, xa0, xoff, 0); res[1] = __builtin_amdgcn_raw_buffer_load_b128(rs, xa0 + 1024, xoff, 0); \
        res2 = __builtin_amdgcn_raw_buffer_load_b64(rs, xa2, xoff, 0); SB(); } while (0)
    if (KIND == 0) {
        K2_MM(0);
        if (wid < 2) {
            K2_LOADW(4, WS_WO + (nt * 18 + 16 + wid) * 4608);
#pragma unroll
            for (int m = 0; m < 4; ++m) K2_SPLIT(4, m, WS_O + LSH * layer * XB + 2097152 + (wid * 8 * 2048 + 64 * mt + 16 * (m ^ wid)) * 8, 16384);
            SB();
        }
        K2_RES();
        K2_MM(1); K2_MM(2); K2_MM(3);
        if (wid < 2) K2_MM(4);
    } else {
#pragma unroll
        for (int s = 0; s < NSL; ++s) {
            K2_MM(s);
            if (s + RD < NSL) K2_LOAD(s + RD);
            if (s + RD == NSL) K2_RES();
        }
    }
    STAMP(layer * 4 + SK, 1);
#pragma unroll
    for (int m = 1; m < 4; ++m)
#pragma unroll
        for (int n = 0; n < 5; ++n) *(LAS f32x4*)(size_t)(lds + (((m ^ wid) * 3 + m - 1) * 5 + n) * 1024 + vo) = acc[m][n];
    __syncthreads();
#pragma unroll
    for (int n = 0; n < 5; ++n)
        acc[0][n] = (acc[0][n] + *(LAS f32x4*)(size_t)(lds + (wid * 15 + n) * 1024 + vo)) +
                    (*(LAS f32x4*)(size_t)(lds + (wid * 15 + 5 + n) * 1024 + vo) + *(LAS f32x4*)(size_t)(lds + (wid * 15 + 10 + n) * 1024 + vo));
    u32x4 o[2]; u32x2 o2;
#pragma unroll
    for (int i = 0; i < 2; ++i) {
        const f32x4 a = acc[0][2 * i], b = acc[0][2 * i + 1];
        o[i] = (u32x4){pk2(a[0] + bflo(res[i][0]), a[1] + bfhi(res[i][0])), pk2(a[2] + bflo(res[i][1]), a[3] + bfhi(res[i][1])),
                       pk2(b[0] + bflo(res[i][2]), b[1] + bfhi(res[i][2])), pk2(b[2] + bflo(res[i][3]), b[3] + bfhi(res[i][3]))};
    }
    o2 = (u32x2){pk2(acc[0][4][0] + bflo(res2[0]), acc[0][4][1] + bfhi(res2[0])), pk2(acc[0][4][2] + bflo(res2[1]), acc[0][4][3] + bfhi(res2[1]))};
    if (KIND == 2) {
        u16* op = out + (size_t)(64 * mt + 16 * wid + c) * 576 + 72 * nt;
        *(u32x4*)(op + 8 * g) = o[0];
        *(u32x4*)(op + 32 + 8 * g) = o[1];
        if (g < 2) *(u32x2*)(op + 64 + 4 * g) = o2;
    } else {
        char* xp = ws + xoff + XB;
        *(u32x4*)(xp + xa0) = o[0];
        *(u32x4*)(xp + xa0 + 1024) = o[1];
        if (g < 2) *(u32x2*)(xp + xa2) = o2; else o2 = (u32x2){0u, 0u};
        float s1 = 0.f, s2 = 0.f;
#pragma unroll
        for (int k = 0; k < 10; ++k) { const u32 wv = k < 8 ? o[k >> 2][k & 3] : o2[k & 1]; const float u = bflo(wv), v = bfhi(wv); s1 += u + v; s2 += u * u + v * v; }
        auto r1 = __builtin_amdgcn_permlane32_swap(F2U(s1), F2U(s2), false, false);
        const float t = U2F(r1[0]) + U2F(r1[1]);
        auto r2 = __builtin_amdgcn_permlane16_swap(F2U(t), F2U(t), false, false);
        const float tt = U2F(r2[0]) + U2F(r2[1]);
        if ((g & 1) == 0) *(float*)(ws + WS_XS + (ver + 1) * XSB + (nt * 2048 + 64 * mt + 16 * wid + c) * 8 + (g >> 1) * 4) = tt;
    }
    STAMP(layer * 4 + SK, 2);
    STAMP_OUT(layer * 4 + SK);
}

constexpr u32 STAGE = 18432, NS = 6;
constexpr u32 K3_PK = NS * STAGE, K3_PP = 304, K3_WS = K3_PK + 128 * K3_PP, K3_S1 = K3_WS + 4096, K3_S2 = K3_S1 + 2048, K3_CS = K3_S2 + 1024, K3_FL = K3_CS + 1280, K3_LDS = K3_FL + 128;

__global__ __launch_bounds__(512, 2) void k34_mlp(char* __restrict__ ws, const u16* __restrict__ wsp, const u16* __restrict__ bsp, u16* __restrict__ out, int layer) {
    extern __shared__ __attribute__((aligned(16))) char shm_[];
    const u32 lds = (u32)(size_t)(LAS char*)shm_;
    const int tid = threadIdx.x, wid = __builtin_amdgcn_readfirstlane(tid >> 6), lane = tid & 63, c = lane & 15, g = lane >> 4;
    const int xcd = blockIdx.x & 7, bi = blockIdx.x >> 3;
    const int pm = MAP3 ? (xcd & 3) * 4 + (bi & 3) : (xcd & 1) * 8 + (bi & 7), pn = MAP3 ? (xcd >> 2) * 8 + (bi >> 2) : (xcd >> 1) * 4 + (bi >> 3);
    const u32 FL = lds + K3_FL;
    STAMP_DECL;
    const auto rs = __builtin_amdgcn_make_buffer_rsrc(ws, 0, WS_END, 0x00020000);
    if (wid == 4) {
        u32 ao[16];
        const u32 vo = lane * 16;
#pragma unroll
        for (int q = 0; q < 16; ++q) { ao[q] = vo + (q >> 1) * 18432 - (q & 2) * 1024 + 4096; PINV(ao[q]); }
        const char* ap = ws + WS_X + (2 * layer + 1) * XB + 8 * pm * 18432 - 4096;
        const char* xp = ws + WS_WIN + (144 + 9 * pn + 8) * 18432;
        u32 slot = 0;
#define K3_ISSUE() do { const u32 dst = lds + slot * STAGE;                                                          \
            DMA4(ao[0], ao[1], ao[2], ao[3], ap, dst); DMA4(ao[4], ao[5], ao[6], ao[7], ap, dst + 4096);             \
            DMA4(ao[8], ao[9], ao[10], ao[11], ap, dst + 8192); DMA4(ao[12], ao[13], ao[14], ao[15], ap, dst + 12288); } while (0)
#define K3_SEG(s0, s1, GATE) for (int s = (s0); s < (s1) + 3; ++s) {                                                 \
            if (s >= (s0) + 3) {                                                                                     \
                if (s <= (s1)) WAIT_V(36); else if (s == (s1) + 1) WAIT_V(18); else WAIT_V(0);                       \
                stsw(FL, s - 2); FENCE(); }                                                                          \
            if (s == 1) BAR();                                                                                       \
            if (s == 2) { WAIT_V(18); stsw(FL, 1u); FENCE(); }                                                       \
            if (s < (s1)) {                                                                                          \
                if (s >= (int)NS) lds_wait_sleep(FL + 16 + (lane & 3) * 4, GATE);                                    \
                if (s == 9) { ap -= 9 * 2048; xp = ws + WS_WIN + (9 * pn + 8) * 18432; }                             \
                K3_ISSUE();                                                                                          \
                DMA2(vo, vo, xp, lds + slot * STAGE + 16384);                                                        \
                xp += 2048; ap += 2048;                                                                              \
                slot = slot + 1 == NS ? 0 : slot + 1; } }
        K3_SEG(0, ZSPLIT, s - NS + 1);
        K3_SEG(ZSPLIT, 18, s >= 15 ? LGATE : max(ZGATE, s - (int)NS + 1));
#pragma unroll
        for (int q = 0; q < 16; ++q) { ao[q] = (q * 8 + (lane >> 3)) * 4096 + (((lane & 7) ^ ((q & 1) * 4 + g)) * 16) - (q & 3) * 1024 + 4096; PINV(ao[q]); }
        ap = (const char*)wsp + (size_t)pm * 128 * 4096 - 4096;
        lds_wait_sleep(FL + 16 + (lane & 3) * 4, LGATE2);
        for (int s = 18; s < 53; ++s) {
            if (s >= 21) {
                if (s <= 50) WAIT_V(32); else if (s == 51) WAIT_V(16); else WAIT_V(0);
                stsw(FL, s - 2);
                FENCE();
            }
            if (s < 50) {
                lds_wait_sleep(FL + 16 + (lane & 3) * 4, s - NS + 1);
                K3_ISSUE();
                ap += AFAKE ? 0 : (s == 33 ? 128 - 15 * 256 : 256);
                slot = slot + 1 == NS ? 0 : slot + 1;
            }
        }
        lds_wait_sleep(FL + 80, 2u);
        for (int i = 0; i < 8; ++i) {
            const int r = i < 2 ? 6 + i : (i < 6 ? i : i - 6);
            if (i >= 2) lds_wait_sleep(FL + 16 + (lane & 3) * 4, 43 + i);
            if (WGATE && i == WGI) lds_wait_sleep(FL + 92, WGATE);
            const char* wp = ws + WS_WOUT + ((MAP5 ? pn & 7 : pn >> 1) * 72 + 18 * (r >> 1) + 4 * (r & 1)) * 4608;
            const u32 dst = lds + r * STAGE;
            DMA4(vo, vo, vo, vo, wp, dst); DMA4(vo, vo, vo, vo, wp + 4096, dst + 4096); DMA4(vo, vo, vo, vo, wp + 8192, dst + 8192);
            DMA4(vo, vo, vo, vo, wp + 12288, dst + 12288); DMA2(vo, vo, wp + 16384, dst + 16384);
            if (i >= 2) WAIT_V(36);
        }
        WAIT_V(0);
        stsw(FL, 100u);
        return;
    }
    if (wid > 4) {
        const int ht = tid - 320;
        if (ht >= 128 && ht < 160) stsw(FL + (ht - 128) * 4, ht == 129 ? 18u : 0u);
        WAIT_L0();
        BAR();
        if (wid != 7) {
            float mean, var;
            x_stats(ws, 2 * layer + 1, 128 * pm + ht, mean, var);
            const float r = __builtin_amdgcn_rsqf(var + 1e-5f);
            f32x4 o = {-mean, -2.302208198f * r, -0.1029432397f * r * r * r, (var + 1e-5f) * r};
            *(LAS f32x4*)(size_t)(lds + K3_S1 + ht * 16) = o;
        } else {
            const int i = min(lane, 35);
            const f32x4* cp = (const f32x4*)(ws + WS_CSIN) + 36 * pn + i;
            const f32x4 a0 = cp[576], a1 = cp[576 + 1152], b0 = cp[0], b1 = cp[1152];
            *(LAS f32x4*)(size_t)(lds + K3_CS + i * 16) = a0 + a1;
            *(LAS f32x4*)(size_t)(lds + K3_CS + 640 + i * 16) = b0 + b1;
        }
        FENCE();
        stsw(FL + 32 + (wid - 5) * 4, 1u);
        u32 vo = lane * 16;
        PINV(vo);
        GAS u32* flg = (GAS u32*)(ws + WS_CNT + (layer * 16 + pn) * 4096);
        HST_DECL;
        if (wid != 5) {
            const int h = wid - 6;
            u32 ta = lds + K3_PK + (32 * h + 8 * g + (c >> 2)) * K3_PP + 16 * (c & 3), tx = ta + 256 - 8 * (c & 3), s2r = lds + K3_S2 + 128 * h + 32 * g;
            PINV(ta); PINV(tx); PINV(s2r);
            const u32 zob = WS_Z + LSH * layer * ZB + (9 * pn * 64 + 2 * pm + h) * 1024;
            lds_wait_sleep(FL + 48 + (lane & 3) * 4, 1u);
            HST(0);
            if (pm < HPRIO) __builtin_amdgcn_s_setprio(3);
            float zf[18][8];
#define K3_TR(fi, A, O) do { u32x2 t0_, t1_, t2_, t3_;                                                               \
                asm volatile("ds_read_b64_tr_b16 %0, %4 offset:%5\n\tds_read_b64_tr_b16 %1, %4 offset:%6\n\tds_read_b64_tr_b16 %2, %4 offset:%7\n\t" \
                             "ds_read_b64_tr_b16 %3, %4 offset:%8\n\ts_waitcnt lgkmcnt(0)" : "=&v"(t0_), "=&v"(t1_), "=&v"(t2_), "=&v"(t3_) \
                             : "v"(A), "i"(O), "i"((O) + 4 * K3_PP), "i"((O) + 64 * K3_PP), "i"((O) + 68 * K3_PP) : "memory"); \
                const u32 w_[8] = {t0_[0], t0_[1], t1_[0], t1_[1], t2_[0], t2_[1], t3_[0], t3_[1]};                  \
                _Pragma("unroll") for (int i_ = 0; i_ < 8; ++i_) { zf[2 * (fi) + (i_ >> 2)][2 * (i_ & 3)] = bflo(w_[i_]); zf[2 * (fi) + (i_ >> 2)][2 * (i_ & 3) + 1] = bfhi(w_[i_]); \
                    PINV(zf[2 * (fi) + (i_ >> 2)][2 * (i_ & 3)]); PINV(zf[2 * (fi) + (i_ >> 2)][2 * (i_ & 3) + 1]); } } while (0)
            K3_TR(0, ta, 0); K3_TR(1, ta, 8); K3_TR(2, ta, 64); K3_TR(3, ta, 72); K3_TR(4, ta, 128); K3_TR(5, ta, 136); K3_TR(6, ta, 192); K3_TR(7, ta, 200);
            K3_TR(8, tx, 0);
            HST(1);
            lds_wait_sleep(FL + 64, 1u);
            for (int i = 2; i < pm; ++i) __builtin_amdgcn_s_sleep(STAG);
            HST(2);
            f32x4 sv[2][4];
#pragma unroll
            for (int q = 0; q < 2; ++q)
#pragma unroll
                for (int k = 0; k < 4; ++k) sv[q][k] = *(LAS f32x4*)(size_t)(s2r + (k >> 1) * 512 + 256 * q + (k & 1) * 16);
#pragma unroll
            for (int ii = 0; ii < 18; ++ii) {
                const int q = ii / 9, i = 2 * (ii % 9) + q;
                u32x4 o;
                asm("v_fma_f32 %4, %4, %12, %20\n\tv_fma_f32 %5, %5, %13, %21\n\tv_fma_f32 %6, %6, %14, %22\n\tv_fma_f32 %7, %7, %15, %23\n\t"
                    "v_fma_f32 %8, %8, %16, %24\n\tv_fma_f32 %9, %9, %17, %25\n\tv_fma_f32 %10, %10, %18, %26\n\tv_fma_f32 %11, %11, %19, %27\n\t"
                    "v_cvt_pk_bf16_f32 %0, %4, %5\n\tv_cvt_pk_bf16_f32 %1, %6, %7\n\tv_cvt_pk_bf16_f32 %2, %8, %9\n\tv_cvt_pk_bf16_f32 %3, %10, %11"
                    : "=&v"(o[0]), "=&v"(o[1]), "=&v"(o[2]), "=&v"(o[3]), "+v"(zf[i][0]), "+v"(zf[i][1]), "+v"(zf[i][2]), "+v"(zf[i][3]), "+v"(zf[i][4]), "+v"(zf[i][5]), "+v"(zf[i][6]), "+v"(zf[i][7])
                    : "v"(sv[q][0][0]), "v"(sv[q][0][1]), "v"(sv[q][0][2]), "v"(sv[q][0][3]), "v"(sv[q][1][0]), "v"(sv[q][1][1]), "v"(sv[q][1][2]), "v"(sv[q][1][3]),
                      "v"(sv[q][2][0]), "v"(sv[q][2][1]), "v"(sv[q][2][2]), "v"(sv[q][2][3]), "v"(sv[q][3][0]), "v"(sv[q][3][1]), "v"(sv[q][3][2]), "v"(sv[q][3][3]));
                __builtin_amdgcn_raw_buffer_store_b128(o, rs, vo, zob + q * 32768 + (i >> 1) * 65536, 16);
                SB();
                if (ii % 9 == 8) {
                    if (q == 0) HST(3);
                    WAIT_V(0);
                    if (q == 0) HST(4);
                    u32 old = 0;
                    if (lane == 0) old = __hip_atomic_fetch_add((LAS u32*)(size_t)(FL + 76 + 4 * q), 1u, __ATOMIC_RELAXED, __HIP_MEMORY_SCOPE_WORKGROUP);
                    if (__builtin_amdgcn_readfirstlane(old) == 1 && lane < 16)
                        __hip_atomic_fetch_add(flg + lane * 64, 1u << (16 * q + pm), RLX_AGENT);
                    if (q == 0) { __builtin_amdgcn_s_setprio(0); __builtin_amdgcn_s_sleep(STAG2); }
                    SB();
                }
            }
            HST(5);
            if (wid == 6) HST_OUT(9 + layer);
            return;
        }
        u32 wsa = lds + K3_WS + lane * 16, s2w = lds + K3_S2 + lane * 8;
        PINV(wsa); PINV(s2w);
        const u32 zsb = WS_ZST + (layer * 16 + pm) * 16384;
        lds_wait_sleep(FL + 16 + (lane & 3) * 4, 9u);
        __builtin_amdgcn_s_sleep(POLLD);
        __builtin_amdgcn_s_setprio(3);
        lds_wait(FL + 48 + (lane & 3) * 4, 1u);
        STAMP(layer * 4 + 2, 8);
        {
            const f32x4 st = sadd4(sadd4(*(LAS f32x4*)(size_t)wsa, *(LAS f32x4*)(size_t)(wsa + 1024)), sadd4(*(LAS f32x4*)(size_t)(wsa + 2048), *(LAS f32x4*)(size_t)(wsa + 3072)));
            u32x4 o = __builtin_bit_cast(u32x4, st);
#pragma unroll
            for (int k = 0; k < 4; ++k) o[k] = o[k] == SENT ? 0x7fc00000u : o[k];
            BST128(o, rs, vo, zsb + pn * 1024, 16);
            __builtin_amdgcn_s_sleep(XDELAY3);
            u32x4 t[16];
#pragma unroll
            for (int k = 0; k < 16; ++k) t[k] = __builtin_amdgcn_raw_buffer_load_b128(rs, vo + (k & 3) * 1024, zsb + (k >> 2) * 4096, 16);
            u32 mx = 0;
#pragma unroll
            for (int k = 0; k < 16; ++k) mx = max(max(mx, max(t[k][0], t[k][1])), max(t[k][2], t[k][3]));
            u32 spins = 0;
            if (__builtin_expect(ANY(mx == SENT), 0)) {
#pragma nounroll
                for (spins = 1; spins < SPIN_CAP; ++spins) {
                    FENCE();
                    u32 miss = 0;
#pragma unroll
                    for (int k = 0; k < 16; ++k) miss |= ANY(max(max(t[k][0], t[k][1]), max(t[k][2], t[k][3])) == SENT) ? 1u << k : 0u;
                    if (!miss) break;
#pragma unroll
                    for (int k = 0; k < 16; ++k) if (miss >> k & 1) t[k] = __builtin_amdgcn_raw_buffer_load_b128(rs, vo + (k & 3) * 1024, zsb + (k >> 2) * 4096, 16);
                }
            }
            f32x4 sa[4];
#pragma unroll
            for (int k = 0; k < 4; ++k)
                sa[k] = sadd4(sadd4(__builtin_bit_cast(f32x4, t[4 * k]), __builtin_bit_cast(f32x4, t[4 * k + 1])), sadd4(__builtin_bit_cast(f32x4, t[4 * k + 2]), __builtin_bit_cast(f32x4, t[4 * k + 3])));
            const f32x4 ss = sadd4(sadd4(sa[0], sa[1]), sadd4(sa[2], sa[3]));
            f32x2 rr, mr;
#pragma unroll
            for (int k = 0; k < 2; ++k) {
                const float m2 = sfma(ss[2 * k], 1.f / 2304.f, 0.f);
                float v2 = fmaxf(sfma(-m2, m2, sfma(ss[2 * k + 1], 1.f / 2304.f, 0.f)), 0.f) + 1e-5f;
                PINV(v2);
                rr[k] = __builtin_amdgcn_rsqf(v2);
                float t2 = -m2 * rr[k];
                PINV(t2);
                mr[k] = t2;
            }
            *(LAS f32x2*)(size_t)s2w = rr;
            *(LAS f32x2*)(size_t)(s2w + 512) = mr;
            FENCE();
            stsw(FL + 64, 1u);
            FENCE();
        }
        STAMP(layer * 4 + 2, 9);
        STAMP(layer * 4 + 2, 10);
        u32 ready = 0;
#define K3_NEED(n) do { _Pragma("nounroll") for (u32 spins_ = 0; ready < (u32)(n) && spins_ < SPIN_CAP; ++spins_) {   \
            const u32 v_ = __builtin_amdgcn_readfirstlane(__hip_atomic_load(flg + pm * 64, RLX_AGENT));                                               \
            ready = __builtin_ctzll(~(u64)v_); } FENCE(); } while (0)
        __builtin_amdgcn_s_sleep(FDELAY);
        K3_NEED(3);
        stsw(FL + 84, 1u);
        FENCE();
        STAMP(layer * 4 + 2, 11);
        STAMP_OUT(layer * 4 + 2);
        __builtin_amdgcn_s_setprio(0);
        const u32 zx = WS_Z + LSH * layer * ZB + (9 * pn + 8) * 65536;
        u32x4 xr[4][2];
#pragma unroll
        for (int k = 0; k < 3; ++k) {
            xr[k][0] = __builtin_amdgcn_raw_buffer_load_b128(rs, vo, zx + k * 2048, 16);
            xr[k][1] = __builtin_amdgcn_raw_buffer_load_b128(rs, vo + 1024, zx + k * 2048, 16);
            SB();
        }
        u32 slot = 0;
        for (int s0 = 0; s0 < 32; s0 += 4) {
#pragma unroll
            for (int k = 0; k < 4; ++k) {
                const int s = s0 + k;
                lds_wait_sleep(FL + 16 + (lane & 3) * 4, 18 + s - NS + 1);
                *(LAS u32x4*)(size_t)(lds + slot * STAGE + 16384 + lane * 16) = xr[k][0];
                *(LAS u32x4*)(size_t)(lds + slot * STAGE + 17408 + lane * 16) = xr[k][1];
                FENCE();
                stsw(FL + 4, 18 + s + 1);
                FENCE();
                const u32 sn = s + 3 < 32 ? s + 3 : 31;
                K3_NEED(sn + 1);
                xr[(k + 3) & 3][0] = __builtin_amdgcn_raw_buffer_load_b128(rs, vo, zx + sn * 2048, 16);
                xr[(k + 3) & 3][1] = __builtin_amdgcn_raw_buffer_load_b128(rs, vo + 1024, zx + sn * 2048, 16);
                slot = slot + 1 == NS ? 0 : slot + 1;
            }
        }
        lds_wait_sleep(FL + 92, 4u);
        __builtin_amdgcn_s_sleep(HDELAY);
        GAS u32* hm = (GAS u32*)(ws + WS_CNT + (layer * 16 + pm) * 4096 + pn * 256 + 128);
#pragma nounroll
        for (u32 spins = 0; spins < SPIN_CAP; ++spins) {
            const u32 v = __builtin_amdgcn_readfirstlane(__hip_atomic_load(hm, RLX_AGENT));
            FENCE();
            stsw(FL + 88, v);
            if (v == 0xffffu) break;
        }
        return;
    }
    STAMP(layer * 4 + 2, 0);
    u32 vA = lane * 16, vB = lane * 16 + 18432;
    u32 so = WS_WIN + (144 + 9 * pn + 2 * wid) * 18432;
    bf16x8 bb[6][2];
#define BLD(voff, imm, aux) __builtin_bit_cast(bf16x8, __builtin_amdgcn_raw_buffer_load_b128(rs, (voff) + (imm), so, aux))
#define BPRO(aux) do { _Pragma("unroll") for (int r = 0; r < 5; ++r) { if (r == 3) so += 3072;                       \
        bb[r][0] = BLD(vA, (r % 3) * 1024, aux); SB(); bb[r][1] = BLD(vB, (r % 3) * 1024, aux); SB(); } } while (0)
    BPRO(0);
    u32 pa[2][4];
#pragma unroll
    for (int q = 0; q < 4; ++q) { pa[0][q] = lds + lane * 16 + ((q ^ wid) * 4096); pa[1][q] = pa[0][q] + 1024; }
    u32 px = lds + 16384 + lane * 16;
    u32 myfree = FL + 16 + wid * 4, fladdr = FL + (lane & 1) * 4, cnt = 0;
    PINV(cnt); PINV(myfree); PINV(fladdr);
    int dstep = 3 * STAGE;
    f32x4 acc[8][2], accx[2];
    bf16x8 a[8], xf;
#define ALOAD() do { a[0] = LDSR(pa[0][0], 0); SB(); xf = LDSR(px, 0); SB();                                         \
        _Pragma("unroll") for (int m = 1; m < 8; ++m) { a[m] = LDSR(pa[0][m >> 1], (m & 1) * 2048); SB(); } } while (0)
#define MFS(acc, x, y) do { MFMA(acc, x, y); SB(); } while (0)
#define HALF(r, RD, kn, sf, rl, imm, aux, E0, E1, E2, E3, E4, E5) do {                                               \
        WCNT(8, 5); MFS(acc[0][0], bb[r][0], a[0]); E0; SB();                                                       \
        MFS(acc[0][1], bb[r][1], a[0]); E1; SB();                                                                   \
        MFS(accx[0], xf, a[0]); if (RD) a[0] = LDSR(pa[kn][0], (sf)); SB();                                         \
        MFS(accx[1], xf, a[1]); if (RD) xf = LDSR(px, (sf) + (kn) * 1024); SB();                                    \
        MFS(acc[1][1], bb[r][1], a[1]); bb[rl][0] = BLD(vA, imm, aux); SB();                                        \
        MFS(acc[1][0], bb[r][0], a[1]); if (RD) a[1] = LDSR(pa[kn][0], (sf) + 2048); SB();                          \
        MFS(acc[2][0], bb[r][0], a[2]); E2; SB();                                                                   \
        MFS(acc[2][1], bb[r][1], a[2]); if (RD) a[2] = LDSR(pa[kn][1], (sf)); SB();                                 \
        WCNT(63, 5); MFS(acc[3][1], bb[r][1], a[3]); SB();                                                          \
        MFS(acc[3][0], bb[r][0], a[3]); if (RD) a[3] = LDSR(pa[kn][1], (sf) + 2048); SB();                          \
        MFS(acc[4][0], bb[r][0], a[4]); bb[rl][1] = BLD(vB, imm, aux); SB();                                        \
        MFS(acc[4][1], bb[r][1], a[4]); if (RD) a[4] = LDSR(pa[kn][2], (sf)); SB();                                 \
        MFS(acc[5][1], bb[r][1], a[5]); E3; SB();                                                                   \
        MFS(acc[5][0], bb[r][0], a[5]); if (RD) a[5] = LDSR(pa[kn][2], (sf) + 2048); SB();                          \
        MFS(acc[6][0], bb[r][0], a[6]); E4; SB();                                                                   \
        MFS(acc[6][1], bb[r][1], a[6]); if (RD) a[6] = LDSR(pa[kn][3], (sf)); SB();                                 \
        MFS(acc[7][1], bb[r][1], a[7]); E5; SB();                                                                   \
        MFS(acc[7][0], bb[r][0], a[7]); if (RD) a[7] = LDSR(pa[kn][3], (sf) + 2048); SB(); } while (0)
#define STEP(u, chk, nso, aux) do { u32 fl_; u64 msk_;                                                               \
        HALF(2 * (u), 1, 1, (u) * STAGE, (2 * (u) + 5) % 6, ((2 * (u) + 5) % 3) * 1024, aux,                         \
             { if ((u) == 2) so += 3072; }, { cnt += 1; PINV(cnt); }, { FENCE(); fl_ = ldsw(fladdr); FENCE(); },     \
             { if ((u) == 2) { pa[0][0] += dstep; pa[0][1] += dstep; PINV(pa[0][0]); PINV(pa[0][1]); } },            \
             { if ((u) == 2) { pa[0][2] += dstep; pa[0][3] += dstep; PINV(pa[0][2]); PINV(pa[0][3]); } },            \
             { asm volatile("v_cmp_le_u32 %0, %1, %2" : "=s"(msk_) : "v"(fl_), "v"(cnt)); if ((u) == 2) { px += dstep; PINV(px); } }); \
        HALF(2 * (u) + 1, 1, 0, (((u) + 1) % 3) * STAGE, (2 * (u) + 6) % 6, ((2 * (u) + 6) % 3) * 1024, aux,         \
             { if (__builtin_expect(msk_ != 0, 0)) { if (chk) lds_wait_gt(fladdr, cnt); } FENCE(); }, { if ((u) == 0) so = (nso); }, \
             { FENCE(); stsw(myfree, cnt); FENCE(); },                                                               \
             { if ((u) == 2) { pa[1][0] += dstep; pa[1][1] += dstep; PINV(pa[1][0]); PINV(pa[1][1]); } },            \
             { if ((u) == 2) { pa[1][2] += dstep; pa[1][3] += dstep; PINV(pa[1][2]); PINV(pa[1][3]); } },            \
             { if ((u) == 2) dstep = -dstep; }); } while (0)
#define PINACC() do { _Pragma("unroll") for (int m = 0; m < 8; ++m) { PINV(acc[m][0]); PINV(acc[m][1]); } PINV(accx[0]); PINV(accx[1]); asm volatile("s_nop 1"); } while (0)
    BAR();
    u32 yp[18][2], early = 0;
    const u32 s1a = lds + K3_S1 + c * 16;
#pragma unroll
    for (int ph = 0; ph < 2; ++ph) {
        if (ph == 0) {
#pragma unroll
            for (int m = 0; m < 8; ++m) { acc[m][0] = acc[m][1] = (f32x4){0.f, 0.f, 0.f, 0.f}; if (m < 2) accx[m] = acc[m][0]; }
        } else {
            const u32 ca = lds + K3_CS + ph * 640 + g * 16;
            const f32x4 c0 = *(LAS f32x4*)(size_t)(ca + wid * 128), c1 = *(LAS f32x4*)(size_t)(ca + wid * 128 + 64), c2 = *(LAS f32x4*)(size_t)(ca + 512);
#pragma unroll
            for (int m = 0; m < 8; ++m) {
                const float nm = *(LAS float*)(size_t)(s1a + ((((m >> 1) ^ wid) << 1) + (m & 1)) * 256);
                acc[m][0] = c0 * nm; acc[m][1] = c1 * nm;
                if (m < 2) accx[m] = c2 * nm;
            }
        }
        PINACC();
        if (ph == 0) { lds_wait(fladdr, 1u); ALOAD(); STAMP(layer * 4 + 2, 1); }
        const u32 nxt = WS_WIN + (9 * pn + 2 * wid) * 18432;
        for (int j = 0; j < 3; ++j) {
            STEP(0, true, j == 2 ? nxt : so + 3072, 0);
            STEP(1, true, 0, 0);
            STEP(2, ph == 0 || j < 2, 0, 0);
        }
        asm volatile("s_nop 15" ::: "memory");
        STAMP(layer * 4 + 2, 2 + 2 * ph);
        if (ph == 1) {
            vB = lane * 16 + 65536;
            so = WS_Z + LSH * layer * ZB + (9 * pn + 2 * wid) * 65536;
            FENCE();
            early = __builtin_amdgcn_readfirstlane(ldsw(FL + 84));
            FENCE();
            if (early) BPRO(16);
        }
        float t1[8], t2[8];
        f32x4 rcs[8], cz[3];
        if (ph == 0) {
            lds_wait(FL + 32 + (lane % 3) * 4, 1u);
            const u32 ca = lds + K3_CS + g * 16;
            cz[0] = *(LAS f32x4*)(size_t)(ca + wid * 128); cz[1] = *(LAS f32x4*)(size_t)(ca + wid * 128 + 64); cz[2] = *(LAS f32x4*)(size_t)(ca + 512);
        }
#pragma unroll
        for (int m = 0; m < 8; ++m) rcs[m] = *(LAS f32x4*)(size_t)(s1a + ((((m >> 1) ^ wid) << 1) + (m & 1)) * 256);
#pragma unroll
        for (int m = 0; m < 8; ++m) {
            if (ph == 1 && m == 4 && !early) { FENCE(); early = __builtin_amdgcn_readfirstlane(ldsw(FL + 84)); FENCE(); if (early) BPRO(16); }
            constexpr int NP = 6;
            const int np = m < 2 ? 6 : 4;
            const f32x2 k1 = {rcs[m][1], rcs[m][1]}, k3 = {rcs[m][2], rcs[m][2]}, sg = {rcs[m][3], rcs[m][3]};
            f32x2 v[NP], e[NP];
#pragma unroll
            for (int i = 0; i < NP; ++i) if (i < np) {
                const f32x4 av = i < 4 ? acc[m][i >> 1] : accx[m & 1];
                v[i] = (f32x2){av[2 * (i & 1)], av[2 * (i & 1) + 1]};
                if (ph == 0) v[i] += (f32x2){cz[i >> 1][2 * (i & 1)], cz[i >> 1][2 * (i & 1) + 1]} * (f32x2){rcs[m][0], rcs[m][0]};
            }
#pragma unroll
            for (int i = 0; i < NP; ++i) if (i < np) e[i] = v[i] * v[i];
#pragma unroll
            for (int i = 0; i < NP; ++i) if (i < np) e[i] = e[i] * k3 + k1;
#pragma unroll
            for (int i = 0; i < NP; ++i) if (i < np) e[i] = e[i] * v[i];
            SB();
#pragma unroll
            for (int i = 0; i < NP; ++i) if (i < np) { e[i][0] = __builtin_amdgcn_exp2f(e[i][0]); e[i][1] = __builtin_amdgcn_exp2f(e[i][1]); }
            SB();
#pragma unroll
            for (int i = 0; i < NP; ++i) if (i < np) e[i] = e[i] * sg + sg;
            SB();
#pragma unroll
            for (int i = 0; i < NP; ++i) if (i < np) { e[i][0] = __builtin_amdgcn_rcpf(e[i][0]); e[i][1] = __builtin_amdgcn_rcpf(e[i][1]); }
            SB();
#pragma unroll
            for (int i = 0; i < NP; ++i) if (i < np) v[i] = v[i] * e[i];
#pragma unroll
            for (int i = 0; i < NP; ++i) if (i < np) yp[i < 4 ? 2 * m + (i >> 1) : 16 + m][i & 1] = pk2(v[i][0], v[i][1]);
            if (ph == 0) {
                f32x2 u1 = (v[0] + v[1]) + (v[2] + v[3]), u2 = v[0] * v[0], u3 = v[1] * v[1];
                u2 = v[2] * v[2] + u2; u3 = v[3] * v[3] + u3;
                if (m < 2) { u1 += v[4] + v[5]; u2 = v[4] * v[4] + u2; u3 = v[5] * v[5] + u3; }
                u2 += u3;
                t1[m] = u1[0] + u1[1]; t2[m] = u2[0] + u2[1];
            }
            PINV(yp[2 * m][0]); PINV(yp[2 * m][1]); PINV(yp[2 * m + 1][0]); PINV(yp[2 * m + 1][1]);
            if (m < 2) { PINV(yp[16 + m][0]); PINV(yp[16 + m][1]); }
            if (ph == 0) {
                const u32 row = (((((m >> 1) ^ wid) << 1) + (m & 1)) << 4) + c;
                u32x4 o = {yp[2 * m][0], yp[2 * m][1], yp[2 * m + 1][0], yp[2 * m + 1][1]};
                *(LAS u32x4*)(size_t)(lds + K3_PK + row * K3_PP + 64 * wid + 16 * g) = o;
                if (m < 2) { u32x2 ox = {yp[16 + m][0], yp[16 + m][1]}; *(LAS u32x2*)(size_t)(lds + K3_PK + row * K3_PP + 256 + 8 * g) = ox; }
            }
        }
        if (ph == 0) {
#pragma unroll
            for (int m = 0; m < 4; ++m) {
                auto r1 = __builtin_amdgcn_permlane32_swap(F2U(t1[m]), F2U(t1[m + 4]), false, false);
                auto r2 = __builtin_amdgcn_permlane32_swap(F2U(t2[m]), F2U(t2[m + 4]), false, false);
                t1[m] = U2F(r1[0]) + U2F(r1[1]); t2[m] = U2F(r2[0]) + U2F(r2[1]);
            }
#pragma unroll
            for (int m = 0; m < 2; ++m) {
                auto r1 = __builtin_amdgcn_permlane16_swap(F2U(t1[m]), F2U(t1[m + 2]), false, false);
                auto r2 = __builtin_amdgcn_permlane16_swap(F2U(t2[m]), F2U(t2[m + 2]), false, false);
                f32x2 o = {U2F(r1[0]) + U2F(r1[1]), U2F(r2[0]) + U2F(r2[1])};
                *(LAS f32x2*)(size_t)(lds + K3_WS + (wid * 128 + 32 * (g ^ wid) + 16 * m + c) * 8) = o;
            }
            FENCE();
            stsw(FL + 48 + wid * 4, 1u);
            FENCE();
        }
        STAMP(layer * 4 + 2, 3 + 2 * ph);
    }
    {
        float bv[8];
#pragma unroll
        for (int m = 0; m < 8; ++m) bv[m] = bflo(bsp[128 * pm + (((((m >> 1) ^ wid) << 1) + (m & 1)) << 4) + c]);
#pragma unroll
        for (int m = 0; m < 8; ++m) {
            acc[m][0] = acc[m][1] = (f32x4){bv[m], bv[m], bv[m], bv[m]};
            if (m < 2) accx[m] = acc[m][0];
        }
    }
#pragma unroll
    for (int q = 0; q < 4; ++q) { pa[0][q] = lds + (c >> 3) * 1024 + (c & 7) * 128 + ((g ^ ((c >> 1) & 7)) * 16) + ((q ^ wid) * 4096); pa[1][q] = pa[0][q] ^ 64; }
    if (!early) { lds_wait(FL + 84, 1u); BPRO(16); }
    lds_wait(fladdr, 19u);
    STAMP(layer * 4 + 2, 6);
    ALOAD();
    PINACC();
    for (int j = 0; j < 10; ++j) {
        STEP(0, true, so + BADV, BAUX);
        STEP(1, true, 0, BAUX);
        STEP(2, true, 0, BAUX);
    }
    if (BADV > 0) so -= 8192;
    STEP(0, true, so, 16);
    STEP(1, false, 0, 16);
    asm volatile("s_nop 15" ::: "memory");
    STAMP(layer * 4 + 2, 7);
    {
        const u32 hb = WS_H + LSH * layer * ZB + (8 * pm * 8 + (pn >> 1)) * 9216;
#pragma unroll
        for (int m = 0; m < 8; ++m) {
            const int mt = (((m >> 1) ^ wid) << 1) + (m & 1);
            u32x4 o;
            o[0] = pk2(bflo(yp[2 * m][0]) * acc[m][0][0], bfhi(yp[2 * m][0]) * acc[m][0][1]);
            o[1] = pk2(bflo(yp[2 * m][1]) * acc[m][0][2], bfhi(yp[2 * m][1]) * acc[m][0][3]);
            o[2] = pk2(bflo(yp[2 * m + 1][0]) * acc[m][1][0], bfhi(yp[2 * m + 1][0]) * acc[m][1][1]);
            o[3] = pk2(bflo(yp[2 * m + 1][1]) * acc[m][1][2], bfhi(yp[2 * m + 1][1]) * acc[m][1][3]);
            BST128(o, rs, vA, hb + mt * 73728 + ((pn & 1) * 4 + wid) * 1024, 16);
            if (m < 2) {
                u32x2 ox = {pk2(bflo(yp[16 + m][0]) * accx[m][0], bfhi(yp[16 + m][0]) * accx[m][1]), pk2(bflo(yp[16 + m][1]) * accx[m][2], bfhi(yp[16 + m][1]) * accx[m][3])};
                __builtin_amdgcn_raw_buffer_store_b64(ox, rs, vA, hb + mt * 73728 + 8192 + (pn & 1) * 8, 16);
            }
        }
        STAMP(layer * 4 + 2, 12);
        WAIT_V(0);
        u32 old = 0;
        if (lane == 0) old = __hip_atomic_fetch_add((LAS u32*)(size_t)(FL + 92), 1u, __ATOMIC_RELAXED, __HIP_MEMORY_SCOPE_WORKGROUP);
        if (__builtin_amdgcn_readfirstlane(old) == 3 && lane < 16)
            __hip_atomic_fetch_add((GAS u32*)(ws + WS_CNT + (layer * 16 + pm) * 4096 + lane * 256 + 128), 1u << pn, RLX_AGENT);
    }
    STAMP_OUT(layer * 4 + 2);
    STAMP(layer * 4 + 3, 0);
    {
        const int mt = 2 * pm + (MAP5 ? pn >> 3 : pn & 1), nt = MAP5 ? pn & 7 : pn >> 1;
        const u32 wb = WS_WOUT + (nt * 72 + 18 * wid) * 4608;
        const u32 vo4 = 4096 + g * 128 + (lane & 7) * 16;
        bf16x8 fa[4][4], fw[3][5], fl[5];
        f32x4 ac[4][5];
#pragma unroll
        for (int m = 0; m < 4; ++m)
#pragma unroll
            for (int n = 0; n < 5; ++n) ac[m][n] = (f32x4){0.f, 0.f, 0.f, 0.f};
        u32 abm[4];
#pragma unroll
        for (int m = 0; m < 4; ++m) abm[m] = WS_H + LSH * layer * ZB + ((4 * mt + (m ^ wid)) * 8 + 2 * wid) * 9216;
#define K5_LD(v, sof, aux) __builtin_bit_cast(bf16x8, __builtin_amdgcn_raw_buffer_load_b128(rs, v, sof, aux))
#define K5_LW(r, s) do { _Pragma("unroll") for (int n = 0; n < 4; ++n) fw[r][n] = K5_LD(vA + n * 1024, wb + (s) * 4608, 0); fw[r][4] = K5_LD(vo4, wb + (s) * 4608, 0); SB(); } while (0)
#define K5_LL(s) do { _Pragma("unroll") for (int n = 0; n < 4; ++n) fl[n] = LDSR(wl, (s) * 4608 + n * 1024); fl[4] = LDSR(wl4, (s) * 4608); SB(); } while (0)
#define K5_LA(r, s) do { _Pragma("unroll") for (int m = 0; m < 4; ++m) fa[r][m] = K5_LD(vA + ((s) & 3) * 1024, abm[m] + ((s) & ~3) * 1024, 16); SB(); } while (0)
#define K5_MM(W, r) do { _Pragma("unroll") for (int m = 0; m < 4; ++m) _Pragma("unroll") for (int n = 0; n < 5; ++n) ac[m][n] = MFMAB(W[n], fa[r][m], ac[m][n]); SB(); } while (0)
        PINV(vA);
        const u32 wl = lds + wid * 2 * STAGE + vA, wl4 = lds + wid * 2 * STAGE + vo4;
        K5_LW(0, 8); K5_LW(1, 9); K5_LW(2, 10);
        u32x4 res[2]; u32x2 res2;
        const u32 xoff = WS_X + (2 * layer + 1) * XB + (4 * mt + wid) * 18432;
        {
            const u32 need = 15u << (4 * wid);
            FENCE();
#pragma nounroll
            for (u32 n = 0; n < SPIN_CAP; ++n) { if ((__builtin_amdgcn_readfirstlane(ldsw(FL + 88)) & need) == need) break; }
            FENCE();
        }
        STAMP(layer * 4 + 3, 1);
        constexpr int ord[18] = {8, 0, 9, 1, 10, 2, 11, 3, 12, 4, 13, 5, 14, 6, 15, 7, 16, 17};
#pragma unroll
        for (int i = 0; i < 4; ++i) K5_LA(i, ord[i]);
        lds_wait(FL, 100u);
#pragma unroll
        for (int m = 0; m < 4; ++m)
#pragma unroll
            for (int n = 0; n < 5; ++n) PINV(ac[m][n]);
        asm volatile("s_nop 1");
#pragma unroll
        for (int i = 0; i < 18; ++i) {
            const int sl = ord[i];
#pragma unroll
            for (int m = 0; m < 4; ++m)
#pragma unroll
                for (int n = 0; n < 5; ++n) {
                    if (sl >= 8) MFS(ac[m][n], fw[(sl - 8) % 3][n], fa[i & 3][m]); else MFS(ac[m][n], fl[n], fa[i & 3][m]);
                    const int e = (n == 1 ? m : (m == 0 && n == 3 ? 4 : -1));
                    if (e >= 0) {
                        if (sl >= 8 && sl < 16) fl[e] = e < 4 ? LDSR(wl, (sl - 8) * 4608 + e * 1024) : LDSR(wl4, (sl - 8) * 4608);
                        if (sl < 7) fw[sl % 3][e] = e < 4 ? K5_LD(vA + e * 1024, wb + (sl + 11) * 4608, 0) : K5_LD(vo4, wb + (sl + 11) * 4608, 0);
                        if (sl == 7 && e == 0) res[0] = __builtin_amdgcn_raw_buffer_load_b128(rs, vA, xoff + 2304 * nt, 0);
                        if (sl == 7 && e == 1) res[1] = __builtin_amdgcn_raw_buffer_load_b128(rs, vA + 1024, xoff + 2304 * nt, 0);
                        if (sl == 7 && e == 2) res2 = __builtin_amdgcn_raw_buffer_load_b64(rs, (vA & 255) + ((vA >> 5) & 8) + 2048, xoff + 2304 * nt, 0);
                    }
                    if (n == 4 && i + 4 < 18) fa[i & 3][m] = K5_LD(vA + (ord[i + 4 < 18 ? i + 4 : 0] & 3) * 1024, abm[m] + (ord[i + 4 < 18 ? i + 4 : 0] & ~3) * 1024, 16);
                    SB();
                }
        }
        asm volatile("s_nop 15" ::: "memory");
        STAMP(layer * 4 + 3, 3);
        const u32 xa0 = 2304 * nt + vA, xa2 = 2304 * nt + 2048 + (vA & 255) + ((vA >> 5) & 8);
#pragma unroll
        for (int m = 1; m < 4; ++m)
#pragma unroll
            for (int n = 0; n < 5; ++n) *(LAS f32x4*)(size_t)(wl + ((m - 1) * 5 + n) * 1024) = ac[m][n];
        FENCE();
        stsw(FL + 96 + wid * 4, 1u);
        lds_wait(FL + 96 + (lane & 3) * 4, 1u);
#pragma unroll
        for (int n = 0; n < 5; ++n)
            ac[0][n] = (ac[0][n] + *(LAS f32x4*)(size_t)(lds + (wid ^ 1) * 2 * STAGE + vA + n * 1024)) +
                       (*(LAS f32x4*)(size_t)(lds + (wid ^ 2) * 2 * STAGE + vA + (5 + n) * 1024) + *(LAS f32x4*)(size_t)(lds + (wid ^ 3) * 2 * STAGE + vA + (10 + n) * 1024));
        u32x4 o[2]; u32x2 o2;
#pragma unroll
        for (int i = 0; i < 2; ++i) {
            const f32x4 p = ac[0][2 * i], q = ac[0][2 * i + 1];
            o[i] = (u32x4){pk2(p[0] + bflo(res[i][0]), p[1] + bfhi(res[i][0])), pk2(p[2] + bflo(res[i][1]), p[3] + bfhi(res[i][1])),
                           pk2(q[0] + bflo(res[i][2]), q[1] + bfhi(res[i][2])), pk2(q[2] + bflo(res[i][3]), q[3] + bfhi(res[i][3]))};
        }
        o2 = (u32x2){pk2(ac[0][4][0] + bflo(res2[0]), ac[0][4][1] + bfhi(res2[0])), pk2(ac[0][4][2] + bflo(res2[1]), ac[0][4][3] + bfhi(res2[1]))};
        if (layer == 1) {
            u16* op = out + (size_t)(64 * mt + 16 * wid + c) * 576 + 72 * nt;
            *(u32x4*)(op + 8 * g) = o[0];
            *(u32x4*)(op + 32 + 8 * g) = o[1];
            if (g < 2) *(u32x2*)(op + 64 + 4 * g) = o2;
        } else {
            char* xp = ws + xoff + XB;
            *(u32x4*)(xp + xa0) = o[0];
            *(u32x4*)(xp + xa0 + 1024) = o[1];
            if (g < 2) *(u32x2*)(xp + xa2) = o2; else o2 = (u32x2){0u, 0u};
            float s1 = 0.f, s2 = 0.f;
#pragma unroll
            for (int k = 0; k < 10; ++k) { const u32 wv = k < 8 ? o[k >> 2][k & 3] : o2[k & 1]; const float u = bflo(wv), v = bfhi(wv); s1 += u + v; s2 += u * u + v * v; }
            auto r1 = __builtin_amdgcn_permlane32_swap(F2U(s1), F2U(s2), false, false);
            const float t = U2F(r1[0]) + U2F(r1[1]);
            auto r2 = __builtin_amdgcn_permlane16_swap(F2U(t), F2U(t), false, false);
            const float tt = U2F(r2[0]) + U2F(r2[1]);
            if ((g & 1) == 0) *(float*)(ws + WS_XS + 2 * XSB + (nt * 2048 + 64 * mt + 16 * wid + c) * 8 + (g >> 1) * 4) = tt;
        }
    }
    STAMP(layer * 4 + 3, 2);
    STAMP_OUT(layer * 4 + 3);
}

extern "C" void kernel_launch(void* const* d_in, const int* in_sizes, int n_in, void* d_out, int out_size, void* d_ws, size_t ws_size,
                              hipStream_t stream) {
    const u16* x = (const u16*)d_in[0];
    const u16* wqkv = (const u16*)d_in[1];
    const u16* wo = (const u16*)d_in[2];
    const u16* win = (const u16*)d_in[4];
    const u16* wsp = (const u16*)d_in[5];
    const u16* bsp = (const u16*)d_in[6];
    const u16* wout = (const u16*)d_in[7];
    char* ws = (char*)d_ws;
    u16* out = (u16*)d_out;
    (void)hipFuncSetAttribute((const void*)k1_attn, hipFuncAttributeMaxDynamicSharedMemorySize, K1_LDS);
    (void)hipFuncSetAttribute((const void*)k34_mlp, hipFuncAttributeMaxDynamicSharedMemorySize, K3_LDS);
    (void)hipFuncSetAttribute((const void*)k2_proj<0>, hipFuncAttributeMaxDynamicSharedMemorySize, K2_LDS);
    k0_prep<<<K0_GRID, 576, 0, stream>>>(x, wqkv, wo, win, wout, wsp, ws);
    for (int layer = 0; layer < 2; ++layer) {
        k1_attn<<<256, 512, K1_LDS, stream>>>(ws, layer);
        k2_proj<0><<<256, 256, K2_LDS, stream>>>(ws, out, layer);
        k34_mlp<<<256, 512, K3_LDS, stream>>>(ws, wsp, bsp, out, layer);
    }
}
```

```cpp
#include <hip/hip_runtime.h>
#include <hip/hip_bf16.h>
#include <stdint.h>

#define LAS __attribute__((address_space(3)))
#define GAS __attribute__((address_space(1)))
typedef unsigned short u16;
typedef unsigned u32;
typedef unsigned long long u64;
typedef short bf16x8 __attribute__((ext_vector_type(8)));
typedef float f32x2 __attribute__((ext_vector_type(2)));
typedef float f32x4 __attribute__((ext_vector_type(4)));
typedef unsigned u32x2 __attribute__((ext_vector_type(2)));
typedef unsigned u32x4 __attribute__((ext_vector_type(4)));
typedef __bf16 bf2_t __attribute__((ext_vector_type(2)));

#define ANY(p) (__builtin_amdgcn_ballot_w64(p) != 0)
#define U2F(x) __builtin_bit_cast(float, (u32)(x))
#define F2U(x) __builtin_bit_cast(u32, (float)(x))
#define WAIT_V(n) asm volatile("s_waitcnt vmcnt(%0)" ::"i"(n) : "memory")
#define WAIT_L0() asm volatile("s_waitcnt lgkmcnt(0)" ::: "memory")
#define FENCE() asm volatile("" ::: "memory")
#define SB() __builtin_amdgcn_sched_barrier(0)
#define BAR() do { FENCE(); __builtin_amdgcn_s_barrier(); FENCE(); } while (0)
#define WCNT(vm, lg) __builtin_amdgcn_s_waitcnt(((vm) & 15) | (7 << 4) | ((lg) << 8) | (((vm) >> 4) << 14))
#define MFMA(acc, x, y) asm volatile("v_mfma_f32_16x16x32_bf16 %0, %1, %2, %0" : "+v"(acc) : "v"(x), "v"(y))
#define MFMAB(x, y, c) __builtin_amdgcn_mfma_f32_16x16x32_bf16(x, y, c, 0, 0, 0)
#define BST128(o, rs, vo, so, aux) do { __builtin_amdgcn_raw_buffer_store_b128(o, rs, vo, so, aux); SB(); asm volatile("s_nop 1"); SB(); } while (0)
#define PINV(x) asm volatile("" : "+v"(x))
#define RLX_AGENT __ATOMIC_RELAXED, __HIP_MEMORY_SCOPE_AGENT

constexpr int S_ = 2048, D_ = 576, FH_ = 2304;
#define XDELAY1 18
#define XDELAY3 20
#define LGATE 12
#define LGATE2 14
#define POLLD 40
#define STAG 3
#define HPRIO 3
#define FDELAY 45
#define STAG2 60
#define MAP1 1
#define MAP3 1
#define CODETOUCH 1
#define WSPTOUCH 1
#define LSH 0
#define L2PF 0
#define ZSPLIT 10
#define ZGATE 8
#define QLATE 0
#define BADV 3072
#define AFAKE 0
#define BAUX 16
#define HDELAY 10
#define MAP5 1
#define WGATE 0
#define WGI 7
#define WEARLY 0
#define ODELAY 12
constexpr u32 SENT = 0xFFFFFFFFu;
constexpr u32 SPIN_CAP = 1u << 16;

constexpr u32 WS_WQ = 0;
constexpr u32 WS_CSQ = WS_WQ + 16 * 129024;
constexpr u32 WS_WO = WS_CSQ + 16384;
constexpr u32 WS_WIN = WS_WO + 8 * 18 * 4608;
constexpr u32 WS_CSIN = WS_WIN + 288 * 18 * 1024;
constexpr u32 WS_WOUT = WS_CSIN + 2 * 4608 * 4;
constexpr u32 WS_X = WS_WOUT + 8 * 72 * 4608;
constexpr u32 XB = 128 * 18 * 1024, XSB = 2048 * 64, ZB = 144 * 64 * 1024;
constexpr u32 WS_XS = WS_X + 4 * XB;
constexpr u32 WS_O = WS_XS + 4 * XSB;
constexpr u32 WS_Z = WS_O + (1 + LSH) * XB;
constexpr u32 WS_H = WS_Z + (1 + LSH) * ZB;
constexpr u32 WS_KVP = WS_H + (1 + LSH) * ZB;
constexpr u32 WS_ZST = WS_KVP + 2 * 256 * 2816;
constexpr u32 WS_CNT = WS_ZST + 2 * 256 * 1024;
constexpr u32 WS_CNO = WS_CNT + 2 * 16 * 32 * 128;
constexpr u32 WS_END = WS_CNO + 2 * 16 * 16 * 128;
constexpr u32 WS_DBG = WS_END;
#define WSTOP 0
#define WS_BASE(n) (WSTOP ? (((n) - WS_END - 0x100000) & ~(size_t)0xfffff) : 0)

#define STAMP_DECL
#define STAMP(k, i) do { } while (0)
#define HST(i) do { } while (0)
#define HST_DECL
#define HST_OUT(k) do { } while (0)
#define STAMPV(k, i, v) do { } while (0)
#define STAMP_OUT(k) do { } while (0)

__device__ __forceinline__ u32 pk2(float lo, float hi) {
    f32x2 v = {lo, hi};
    bf2_t b = __builtin_convertvector(v, bf2_t);
    return __builtin_bit_cast(u32, b);
}
__device__ __forceinline__ float bflo(u32 w) { return U2F(w << 16); }
__device__ __forceinline__ float bfhi(u32 w) { return U2F(w & 0xffff0000u); }
__device__ __forceinline__ bf16x8 mk8(u32 a, u32 b, u32 c, u32 d) { u32x4 v = {a, b, c, d}; return __builtin_bit_cast(bf16x8, v); }
__device__ __forceinline__ float sfma(float a, float b, float c) { float r; asm("v_fma_f32 %0, %1, %2, %3" : "=v"(r) : "v"(a), "v"(b), "v"(c)); return r; }
__device__ __forceinline__ f32x4 sadd4(f32x4 a, f32x4 b) {
    float r0 = a[0], r1 = a[1], r2 = a[2], r3 = a[3];
    asm("v_add_f32 %0, %0, %4\n\tv_add_f32 %1, %1, %5\n\tv_add_f32 %2, %2, %6\n\tv_add_f32 %3, %3, %7" : "+v"(r0), "+v"(r1), "+v"(r2), "+v"(r3) : "v"(b[0]), "v"(b[1]), "v"(b[2]), "v"(b[3]));
    f32x4 r = {r0, r1, r2, r3};
    return r;
}
__device__ __forceinline__ f32x4 phi4(f32x4 t) {
    const f32x4 u = t * 1.44269504089f;
    f32x4 e, m;
#pragma unroll
    for (int r = 0; r < 4; ++r) { e[r] = __builtin_amdgcn_exp2f(__builtin_amdgcn_fmed3f(u[r], 0.f, -__builtin_inff())); m[r] = __builtin_amdgcn_fmed3f(t[r], 0.f, __builtin_inff()); }
    return (m + e) + 1e-6f;
}
__device__ __forceinline__ u32 ldsw(u32 a) { return *(volatile LAS u32*)(size_t)a; }
__device__ __forceinline__ void stsw(u32 a, u32 v) { *(volatile LAS u32*)(size_t)a = v; }
#define LDSR(addr, off) (*(LAS bf16x8*)((LAS char*)(size_t)(addr) + (off)))
__device__ __forceinline__ void lds_wait(u32 a, u32 target) {
    FENCE();
#pragma nounroll
    for (u32 n = 0; n < SPIN_CAP; ++n) { const u32 v = ldsw(a); if (!ANY(v < target)) break; }
    FENCE();
}
__device__ __forceinline__ void lds_wait_gt(u32 a, u32 low) {
    FENCE();
#pragma nounroll
    for (u32 n = 0; n < SPIN_CAP; ++n) { const u32 v = ldsw(a); if (!ANY(v <= low)) break; }
    FENCE();
}
__device__ __forceinline__ void lds_wait_sleep(u32 a, u32 target) {
    FENCE();
#pragma nounroll
    for (u32 n = 0; n < SPIN_CAP; ++n) { const u32 v = ldsw(a); if (!ANY(v < target)) break; __builtin_amdgcn_s_sleep(1); }
    FENCE();
}
#define DMA4(v0, v1, v2, v3, sbase, m0v)                                                                            \
    asm volatile("s_mov_b32 m0, %5\n\ts_nop 0\n\tglobal_load_lds_dwordx4 %0, %4\n\t"                                \
                 "global_load_lds_dwordx4 %1, %4 offset:1024\n\tglobal_load_lds_dwordx4 %2, %4 offset:2048\n\t"     \
                 "global_load_lds_dwordx4 %3, %4 offset:3072" ::"v"(v0), "v"(v1), "v"(v2), "v"(v3), "s"(sbase), "s"(m0v) : "memory")
#define DMA2(v0, v1, sbase, m0v)                                                                                    \
    asm volatile("s_mov_b32 m0, %3\n\ts_nop 0\n\tglobal_load_lds_dwordx4 %0, %2\n\t"                                \
                 "global_load_lds_dwordx4 %1, %2 offset:1024" ::"v"(v0), "v"(v1), "s"(sbase), "s"(m0v) : "memory")

__device__ __forceinline__ u32x4 tcol(const u32x2 (&ld)[8], int i) {
    const u32 sel = (i & 1) ? 0x07060302u : 0x05040100u;
    u32x4 o;
    o.x = __builtin_amdgcn_perm(ld[1][i >> 1], ld[0][i >> 1], sel);
    o.y = __builtin_amdgcn_perm(ld[3][i >> 1], ld[2][i >> 1], sel);
    o.z = __builtin_amdgcn_perm(ld[5][i >> 1], ld[4][i >> 1], sel);
    o.w = __builtin_amdgcn_perm(ld[7][i >> 1], ld[6][i >> 1], sel);
    return o;
}
__device__ __forceinline__ float sum8(u32x4 o) {
    return ((bflo(o.x) + bfhi(o.x)) + (bflo(o.y) + bfhi(o.y))) + ((bflo(o.z) + bfhi(o.z)) + (bflo(o.w) + bfhi(o.w)));
}

constexpr int K0_QKV = 128, K0_X = 64, K0_WIN = 144, K0_WO = 90, K0_INIT = 54, K0_TOUCH = WSPTOUCH ? 114 : 0, K0_GRID = K0_QKV + K0_X + K0_WIN + K0_WO + K0_INIT + K0_TOUCH;
__device__ __forceinline__ void k0_body(const u16* __restrict__ x, const u16* __restrict__ wqkv, const u16* __restrict__ wo,
                                        const u16* __restrict__ win, const u16* __restrict__ wout, const u16* __restrict__ wsp, char* __restrict__ ws) {
    __shared__ __attribute__((aligned(16))) char img[36864 + 576 * 8];
    float (*red)[64] = (float (*)[64])(img + 36864);
    const int tid = threadIdx.x, w = __builtin_amdgcn_readfirstlane(tid >> 6), lane = tid & 63, c = lane & 15, g = lane >> 4;
    int b = blockIdx.x;
    if (b < K0_QKV) {
        const int hf = b & 1, fp = (b >> 1) & 3, p = b >> 3, kt = 9 * hf + w;
        const int fi = 2 * fp + (c >> 3), cc = 2 * (c & 7);
        int col; bool ok = true;
        if (fi == 4) { ok = cc < 12; const int tt = (cc >> 2) == 0 ? 1 : ((cc >> 2) == 1 ? 2 : 0); col = 576 * tt + 18 * (2 * p + ((cc >> 1) & 1)) + 16; }
        else if (fi < 4) col = 576 * (1 + (fi >> 1)) + 18 * (2 * p + (fi & 1)) + cc;
        else col = 18 * (2 * p + ((fi - 5) & 1)) + cc;
        u32 ld[8];
#pragma unroll
        for (int j = 0; j < 8; ++j) ld[j] = *(const u32*)(wqkv + (size_t)(32 * kt + 8 * g + j) * 1728 + col);
        SB();
        const u32 msk = ok ? 0xffffffffu : 0u;
#pragma unroll
        for (int j = 0; j < 8; ++j) ld[j] &= msk;
#pragma unroll
        for (int i = 0; i < 2; ++i) {
            const u32 sel = i ? 0x07060302u : 0x05040100u;
            u32x4 o;
            o.x = __builtin_amdgcn_perm(ld[1], ld[0], sel); o.y = __builtin_amdgcn_perm(ld[3], ld[2], sel);
            o.z = __builtin_amdgcn_perm(ld[5], ld[4], sel); o.w = __builtin_amdgcn_perm(ld[7], ld[6], sel);
            *(u32x4*)(img + w * 2048 + (c >> 3) * 1024 + (cc + i + 16 * g) * 16) = o;
            float cs = sum8(o);
            cs += __shfl_xor(cs, 16); cs += __shfl_xor(cs, 32);
            if (g == 0) red[w][2 * c + i] = cs;
        }
        FENCE();
#pragma unroll
        for (int t = 0; t < 2; ++t) {
            const int f2 = 2 * fp + t;
            const u32 tile = f2 < 5 ? (kt * 5 + f2) * 1024 : 92160 + (kt * 2 + f2 - 5) * 1024;
            if (f2 < 7) *(u32x4*)(ws + WS_WQ + p * 129024 + tile + lane * 16) = *(const u32x4*)(img + w * 2048 + t * 1024 + lane * 16);
        }
        __syncthreads();
        if (tid < 32) {
            float s = 0.f;
#pragma unroll
            for (int k = 0; k < 9; ++k) s += red[k][tid];
            ((float*)(ws + WS_CSQ))[(hf * 16 + p) * 128 + fp * 32 + tid] = s;
        }
        return;
    }
    b -= K0_QKV;
    if (b < K0_X) {
        const int rl = tid / 18, t18 = tid - 18 * rl;
        u32x4 v[4];
#pragma unroll
        for (int i = 0; i < 4; ++i) v[i] = *(const u32x4*)(x + (size_t)(32 * b + rl) * 576 + (18 * i + t18) * 8);
        float s1 = 0.f, s2 = 0.f;
#pragma unroll
        for (int i = 0; i < 4; ++i) {
            const int ch = 18 * i + t18;
            *(u32x4*)(img + ((rl >> 4) * 18 + (ch >> 2)) * 1024 + ((rl & 15) + 16 * (ch & 3)) * 16) = v[i];
#pragma unroll
            for (int k = 0; k < 4; ++k) { const float a = bflo(v[i][k]), bb = bfhi(v[i][k]); s1 += a + bb; s2 += a * a + bb * bb; }
        }
        f32x2 o = {s1, s2};
        ((f32x2*)(img + 36864))[tid] = o;
        __syncthreads();
#pragma unroll
        for (int t = 0; t < 4; ++t) *(u32x4*)(ws + WS_X + b * 36864 + (w * 4 + t) * 1024 + lane * 16) = *(const u32x4*)(img + (w * 4 + t) * 1024 + lane * 16);
        if (tid < 256) {
            const int r = tid & 31, q = tid >> 5;
            const f32x2* pp = (const f32x2*)(img + 36864) + r * 18;
            o = pp[2 * q] + pp[2 * q + 1];
            if (q < 2) o += pp[16 + q];
            *(f32x2*)(ws + WS_XS + (q * 2048 + 32 * b + r) * 8) = o;
        }
        return;
    }
    b -= K0_X;
    if (b < K0_WIN) {
        const int hf = b & 1, cb = b >> 1, kt = 9 * hf + w;
        u32x2 ld[8];
#pragma unroll
        for (int j = 0; j < 8; ++j) ld[j] = *(const u32x2*)(win + (size_t)(32 * kt + 8 * g + j) * 4608 + 64 * cb + 4 * c);
#pragma unroll
        for (int i = 0; i < 4; ++i) {
            const u32x4 o = tcol(ld, i);
            *(u32x4*)(img + w * 4096 + (c >> 2) * 1024 + (4 * (c & 3) + i + 16 * g) * 16) = o;
            float cs = sum8(o);
            cs += __shfl_xor(cs, 16); cs += __shfl_xor(cs, 32);
            if (g == 0) red[w][4 * c + i] = cs;
        }
        FENCE();
#pragma unroll
        for (int t = 0; t < 4; ++t) *(u32x4*)(ws + WS_WIN + ((4 * cb + t) * 18 + kt) * 1024 + lane * 16) = *(const u32x4*)(img + w * 4096 + t * 1024 + lane * 16);
        __syncthreads();
        if (tid < 64) {
            float s = 0.f;
#pragma unroll
            for (int k = 0; k < 9; ++k) s += red[k][tid];
            ((float*)(ws + WS_CSIN))[hf * 4608 + 64 * cb + tid] = s;
        }
        return;
    }
    b -= K0_WIN;
    if (b < K0_WO) {
        const bool iso = b < 18;
        const int sl = iso ? b : b - 18, G = sl / 9, t = sl - 9 * G;
        const u16* src = iso ? wo : wout;
        const int col = 64 * w + 4 * c, nt = col / 72, cl = col - 72 * nt;
        const int r0 = iso ? (sl < 16 ? 36 * sl : 288 * (sl - 16) + 16) : (t < 8 ? 288 * G + 144 * (t >> 2) + 32 * (t & 3) : 288 * G + 128);
        const int gs = (iso && sl >= 16) ? 72 : 4, s4 = iso ? (sl < 16 ? 18 : 36) : (t < 8 ? 16 : 144), s2 = (iso && sl >= 16) ? 18 : 2;
        u32x2 ld[8];
#pragma unroll
        for (int j = 0; j < 8; ++j) ld[j] = *(const u32x2*)(src + (size_t)(r0 + gs * g + s4 * (j >> 2) + s2 * ((j >> 1) & 1) + (j & 1)) * 576 + col);
        const int n = cl < 64 ? 2 * (cl >> 5) + ((cl >> 2) & 1) : 4, ii = cl < 64 ? 4 * ((cl >> 3) & 3) : cl - 64;
#pragma unroll
        for (int i = 0; i < 4; ++i) {
            const u32 off = n < 4 ? n * 1024 + (ii + i + 16 * g) * 16 : 4096 + g * 128 + (ii + i) * 16;
            *(u32x4*)(img + nt * 4608 + off) = tcol(ld, i);
        }
        __syncthreads();
#pragma unroll
        for (int k = 0; k < 4; ++k) {
            const u32 i = (w * 4 + k) * 1024 + lane * 16, n2 = i / 4608, off = i - 4608 * n2;
            *(u32x4*)(ws + (iso ? WS_WO + (n2 * 18 + sl) * 4608 : WS_WOUT + (n2 * 72 + sl) * 4608) + off) = *(const u32x4*)(img + i);
        }
        return;
    }
    b -= K0_WO;
    if (b >= K0_INIT) {
        __builtin_amdgcn_s_sleep(WSPTOUCH);
        const u32 ln = (b - K0_INIT) * 576 + tid;
        if (ln < 65536) { const u32 v = *(const u32*)(wsp + (size_t)ln * 64); asm volatile("" :: "v"(v)); }
        return;
    }
    if (b < 16) {
        u64 pc; int dist;
        asm volatile("s_getpc_b64 %0\n\ts_mov_b32 %1, k9_end@rel32@lo+4" : "=&s"(pc), "=&s"(dist));
        if (tid * 128 < dist) { u32 v = *(const GAS u32*)(pc + tid * 128); asm volatile("" :: "v"(v)); }
    }
    {
        const auto rs = __builtin_amdgcn_make_buffer_rsrc(ws, 0, WS_END, 0x00020000);
        const u32 n16 = (WS_END - WS_KVP) / 16, nz = (WS_CNT - WS_KVP) / 16;
        for (u32 i = b * 576 + tid; i < n16; i += K0_INIT * 576) {
            const u32 v = i < nz ? SENT : 0u;
            u32x4 o = {v, v, v, v};
            BST128(o, rs, i * 16, WS_KVP, 16);
        }
    }
}

__global__ __launch_bounds__(576) void k0_prep(const u16* __restrict__ x, const u16* __restrict__ wqkv, const u16* __restrict__ wo,
                                               const u16* __restrict__ win, const u16* __restrict__ wout, const u16* __restrict__ wsp, char* __restrict__ ws) {
    STAMP_DECL;
    STAMP(8, 0);
    k0_body(x, wqkv, wo, win, wout, wsp, ws);
    STAMP(8, 1);
    STAMP_OUT(11);
}

__device__ __forceinline__ void x_stats(const char* ws, int ver, int row, float& mean, float& var) {
    const f32x2* p = (const f32x2*)(ws + WS_XS + ver * XSB) + row;
    f32x2 v[8];
#pragma unroll
    for (int k = 0; k < 8; ++k) v[k] = p[k * 2048];
    const f32x2 sm = ((v[0] + v[1]) + (v[2] + v[3])) + ((v[4] + v[5]) + (v[6] + v[7]));
    const float s1 = sm[0], s2 = sm[1];
    mean = s1 * (1.f / 576.f);
    var = fmaxf(s2 * (1.f / 576.f) - mean * mean, 0.f);
}

__device__ __forceinline__ void proj_store(const f32x4 (&y)[5], char* ws, u16* out, int ver, int mt, int nt, int wid, int lane) {
    const u32 vo = lane * 16;
    const int c = lane & 15, g = lane >> 4;
    u32x4 o[2]; u32x2 o2;
#pragma unroll
    for (int i = 0; i < 2; ++i) o[i] = (u32x4){pk2(y[2 * i][0], y[2 * i][1]), pk2(y[2 * i][2], y[2 * i][3]), pk2(y[2 * i + 1][0], y[2 * i + 1][1]), pk2(y[2 * i + 1][2], y[2 * i + 1][3])};
    o2 = (u32x2){pk2(y[4][0], y[4][1]), pk2(y[4][2], y[4][3])};
    if (ver == 4) {
        u16* op = out + (size_t)(64 * mt + 16 * wid + c) * 576 + 72 * nt;
        *(u32x4*)(op + 8 * g) = o[0];
        *(u32x4*)(op + 32 + 8 * g) = o[1];
        if (g < 2) *(u32x2*)(op + 64 + 4 * g) = o2;
    } else {
        char* xp = ws + WS_X + ver * XB + (4 * mt + wid) * 18432 + 2304 * nt;
        *(u32x4*)(xp + vo) = o[0];
        *(u32x4*)(xp + vo + 1024) = o[1];
        if (g < 2) *(u32x2*)(xp + 2048 + c * 16 + g * 8) = o2; else o2 = (u32x2){0u, 0u};
        float s1 = 0.f, s2 = 0.f;
#pragma unroll
        for (int k = 0; k < 10; ++k) { const u32 wv = k < 8 ? o[k >> 2][k & 3] : o2[k & 1]; const float u = bflo(wv), v = bfhi(wv); s1 += u + v; s2 += u * u + v * v; }
        auto r1 = __builtin_amdgcn_permlane32_swap(F2U(s1), F2U(s2), false, false);
        const float t = U2F(r1[0]) + U2F(r1[1]);
        auto r2 = __builtin_amdgcn_permlane16_swap(F2U(t), F2U(t), false, false);
        const float tt = U2F(r2[0]) + U2F(r2[1]);
        if ((g & 1) == 0) *(float*)(ws + WS_XS + ver * XSB + (nt * 2048 + 64 * mt + 16 * wid + c) * 8 + (g >> 1) * 4) = tt;
    }
}

__device__ __forceinline__ void proj_tail(f32x4 (&ac)[4][5], const u32x4 (&res)[2], u32x2 res2, u32 lds, u32 wstride, u32 flags, char* ws, u16* out,
                                          int ver, int mt, int nt, int wid, int lane) {
    const u32 vo = lane * 16;
#pragma unroll
    for (int m = 1; m < 4; ++m)
#pragma unroll
        for (int n = 0; n < 5; ++n) *(LAS f32x4*)(size_t)(lds + wid * wstride + vo + ((m - 1) * 5 + n) * 1024) = ac[m][n];
    FENCE();
    stsw(flags + wid * 4, 1u);
#pragma unroll
    for (int i = 0; i < 4; ++i) {
        const u32 w0 = res[i >> 1][2 * (i & 1)], w1 = res[i >> 1][2 * (i & 1) + 1];
        ac[0][i] += (f32x4){bflo(w0), bfhi(w0), bflo(w1), bfhi(w1)};
        PINV(ac[0][i]);
    }
    ac[0][4] += (f32x4){bflo(res2[0]), bfhi(res2[0]), bflo(res2[1]), bfhi(res2[1])};
    PINV(ac[0][4]);
    lds_wait(flags + (lane & 3) * 4, 1u);
#pragma unroll
    for (int n = 0; n < 5; ++n)
        ac[0][n] = (ac[0][n] + *(LAS f32x4*)(size_t)(lds + (wid ^ 1) * wstride + vo + n * 1024)) +
                   (*(LAS f32x4*)(size_t)(lds + (wid ^ 2) * wstride + vo + (5 + n) * 1024) + *(LAS f32x4*)(size_t)(lds + (wid ^ 3) * wstride + vo + (10 + n) * 1024));
    proj_store(ac[0], ws, out, ver, mt, nt, wid, lane);
}

constexpr u32 K1_PW = 129024, K1_OP = K1_PW + 4 * 176 * 16, K1_ST = K1_OP + 1536, K1_CS = K1_ST + 1024, K1_FL = K1_CS + 512, K1_LDS = K1_FL + 128;

__global__ __launch_bounds__(512, 2) void k1_attn(char* __restrict__ ws, int layer) {
    extern __shared__ __attribute__((aligned(16))) char shm_[];
    const u32 lds = (u32)(size_t)(LAS char*)shm_;
    const int tid = threadIdx.x, wid = __builtin_amdgcn_readfirstlane(tid >> 6), lane = tid & 63, c = lane & 15, g = lane >> 4;
    const int xcd = blockIdx.x & 7, bi = blockIdx.x >> 3;
    const int pm = MAP1 ? (xcd & 3) * 4 + (bi & 3) : (xcd & 1) * 8 + (bi & 7), p = MAP1 ? (xcd >> 2) * 8 + (bi >> 2) : (xcd >> 1) * 4 + (bi >> 3);
    const u32 FL = lds + K1_FL;
    STAMP_DECL;
    if (wid == 4) {
        const char* src = ws + WS_WQ + p * 129024;
        const u32 vo = lane * 16;
#define K1_U10(u) do { DMA4(vo, vo, vo, vo, src + (u) * 10240, lds + (u) * 10240); DMA4(vo, vo, vo, vo, src + (u) * 10240 + 4096, lds + (u) * 10240 + 4096); \
                       DMA2(vo, vo, src + (u) * 10240 + 8192, lds + (u) * 10240 + 8192); } while (0)
#define K1_U12(u) do { DMA4(vo, vo, vo, vo, src + 92160 + (u) * 12288, lds + 92160 + (u) * 12288); DMA4(vo, vo, vo, vo, src + 92160 + (u) * 12288 + 4096, lds + 92160 + (u) * 12288 + 4096); \
                       DMA4(vo, vo, vo, vo, src + 92160 + (u) * 12288 + 8192, lds + 92160 + (u) * 12288 + 8192); } while (0)
        K1_U10(0);
        BAR();
        K1_U10(1); K1_U10(2);
#pragma unroll
        for (int u = 3; u < 9; ++u) { WAIT_V(20); stsw(FL, u - 2); FENCE(); K1_U10(u); }
        WAIT_V(20); stsw(FL, 7); FENCE(); K1_U12(0);
        WAIT_V(22); stsw(FL, 8); FENCE(); K1_U12(1);
        WAIT_V(24); stsw(FL, 9); FENCE(); K1_U12(2);
        WAIT_V(24); stsw(FL, 10); FENCE();
        WAIT_V(12); stsw(FL, 11); FENCE();
        WAIT_V(0); stsw(FL, 12);
        lds_wait_sleep(FL + 16 + (lane & 3) * 4, 2u);
        src = ws + WS_WO + (p & 7) * 82944;
#pragma unroll
        for (int u = 0; u < 8; ++u) { K1_U10(u); if (u >= 4) WAIT_V(40); }
        asm volatile("s_mov_b32 m0, %2\n\ts_nop 0\n\tglobal_load_lds_dwordx4 %0, %1" ::"v"(vo), "s"(src + 81920), "s"(lds + 81920) : "memory");
        WAIT_V(0); stsw(FL, 13);
        return;
    }
    if (wid > 4) {
        const int ht = tid - 320;
        if (ht >= 128 && ht < 160) stsw(FL + (ht - 128) * 4, 0u);
        WAIT_L0();
        BAR();
        if (wid != 7) {
            float mean, var;
            x_stats(ws, 2 * layer, 128 * pm + ht, mean, var);
            const float r = __builtin_amdgcn_rsqf(var + 1e-5f);
            *(LAS float*)(size_t)(lds + K1_ST + ht * 4) = r;
            *(LAS float*)(size_t)(lds + K1_ST + 512 + ht * 4) = -mean * r;
        } else {
            const f32x2* cp = (const f32x2*)(ws + WS_CSQ) + p * 64 + lane;
            *(LAS f32x2*)(size_t)(lds + K1_CS + lane * 8) = cp[0] + cp[1024];
        }
        FENCE();
        stsw(FL + 4 + (wid - 5) * 4, 1u);
        __builtin_amdgcn_s_setprio(3);
        lds_wait_sleep(FL + 16 + (lane & 3) * 4, 1u);
        STAMP(layer * 4 + 0, 7);
        const int it = ht < 173 ? ht : 172;
        f32x4 v = *(LAS f32x4*)(size_t)(lds + K1_PW + it * 16);
#pragma unroll
        for (int k = 1; k < 4; ++k) v = sadd4(v, *(LAS f32x4*)(size_t)(lds + K1_PW + k * 2816 + it * 16));
        const auto rs = __builtin_amdgcn_make_buffer_rsrc(ws, 0, WS_END, 0x00020000);
        const u32 gbase = WS_KVP + (layer * 16 + p) * 16 * 2816;
        {
            u32x4 o = __builtin_bit_cast(u32x4, v);
#pragma unroll
            for (int k = 0; k < 4; ++k) o[k] = o[k] == SENT ? 0x7fc00000u : o[k];
            if (ht < 173) BST128(o, rs, it * 16, gbase + pm * 2816, 16);
        }
        u32x4 t[16];
        STAMP(layer * 4 + 0, 8); STAMP(layer * 4 + 0, 9);
        __builtin_amdgcn_s_sleep(XDELAY1);
#pragma unroll
        for (int k = 0; k < 16; ++k) t[k] = __builtin_amdgcn_raw_buffer_load_b128(rs, it * 16, gbase + k * 2816, 16);
        u32 mx = 0, spins = 0;
#pragma unroll
        for (int k = 0; k < 16; ++k) mx = max(max(mx, max(t[k][0], t[k][1])), max(t[k][2], t[k][3]));
        if (__builtin_expect(ANY(mx == SENT), 0)) {
#pragma nounroll
            for (spins = 1; spins < SPIN_CAP; ++spins) {
                FENCE();
                u32 miss = 0;
#pragma unroll
                for (int k = 0; k < 16; ++k) miss |= ANY(max(max(t[k][0], t[k][1]), max(t[k][2], t[k][3])) == SENT) ? 1u << k : 0u;
                if (!miss) break;
#pragma unroll
                for (int k = 0; k < 16; ++k) if (miss >> k & 1) t[k] = __builtin_amdgcn_raw_buffer_load_b128(rs, it * 16, gbase + k * 2816, 16);
            }
        }
        STAMP(layer * 4 + 0, 10); STAMPV(layer * 4 + 0, 15, spins);
        v = __builtin_bit_cast(f32x4, t[0]);
#pragma unroll
        for (int k = 1; k < 16; ++k) v = sadd4(v, __builtin_bit_cast(f32x4, t[k]));
        u32x2 lh = {pk2(v[0], v[1]), pk2(v[2], v[3])};
        if (ht >= 173) lh = (u32x2){0u, 0u};
        *(LAS u32x2*)(size_t)(lds + K1_OP + ht * 8) = lh;
        FENCE();
        stsw(FL + 32 + (wid - 5) * 4, 1u);
        if (wid != 5) return;
        STAMP_OUT(layer * 4 + 0);
        lds_wait_sleep(FL + 48, 4u);
        __builtin_amdgcn_s_sleep(ODELAY);
        GAS u32* om = (GAS u32*)(ws + WS_CNO + ((layer * 16 + pm) * 16 + p) * 128);
#pragma nounroll
        for (u32 n = 0; n < SPIN_CAP; ++n) { if (__builtin_amdgcn_readfirstlane(__hip_atomic_load(om, RLX_AGENT)) == 0xffffu) break; }
        FENCE();
        stsw(FL + 52, 1u);
        return;
    }
    STAMP(layer * 4 + 0, 0);

    const auto rs = __builtin_amdgcn_make_buffer_rsrc(ws, 0, WS_END, 0x00020000);
    const u32 xb = WS_X + 2 * layer * XB + (8 * pm + 2 * wid) * 18432, vo = lane * 16;
    bf16x8 xa[2][18];
#define K1_LX(m, kt) xa[m][kt] = __builtin_bit_cast(bf16x8, __builtin_amdgcn_raw_buffer_load_b128(rs, vo, xb + ((m) * 18 + (kt)) * 1024, 0))
#pragma unroll
    for (int kt = 0; kt < 6; ++kt) { K1_LX(0, kt); SB(); K1_LX(1, kt); SB(); }
    f32x4 acc[2][5];
#pragma unroll
    for (int f = 0; f < 5; ++f) { acc[0][f] = acc[1][f] = (f32x4){0.f, 0.f, 0.f, 0.f}; PINV(acc[0][f]); PINV(acc[1][f]); }
    u32 wb0 = lds + lane * 16, wb1 = wb0 + 61440, wb2 = wb0 + 92160;
    PINV(wb0); PINV(wb1); PINV(wb2);
#define K1_W1(kt, f) ((kt) < 12 ? LDSR(wb0, ((kt) * 5 + (f)) * 1024) : LDSR(wb1, (((kt) - 12) * 5 + (f)) * 1024))
    BAR();
    lds_wait(FL, 1u);
    STAMP(layer * 4 + 0, 1);
    bf16x8 wf[2][5];
#pragma unroll
    for (int f = 0; f < 5; ++f) { wf[0][f] = K1_W1(0, f); SB(); }
#pragma unroll
    for (int f = 0; f < 5; ++f) { wf[1][f] = K1_W1(1, f); SB(); }
    u32 fl = 0;
#pragma unroll
    for (int kt = 0; kt < 18; ++kt) {
        if ((kt & 1) == 0 && kt + 2 < 18) { if (__builtin_expect(ANY(fl < (u32)(kt / 2 + 2)), 0)) lds_wait(FL, kt / 2 + 2); FENCE(); }
#pragma unroll
        for (int f = 0; f < 5; ++f) {
            MFMA(acc[0][f], xa[0][kt], wf[kt & 1][f]);
            if (f == 1 && kt + 6 < 18) K1_LX(0, kt + 6);
            if (f == 3 && kt + 6 < 18) K1_LX(1, kt + 6);
            if (f == 0 && (kt & 1) && kt < 16) { FENCE(); fl = ldsw(FL); FENCE(); }
            SB();
            MFMA(acc[1][f], xa[1][kt], wf[kt & 1][f]);
            if (kt + 2 < 18) wf[kt & 1][f] = K1_W1(kt + 2, f);
            SB();
        }
    }
    asm volatile("s_nop 15" ::: "memory");
    STAMP(layer * 4 + 0, 2);
    lds_wait(FL + 4 + (lane % 3) * 4, 1u);
    {
        u32 op[5][4], rk[4];
        float cs1[5];
#pragma unroll
        for (int f = 0; f < 5; ++f) cs1[f] = *(LAS float*)(size_t)(lds + K1_CS + f * 64 + c * 4);
#pragma unroll
        for (int m = 0; m < 2; ++m) {
            const f32x4 rr = *(LAS f32x4*)(size_t)(lds + K1_ST + (32 * wid + 16 * m + 4 * g) * 4);
            const f32x4 mm = *(LAS f32x4*)(size_t)(lds + K1_ST + 512 + (32 * wid + 16 * m + 4 * g) * 4);
#pragma unroll
            for (int f = 0; f < 5; ++f) {
                f32x4 v = rr * acc[m][f] + mm * cs1[f];
                if (f < 2) v = phi4(v);
                if (f == 4) { const f32x4 u = phi4(v); rk[2 * m] = pk2(u[0], u[1]); rk[2 * m + 1] = pk2(u[2], u[3]); }
                op[f][2 * m] = pk2(v[0], v[1]); op[f][2 * m + 1] = pk2(v[2], v[3]);
                if (f == 4 && c >= 12) { op[f][2 * m] = 0x3f803f80u; op[f][2 * m + 1] = 0x3f803f80u; }
            }
        }
        const bf16x8 KAa = mk8(op[0][0], op[0][1], op[0][2], op[0][3]), KAb = mk8(op[1][0], op[1][1], op[1][2], op[1][3]);
        const bf16x8 VBa = mk8(op[2][0], op[2][1], op[2][2], op[2][3]), VBb = mk8(op[3][0], op[3][1], op[3][2], op[3][3]);
        const bf16x8 RV = mk8(op[4][0], op[4][1], op[4][2], op[4][3]), RK = mk8(rk[0], rk[1], rk[2], rk[3]);
        const f32x4 z4 = {0.f, 0.f, 0.f, 0.f};
        const f32x4 T1a = MFMAB(KAa, VBa, z4), T1b = MFMAB(KAb, VBb, z4), T2a = MFMAB(KAa, RV, z4), T2b = MFMAB(KAb, RV, z4);
        const f32x4 T3a = MFMAB(RK, VBa, z4), T3b = MFMAB(RK, VBb, z4), T4 = MFMAB(RK, RV, z4);
        const u32 pw = lds + K1_PW + wid * 2816;
        *(LAS f32x4*)(size_t)(pw + lane * 16) = T1a;
        *(LAS f32x4*)(size_t)(pw + 1024 + lane * 16) = T1b;
        const f32x4 u3 = {T3a[0], T3a[1], T3b[2], T3b[3]};
        *(LAS f32x4*)(size_t)(pw + (g == 0 ? 128 + c : 175) * 16) = u3;
        *(LAS f32x4*)(size_t)(pw + (c == 12 ? 152 + g : ((c & 14) == 4 ? 144 + (c - 4) * 4 + g : 175)) * 16) = T2a;
        *(LAS f32x4*)(size_t)(pw + (c == 12 ? 164 + g : ((c & 14) == 6 ? 156 + (c - 6) * 4 + g : 175)) * 16) = T2b;
        *(LAS f32x4*)(size_t)(pw + (lane == 12 ? 172 : ((lane & 60) == 4 ? 164 + lane : 175)) * 16) = T4;
        FENCE();
        stsw(FL + 16 + wid * 4, 1u);
    }
    STAMP(layer * 4 + 0, 3);
    f32x4 qa[2][3];
#pragma unroll
    for (int f = 0; f < 3; ++f) { qa[0][f] = qa[1][f] = (f32x4){0.f, 0.f, 0.f, 0.f}; PINV(qa[0][f]); PINV(qa[1][f]); }
#define K1_W2(kt, f) ((f) < 2 ? LDSR(wb2, ((kt) * 2 + (f)) * 1024) : K1_W1(kt, 4))
    lds_wait(FL, 10u);
    bf16x8 wq[3][3];
#pragma unroll
    for (int k = 0; k < 3; ++k)
#pragma unroll
        for (int f = 0; f < 3; ++f) { wq[k][f] = K1_W2(k, f); SB(); }
#pragma unroll
    for (int kt = 0; kt < 18; ++kt) {
        if (kt == 3) lds_wait(FL, 11u);
        if (kt == 9) lds_wait(FL, 12u);
#pragma unroll
        for (int f = 0; f < 3; ++f) {
            MFMA(qa[0][f], wq[kt % 3][f], xa[0][kt]);
            SB();
            MFMA(qa[1][f], wq[kt % 3][f], xa[1][kt]);
            if (kt + 3 < 18) wq[kt % 3][f] = K1_W2(kt + 3, f);
            SB();
        }
    }
    asm volatile("s_nop 15" ::: "memory");
    STAMP(layer * 4 + 0, 4);
    stsw(FL + 16 + wid * 4, 2u);
    f32x4 cs2[3];
#pragma unroll
    for (int f = 0; f < 3; ++f) cs2[f] = *(LAS f32x4*)(size_t)(lds + K1_CS + (f < 2 ? 5 + f : 4) * 64 + g * 16);
    bf16x8 PQ[2][2];
#pragma unroll
    for (int m = 0; m < 2; ++m) {
        const float rr = *(LAS float*)(size_t)(lds + K1_ST + (32 * wid + 16 * m + c) * 4), mm = *(LAS float*)(size_t)(lds + K1_ST + 512 + (32 * wid + 16 * m + c) * 4);
        u32 w2[3][2];
#pragma unroll
        for (int f = 0; f < 3; ++f) {
            const f32x4 v = phi4(qa[m][f] * rr + cs2[f] * mm);
            w2[f][0] = pk2(v[0], v[1]); w2[f][1] = pk2(v[2], v[3]);
        }
        if (g != 2) { w2[2][0] = 0u; w2[2][1] = 0u; }
        PQ[m][0] = mk8(w2[0][0], w2[0][1], w2[2][0], w2[2][1]);
        PQ[m][1] = mk8(w2[1][0], w2[1][1], w2[2][0], w2[2][1]);
        PINV(PQ[m][0]); PINV(PQ[m][1]);
    }
    u32 ga[6];
    ga[0] = lane; ga[1] = g == 2 ? 128 + c : 175;
    ga[2] = c < 2 ? 144 + 4 * c + g : ((c & 3) == 2 ? 152 + g : 175);
    ga[3] = (c & 14) == 4 ? 156 + 4 * (c - 4) + g : ((c & 3) == 3 ? 164 + g : 175);
    ga[4] = g != 2 ? 175 : (c < 2 ? 168 + c : ((c & 3) == 2 ? 172 : 175));
    ga[5] = g != 2 ? 175 : ((c & 14) == 4 ? 166 + c : ((c & 3) == 3 ? 172 : 175));
#pragma unroll
    for (int i = 0; i < 6; ++i) { ga[i] = lds + K1_OP + ga[i] * 8; PINV(ga[i]); }
    const u32 ob = WS_O + LSH * layer * XB + (8 * pm + 2 * wid) * 18432;
    u32 orem = (16 + (p >> 3)) * 1024 + (c + 16 * ((p & 7) >> 1)) * 16 + (p & 1) * 8 + g * 4;
    PINV(orem);
    lds_wait(FL + 32 + (lane % 3) * 4, 1u);
    STAMP(layer * 4 + 0, 5);
    {
        const u32x2 l0 = *(LAS u32x2*)(size_t)ga[0], l1 = *(LAS u32x2*)(size_t)(ga[0] + 512), h01 = *(LAS u32x2*)(size_t)ga[1];
        const u32x2 l2 = *(LAS u32x2*)(size_t)ga[2], l3 = *(LAS u32x2*)(size_t)ga[3];
        const u32 h2 = *(LAS u32*)(size_t)ga[4], h3 = *(LAS u32*)(size_t)(ga[5] + 4);
        const bf16x8 A1a = mk8(l0[0], l0[1], h01[0], 0u), A1b = mk8(l1[0], l1[1], 0u, h01[1]), A2a = mk8(l2[0], l2[1], h2, 0u), A2b = mk8(l3[0], l3[1], 0u, h3);
        const f32x4 z4 = {0.f, 0.f, 0.f, 0.f};
        f32x4 O1a[2], O1b[2], O2[2];
#pragma unroll
        for (int m = 0; m < 2; ++m) O2[m] = MFMAB(A2a, PQ[m][0], z4);
#pragma unroll
        for (int m = 0; m < 2; ++m) O2[m] = MFMAB(A2b, PQ[m][1], O2[m]);
#pragma unroll
        for (int m = 0; m < 2; ++m) { O1a[m] = MFMAB(A1a, PQ[m][0], z4); O1b[m] = MFMAB(A1b, PQ[m][1], z4); }
#pragma unroll
        for (int m = 0; m < 2; ++m) {
            const float ia = __builtin_amdgcn_rcpf(O2[m][2] + 1e-6f), ib = __builtin_amdgcn_rcpf(O2[m][3] + 1e-6f);
            const f32x4 ya = O1a[m] * ia, yb = O1b[m] * ib;
            const u32x4 o = {pk2(ya[0], ya[1]), pk2(ya[2], ya[3]), pk2(yb[0], yb[1]), pk2(yb[2], yb[3])};
            BST128(o, rs, vo, ob + m * 18432 + p * 1024, 16);
            const float ir = g == 0 ? ia : ib;
            if (g < 2) __builtin_amdgcn_raw_buffer_store_b32(pk2(O2[m][0] * ir, O2[m][1] * ir), rs, orem, ob + m * 18432, 16);
        }
    }
    STAMP(layer * 4 + 0, 6);
    STAMP_OUT(layer * 4 + 0);
    WAIT_V(0);
    {
        u32 old = 0;
        if (lane == 0) old = __hip_atomic_fetch_add((LAS u32*)(size_t)(FL + 48), 1u, __ATOMIC_RELAXED, __HIP_MEMORY_SCOPE_WORKGROUP);
        if (__builtin_amdgcn_readfirstlane(old) == 3 && lane < 16)
            __hip_atomic_fetch_add((GAS u32*)(ws + WS_CNO + ((layer * 16 + pm) * 16 + lane) * 128), 1u << p, RLX_AGENT);
    }
    STAMP(layer * 4 + 1, 0);
    const int mt = 2 * pm + (p >> 3), nt = p & 7;
    const u32 vo4 = 4096 + g * 128 + (lane & 7) * 16;
#define K2_LD(v, sof, aux) __builtin_bit_cast(bf16x8, __builtin_amdgcn_raw_buffer_load_b128(rs, v, sof, aux))
    bf16x8 fw[6][5], fa[18];
    f32x4 ac[5];
    {
        const u32 xo = WS_X + 2 * layer * XB + (4 * mt + wid) * 18432 + 2304 * nt;
        const u32x4 r0 = __builtin_amdgcn_raw_buffer_load_b128(rs, vo, xo, 0), r1 = __builtin_amdgcn_raw_buffer_load_b128(rs, vo + 1024, xo, 0);
        const u32x2 r2 = __builtin_amdgcn_raw_buffer_load_b64(rs, 2048 + c * 16 + (g & 1) * 8, xo, 0);
        ac[0] = (f32x4){bflo(r0[0]), bfhi(r0[0]), bflo(r0[1]), bfhi(r0[1])}; ac[1] = (f32x4){bflo(r0[2]), bfhi(r0[2]), bflo(r0[3]), bfhi(r0[3])};
        ac[2] = (f32x4){bflo(r1[0]), bfhi(r1[0]), bflo(r1[1]), bfhi(r1[1])}; ac[3] = (f32x4){bflo(r1[2]), bfhi(r1[2]), bflo(r1[3]), bfhi(r1[3])};
        ac[4] = (f32x4){bflo(r2[0]), bfhi(r2[0]), bflo(r2[1]), bfhi(r2[1])};
    }
    u32 wl[2] = {lds + vo, lds + vo + 41472}, wl4[2] = {lds + vo4, lds + vo4 + 41472};
    PINV(wl[0]); PINV(wl[1]); PINV(wl4[0]); PINV(wl4[1]);
#define K2_W(s, n) ((n) < 4 ? LDSR(wl[(s) / 9], ((s) % 9) * 4608 + (n) * 1024) : LDSR(wl4[(s) / 9], ((s) % 9) * 4608))
    lds_wait(FL, 13u);
#pragma unroll
    for (int sl = 0; sl < 6; ++sl)
#pragma unroll
        for (int n = 0; n < 5; ++n) { fw[sl][n] = K2_W(sl, n); SB(); }
#pragma unroll
    for (int n = 0; n < 5; ++n) PINV(ac[n]);
    lds_wait(FL + 52, 1u);
    STAMP(layer * 4 + 1, 1);
#pragma unroll
    for (int sl = 0; sl < 18; ++sl) { fa[sl] = K2_LD(vo + (sl & 3) * 1024, WS_O + LSH * layer * XB + ((4 * mt + wid) * 18 + (sl & ~3)) * 1024, 16); SB(); }
#pragma unroll
    for (int sl = 0; sl < 18; ++sl)
#pragma unroll
        for (int n = 0; n < 5; ++n) {
            MFMA(ac[n], fw[sl % 6][n], fa[sl]);
            SB();
            if (sl + 6 < 18) fw[sl % 6][n] = K2_W(sl + 6, n);
            SB();
        }
    asm volatile("s_nop 15" ::: "memory");
    STAMP(layer * 4 + 1, 3);
    proj_store(ac, ws, nullptr, 2 * layer + 1, mt, nt, wid, lane);
    STAMP(layer * 4 + 1, 2);
    STAMP_OUT(layer * 4 + 1);
}

constexpr u32 STAGE = 18432, NS = 6;
constexpr u32 K3_PK = NS * STAGE, K3_PP = 304, K3_WS = K3_PK + 128 * K3_PP, K3_S1 = K3_WS + 4096, K3_S2 = K3_S1 + 2048, K3_CS = K3_S2 + 1024, K3_FL = K3_CS + 1280, K3_LDS = K3_FL + 128;

__global__ __launch_bounds__(512, 2) void k34_mlp(char* __restrict__ ws, const u16* __restrict__ wsp, const u16* __restrict__ bsp, u16* __restrict__ out, int layer) {
    extern __shared__ __attribute__((aligned(16))) char shm_[];
    const u32 lds = (u32)(size_t)(LAS char*)shm_;
    const int tid = threadIdx.x, wid = __builtin_amdgcn_readfirstlane(tid >> 6), lane = tid & 63, c = lane & 15, g = lane >> 4;
    const int xcd = blockIdx.x & 7, bi = blockIdx.x >> 3;
    const int pm = MAP3 ? (xcd & 3) * 4 + (bi & 3) : (xcd & 1) * 8 + (bi & 7), pn = MAP3 ? (xcd >> 2) * 8 + (bi >> 2) : (xcd >> 1) * 4 + (bi >> 3);
    const u32 FL = lds + K3_FL;
    STAMP_DECL;
    const auto rs = __builtin_amdgcn_make_buffer_rsrc(ws, 0, WS_END, 0x00020000);
    if (wid == 4) {
        u32 ao[16];
        const u32 vo = lane * 16;
#pragma unroll
        for (int q = 0; q < 16; ++q) { ao[q] = vo + (q >> 1) * 18432 - (q & 2) * 1024 + 4096; PINV(ao[q]); }
        const char* ap = ws + WS_X + (2 * layer + 1) * XB + 8 * pm * 18432 - 4096;
        const char* xp = ws + WS_WIN + (144 + 9 * pn + 8) * 18432;
        u32 slot = 0;
#define K3_ISSUE() do { const u32 dst = lds + slot * STAGE;                                                          \
            DMA4(ao[0], ao[1], ao[2], ao[3], ap, dst); DMA4(ao[4], ao[5], ao[6], ao[7], ap, dst + 4096);             \
            DMA4(ao[8], ao[9], ao[10], ao[11], ap, dst + 8192); DMA4(ao[12], ao[13], ao[14], ao[15], ap, dst + 12288); } while (0)
#define K3_SEG(s0, s1, GATE) for (int s = (s0); s < (s1) + 3; ++s) {                                                 \
            if (s >= (s0) + 3) {                                                                                     \
                if (s <= (s1)) WAIT_V(36); else if (s == (s1) + 1) WAIT_V(18); else WAIT_V(0);                       \
                stsw(FL, s - 2); FENCE(); }                                                                          \
            if (s == 1) BAR();                                                                                       \
            if (s == 2) { WAIT_V(18); stsw(FL, 1u); FENCE(); }                                                       \
            if (s < (s1)) {                                                                                          \
                if (s >= (int)NS) lds_wait_sleep(FL + 16 + (lane & 3) * 4, GATE);                                    \
                if (s == 9) { ap -= 9 * 2048; xp = ws + WS_WIN + (9 * pn + 8) * 18432; }                             \
                K3_ISSUE();                                                                                          \
                DMA2(vo, vo, xp, lds + slot * STAGE + 16384);                                                        \
                xp += 2048; ap += 2048;                                                                              \
                slot = slot + 1 == NS ? 0 : slot + 1; } }
        K3_SEG(0, ZSPLIT, s - NS + 1);
        K3_SEG(ZSPLIT, 18, s >= 15 ? LGATE : max(ZGATE, s - (int)NS + 1));
#pragma unroll
        for (int q = 0; q < 16; ++q) { ao[q] = (q * 8 + (lane >> 3)) * 4096 + (((lane & 7) ^ ((q & 1) * 4 + g)) * 16) - (q & 3) * 1024 + 4096; PINV(ao[q]); }
        ap = (const char*)wsp + (size_t)pm * 128 * 4096 - 4096;
        lds_wait_sleep(FL + 16 + (lane & 3) * 4, LGATE2);
        for (int s = 18; s < 53; ++s) {
            if (s >= 21) {
                if (s <= 50) WAIT_V(32); else if (s == 51) WAIT_V(16); else WAIT_V(0);
                stsw(FL, s - 2);
                FENCE();
            }
            if (s < 50) {
                lds_wait_sleep(FL + 16 + (lane & 3) * 4, s - NS + 1);
                K3_ISSUE();
                ap += AFAKE ? 0 : (s == 33 ? 128 - 15 * 256 : 256);
                slot = slot + 1 == NS ? 0 : slot + 1;
            }
        }
        lds_wait_sleep(FL + 80, 2u);
        for (int i = 0; i < 8; ++i) {
            const int r = i < 2 ? 6 + i : (i < 6 ? i : i - 6);
            if (i >= 2) lds_wait_sleep(FL + 16 + (lane & 3) * 4, 43 + i);
            if (WGATE && i == WGI) lds_wait_sleep(FL + 92, WGATE);
            const char* wp = ws + WS_WOUT + ((MAP5 ? pn & 7 : pn >> 1) * 72 + 18 * (r >> 1) + 4 * (r & 1)) * 4608;
            const u32 dst = lds + r * STAGE;
            DMA4(vo, vo, vo, vo, wp, dst); DMA4(vo, vo, vo, vo, wp + 4096, dst + 4096); DMA4(vo, vo, vo, vo, wp + 8192, dst + 8192);
            DMA4(vo, vo, vo, vo, wp + 12288, dst + 12288); DMA2(vo, vo, wp + 16384, dst + 16384);
            if (i >= 2) WAIT_V(36);
        }
        WAIT_V(0);
        stsw(FL, 100u);
        return;
    }
    if (wid > 4) {
        const int ht = tid - 320;
        if (ht >= 128 && ht < 160) stsw(FL + (ht - 128) * 4, ht == 129 ? 18u : 0u);
        WAIT_L0();
        BAR();
        if (wid != 7) {
            float mean, var;
            x_stats(ws, 2 * layer + 1, 128 * pm + ht, mean, var);
            const float r = __builtin_amdgcn_rsqf(var + 1e-5f);
            f32x4 o = {-mean, -2.302208198f * r, -0.1029432397f * r * r * r, (var + 1e-5f) * r};
            *(LAS f32x4*)(size_t)(lds + K3_S1 + ht * 16) = o;
        } else {
            const int i = min(lane, 35);
            const f32x4* cp = (const f32x4*)(ws + WS_CSIN) + 36 * pn + i;
            const f32x4 a0 = cp[576], a1 = cp[576 + 1152], b0 = cp[0], b1 = cp[1152];
            *(LAS f32x4*)(size_t)(lds + K3_CS + i * 16) = a0 + a1;
            *(LAS f32x4*)(size_t)(lds + K3_CS + 640 + i * 16) = b0 + b1;
        }
        FENCE();
        stsw(FL + 32 + (wid - 5) * 4, 1u);
        u32 vo = lane * 16;
        PINV(vo);
        GAS u32* flg = (GAS u32*)(ws + WS_CNT + (layer * 16 + pn) * 4096);
        HST_DECL;
        if (wid != 5) {
            const int h = wid - 6;
            u32 ta = lds + K3_PK + (32 * h + 8 * g + (c >> 2)) * K3_PP + 16 * (c & 3), tx = ta + 256 - 8 * (c & 3), s2r = lds + K3_S2 + 128 * h + 32 * g;
            PINV(ta); PINV(tx); PINV(s2r);
            const u32 zob = WS_Z + LSH * layer * ZB + (9 * pn * 64 + 2 * pm + h) * 1024;
            lds_wait_sleep(FL + 48 + (lane & 3) * 4, 1u);
            HST(0);
            if (pm < HPRIO) __builtin_amdgcn_s_setprio(3);
            float zf[18][8];
#define K3_TR(fi, A, O) do { u32x2 t0_, t1_, t2_, t3_;                                                               \
                asm volatile("ds_read_b64_tr_b16 %0, %4 offset:%5\n\tds_read_b64_tr_b16 %1, %4 offset:%6\n\tds_read_b64_tr_b16 %2, %4 offset:%7\n\t" \
                             "ds_read_b64_tr_b16 %3, %4 offset:%8\n\ts_waitcnt lgkmcnt(0)" : "=&v"(t0_), "=&v"(t1_), "=&v"(t2_), "=&v"(t3_) \
                             : "v"(A), "i"(O), "i"((O) + 4 * K3_PP), "i"((O) + 64 * K3_PP), "i"((O) + 68 * K3_PP) : "memory"); \
                const u32 w_[8] = {t0_[0], t0_[1], t1_[0], t1_[1], t2_[0], t2_[1], t3_[0], t3_[1]};                  \
                _Pragma("unroll") for (int i_ = 0; i_ < 8; ++i_) { zf[2 * (fi) + (i_ >> 2)][2 * (i_ & 3)] = bflo(w_[i_]); zf[2 * (fi) + (i_ >> 2)][2 * (i_ & 3) + 1] = bfhi(w_[i_]); \
                    PINV(zf[2 * (fi) + (i_ >> 2)][2 * (i_ & 3)]); PINV(zf[2 * (fi) + (i_ >> 2)][2 * (i_ & 3) + 1]); } } while (0)
            K3_TR(0, ta, 0); K3_TR(1, ta, 8); K3_TR(2, ta, 64); K3_TR(3, ta, 72); K3_TR(4, ta, 128); K3_TR(5, ta, 136); K3_TR(6, ta, 192); K3_TR(7, ta, 200);
            K3_TR(8, tx, 0);
            HST(1);
            lds_wait_sleep(FL + 64, 1u);
            for (int i = 2; i < pm; ++i) __builtin_amdgcn_s_sleep(STAG);
            HST(2);
            f32x4 sv[2][4];
#pragma unroll
            for (int q = 0; q < 2; ++q)
#pragma unroll
                for (int k = 0; k < 4; ++k) sv[q][k] = *(LAS f32x4*)(size_t)(s2r + (k >> 1) * 512 + 256 * q + (k & 1) * 16);
#pragma unroll
            for (int ii = 0; ii < 18; ++ii) {
                const int q = ii / 9, i = 2 * (ii % 9) + q;
                u32x4 o;
                asm("v_fma_f32 %4, %4, %12, %20\n\tv_fma_f32 %5, %5, %13, %21\n\tv_fma_f32 %6, %6, %14, %22\n\tv_fma_f32 %7, %7, %15, %23\n\t"
                    "v_fma_f32 %8, %8, %16, %24\n\tv_fma_f32 %9, %9, %17, %25\n\tv_fma_f32 %10, %10, %18, %26\n\tv_fma_f32 %11, %11, %19, %27\n\t"
                    "v_cvt_pk_bf16_f32 %0, %4, %5\n\tv_cvt_pk_bf16_f32 %1, %6, %7\n\tv_cvt_pk_bf16_f32 %2, %8, %9\n\tv_cvt_pk_bf16_f32 %3, %10, %11"
                    : "=&v"(o[0]), "=&v"(o[1]), "=&v"(o[2]), "=&v"(o[3]), "+v"(zf[i][0]), "+v"(zf[i][1]), "+v"(zf[i][2]), "+v"(zf[i][3]), "+v"(zf[i][4]), "+v"(zf[i][5]), "+v"(zf[i][6]), "+v"(zf[i][7])
                    : "v"(sv[q][0][0]), "v"(sv[q][0][1]), "v"(sv[q][0][2]), "v"(sv[q][0][3]), "v"(sv[q][1][0]), "v"(sv[q][1][1]), "v"(sv[q][1][2]), "v"(sv[q][1][3]),
                      "v"(sv[q][2][0]), "v"(sv[q][2][1]), "v"(sv[q][2][2]), "v"(sv[q][2][3]), "v"(sv[q][3][0]), "v"(sv[q][3][1]), "v"(sv[q][3][2]), "v"(sv[q][3][3]));
                __builtin_amdgcn_raw_buffer_store_b128(o, rs, vo, zob + q * 32768 + (i >> 1) * 65536, 16);
                SB();
                if (ii % 9 == 8) {
                    if (q == 0) HST(3);
                    WAIT_V(0);
                    if (q == 0) HST(4);
                    u32 old = 0;
                    if (lane == 0) old = __hip_atomic_fetch_add((LAS u32*)(size_t)(FL + 76 + 4 * q), 1u, __ATOMIC_RELAXED, __HIP_MEMORY_SCOPE_WORKGROUP);
                    if (__builtin_amdgcn_readfirstlane(old) == 1 && lane < 16)
                        __hip_atomic_fetch_add(flg + lane * 64, 1u << (16 * q + pm), RLX_AGENT);
                    if (q == 0) { __builtin_amdgcn_s_setprio(0); __builtin_amdgcn_s_sleep(STAG2); }
                    SB();
                }
            }
            HST(5);
            if (wid == 6) HST_OUT(9 + layer);
            return;
        }
        u32 wsa = lds + K3_WS + lane * 16, s2w = lds + K3_S2 + lane * 8;
        PINV(wsa); PINV(s2w);
        const u32 zsb = WS_ZST + (layer * 16 + pm) * 16384;
        lds_wait_sleep(FL + 16 + (lane & 3) * 4, 9u);
        __builtin_amdgcn_s_sleep(POLLD);
        __builtin_amdgcn_s_setprio(3);
        lds_wait(FL + 48 + (lane & 3) * 4, 1u);
        STAMP(layer * 4 + 2, 8);
        {
            const f32x4 st = sadd4(sadd4(*(LAS f32x4*)(size_t)wsa, *(LAS f32x4*)(size_t)(wsa + 1024)), sadd4(*(LAS f32x4*)(size_t)(wsa + 2048), *(LAS f32x4*)(size_t)(wsa + 3072)));
            u32x4 o = __builtin_bit_cast(u32x4, st);
#pragma unroll
            for (int k = 0; k < 4; ++k) o[k] = o[k] == SENT ? 0x7fc00000u : o[k];
            BST128(o, rs, vo, zsb + pn * 1024, 16);
            __builtin_amdgcn_s_sleep(XDELAY3);
            u32x4 t[16];
#pragma unroll
            for (int k = 0; k < 16; ++k) t[k] = __builtin_amdgcn_raw_buffer_load_b128(rs, vo + (k & 3) * 1024, zsb + (k >> 2) * 4096, 16);
            u32 mx = 0;
#pragma unroll
            for (int k = 0; k < 16; ++k) mx = max(max(mx, max(t[k][0], t[k][1])), max(t[k][2], t[k][3]));
            u32 spins = 0;
            if (__builtin_expect(ANY(mx == SENT), 0)) {
#pragma nounroll
                for (spins = 1; spins < SPIN_CAP; ++spins) {
                    FENCE();
                    u32 miss = 0;
#pragma unroll
                    for (int k = 0; k < 16; ++k) miss |= ANY(max(max(t[k][0], t[k][1]), max(t[k][2], t[k][3])) == SENT) ? 1u << k : 0u;
                    if (!miss) break;
#pragma unroll
                    for (int k = 0; k < 16; ++k) if (miss >> k & 1) t[k] = __builtin_amdgcn_raw_buffer_load_b128(rs, vo + (k & 3) * 1024, zsb + (k >> 2) * 4096, 16);
                }
            }
            f32x4 sa[4];
#pragma unroll
            for (int k = 0; k < 4; ++k)
                sa[k] = sadd4(sadd4(__builtin_bit_cast(f32x4, t[4 * k]), __builtin_bit_cast(f32x4, t[4 * k + 1])), sadd4(__builtin_bit_cast(f32x4, t[4 * k + 2]), __builtin_bit_cast(f32x4, t[4 * k + 3])));
            const f32x4 ss = sadd4(sadd4(sa[0], sa[1]), sadd4(sa[2], sa[3]));
            f32x2 rr, mr;
#pragma unroll
            for (int k = 0; k < 2; ++k) {
                const float m2 = sfma(ss[2 * k], 1.f / 2304.f, 0.f);
                float v2 = fmaxf(sfma(-m2, m2, sfma(ss[2 * k + 1], 1.f / 2304.f, 0.f)), 0.f) + 1e-5f;
                PINV(v2);
                rr[k] = __builtin_amdgcn_rsqf(v2);
                float t2 = -m2 * rr[k];
                PINV(t2);
                mr[k] = t2;
            }
            *(LAS f32x2*)(size_t)s2w = rr;
            *(LAS f32x2*)(size_t)(s2w + 512) = mr;
            FENCE();
            stsw(FL + 64, 1u);
            FENCE();
        }
        STAMP(layer * 4 + 2, 9);
        STAMP(layer * 4 + 2, 10);
        u32 ready = 0;
#define K3_NEED(n) do { _Pragma("nounroll") for (u32 spins_ = 0; ready < (u32)(n) && spins_ < SPIN_CAP; ++spins_) {   \
            const u32 v_ = __builtin_amdgcn_readfirstlane(__hip_atomic_load(flg + pm * 64, RLX_AGENT));                                               \
            ready = __builtin_ctzll(~(u64)v_); } FENCE(); } while (0)
        __builtin_amdgcn_s_sleep(FDELAY);
        K3_NEED(3);
        stsw(FL + 84, 1u);
        FENCE();
        STAMP(layer * 4 + 2, 11);
        STAMP_OUT(layer * 4 + 2);
        __builtin_amdgcn_s_setprio(0);
        const u32 zx = WS_Z + LSH * layer * ZB + (9 * pn + 8) * 65536;
        u32x4 xr[4][2];
#pragma unroll
        for (int k = 0; k < 3; ++k) {
            xr[k][0] = __builtin_amdgcn_raw_buffer_load_b128(rs, vo, zx + k * 2048, 16);
            xr[k][1] = __builtin_amdgcn_raw_buffer_load_b128(rs, vo + 1024, zx + k * 2048, 16);
            SB();
        }
        u32 slot = 0;
        for (int s0 = 0; s0 < 32; s0 += 4) {
#pragma unroll
            for (int k = 0; k < 4; ++k) {
                const int s = s0 + k;
                lds_wait_sleep(FL + 16 + (lane & 3) * 4, 18 + s - NS + 1);
                *(LAS u32x4*)(size_t)(lds + slot * STAGE + 16384 + lane * 16) = xr[k][0];
                *(LAS u32x4*)(size_t)(lds + slot * STAGE + 17408 + lane * 16) = xr[k][1];
                FENCE();
                stsw(FL + 4, 18 + s + 1);
                FENCE();
                const u32 sn = s + 3 < 32 ? s + 3 : 31;
                K3_NEED(sn + 1);
                xr[(k + 3) & 3][0] = __builtin_amdgcn_raw_buffer_load_b128(rs, vo, zx + sn * 2048, 16);
                xr[(k + 3) & 3][1] = __builtin_amdgcn_raw_buffer_load_b128(rs, vo + 1024, zx + sn * 2048, 16);
                slot = slot + 1 == NS ? 0 : slot + 1;
            }
        }
        lds_wait_sleep(FL + 92, 4u);
        __builtin_amdgcn_s_sleep(HDELAY);
        GAS u32* hm = (GAS u32*)(ws + WS_CNT + (layer * 16 + pm) * 4096 + pn * 256 + 128);
#pragma nounroll
        for (u32 spins = 0; spins < SPIN_CAP; ++spins) {
            const u32 v = __builtin_amdgcn_readfirstlane(__hip_atomic_load(hm, RLX_AGENT));
            FENCE();
            stsw(FL + 88, v);
            if (v == 0xffffu) break;
        }
        return;
    }
    STAMP(layer * 4 + 2, 0);
    u32 vA = lane * 16, vB = lane * 16 + 18432;
    u32 so = WS_WIN + (144 + 9 * pn + 2 * wid) * 18432;
    bf16x8 bb[6][2];
#define BLD(voff, imm, aux) __builtin_bit_cast(bf16x8, __builtin_amdgcn_raw_buffer_load_b128(rs, (voff) + (imm), so, aux))
#define BPRO(aux) do { _Pragma("unroll") for (int r = 0; r < 5; ++r) { if (r == 3) so += 3072;                       \
        bb[r][0] = BLD(vA, (r % 3) * 1024, aux); SB(); bb[r][1] = BLD(vB, (r % 3) * 1024, aux); SB(); } } while (0)
    BPRO(0);
    u32 pa[2][4];
#pragma unroll
    for (int q = 0; q < 4; ++q) { pa[0][q] = lds + lane * 16 + ((q ^ wid) * 4096); pa[1][q] = pa[0][q] + 1024; }
    u32 px = lds + 16384 + lane * 16;
    u32 myfree = FL + 16 + wid * 4, fladdr = FL + (lane & 1) * 4, cnt = 0;
    PINV(cnt); PINV(myfree); PINV(fladdr);
    int dstep = 3 * STAGE;
    f32x4 acc[8][2], accx[2];
    bf16x8 a[8], xf;
#define ALOAD() do { a[0] = LDSR(pa[0][0], 0); SB(); xf = LDSR(px, 0); SB();                                         \
        _Pragma("unroll") for (int m = 1; m < 8; ++m) { a[m] = LDSR(pa[0][m >> 1], (m & 1) * 2048); SB(); } } while (0)
#define MFS(acc, x, y) do { MFMA(acc, x, y); SB(); } while (0)
#define HALF(r, RD, kn, sf, rl, imm, aux, E0, E1, E2, E3, E4, E5) do {                                               \
        WCNT(8, 5); MFS(acc[0][0], bb[r][0], a[0]); E0; SB();                                                       \
        MFS(acc[0][1], bb[r][1], a[0]); E1; SB();                                                                   \
        MFS(accx[0], xf, a[0]); if (RD) a[0] = LDSR(pa[kn][0], (sf)); SB();                                         \
        MFS(accx[1], xf, a[1]); if (RD) xf = LDSR(px, (sf) + (kn) * 1024); SB();                                    \
        MFS(acc[1][1], bb[r][1], a[1]); bb[rl][0] = BLD(vA, imm, aux); SB();                                        \
        MFS(acc[1][0], bb[r][0], a[1]); if (RD) a[1] = LDSR(pa[kn][0], (sf) + 2048); SB();                          \
        MFS(acc[2][0], bb[r][0], a[2]); E2; SB();                                                                   \
        MFS(acc[2][1], bb[r][1], a[2]); if (RD) a[2] = LDSR(pa[kn][1], (sf)); SB();                                 \
        WCNT(63, 5); MFS(acc[3][1], bb[r][1], a[3]); SB();                                                          \
        MFS(acc[3][0], bb[r][0], a[3]); if (RD) a[3] = LDSR(pa[kn][1], (sf) + 2048); SB();                          \
        MFS(acc[4][0], bb[r][0], a[4]); bb[rl][1] = BLD(vB, imm, aux); SB();                                        \
        MFS(acc[4][1], bb[r][1], a[4]); if (RD) a[4] = LDSR(pa[kn][2], (sf)); SB();                                 \
        MFS(acc[5][1], bb[r][1], a[5]); E3; SB();                                                                   \
        MFS(acc[5][0], bb[r][0], a[5]); if (RD) a[5] = LDSR(pa[kn][2], (sf) + 2048); SB();                          \
        MFS(acc[6][0], bb[r][0], a[6]); E4; SB();                                                                   \
        MFS(acc[6][1], bb[r][1], a[6]); if (RD) a[6] = LDSR(pa[kn][3], (sf)); SB();                                 \
        MFS(acc[7][1], bb[r][1], a[7]); E5; SB();                                                                   \
        MFS(acc[7][0], bb[r][0], a[7]); if (RD) a[7] = LDSR(pa[kn][3], (sf) + 2048); SB(); } while (0)
#define STEP(u, chk, nso, aux) do { u32 fl_; u64 msk_;                                                               \
        HALF(2 * (u), 1, 1, (u) * STAGE, (2 * (u) + 5) % 6, ((2 * (u) + 5) % 3) * 1024, aux,                         \
             { if ((u) == 2) so += 3072; }, { cnt += 1; PINV(cnt); }, { FENCE(); fl_ = ldsw(fladdr); FENCE(); },     \
             { if ((u) == 2) { pa[0][0] += dstep; pa[0][1] += dstep; PINV(pa[0][0]); PINV(pa[0][1]); } },            \
             { if ((u) == 2) { pa[0][2] += dstep; pa[0][3] += dstep; PINV(pa[0][2]); PINV(pa[0][3]); } },            \
             { asm volatile("v_cmp_le_u32 %0, %1, %2" : "=s"(msk_) : "v"(fl_), "v"(cnt)); if ((u) == 2) { px += dstep; PINV(px); } }); \
        HALF(2 * (u) + 1, 1, 0, (((u) + 1) % 3) * STAGE, (2 * (u) + 6) % 6, ((2 * (u) + 6) % 3) * 1024, aux,         \
             { if (__builtin_expect(msk_ != 0, 0)) { if (chk) lds_wait_gt(fladdr, cnt); } FENCE(); }, { if ((u) == 0) so = (nso); }, \
             { FENCE(); stsw(myfree, cnt); FENCE(); },                                                               \
             { if ((u) == 2) { pa[1][0] += dstep; pa[1][1] += dstep; PINV(pa[1][0]); PINV(pa[1][1]); } },            \
             { if ((u) == 2) { pa[1][2] += dstep; pa[1][3] += dstep; PINV(pa[1][2]); PINV(pa[1][3]); } },            \
             { if ((u) == 2) dstep = -dstep; }); } while (0)
#define PINACC() do { _Pragma("unroll") for (int m = 0; m < 8; ++m) { PINV(acc[m][0]); PINV(acc[m][1]); } PINV(accx[0]); PINV(accx[1]); asm volatile("s_nop 1"); } while (0)
    BAR();
    u32 yp[18][2], early = 0;
    const u32 s1a = lds + K3_S1 + c * 16;
#pragma unroll
    for (int ph = 0; ph < 2; ++ph) {
        if (ph == 0) {
#pragma unroll
            for (int m = 0; m < 8; ++m) { acc[m][0] = acc[m][1] = (f32x4){0.f, 0.f, 0.f, 0.f}; if (m < 2) accx[m] = acc[m][0]; }
        } else {
            const u32 ca = lds + K3_CS + ph * 640 + g * 16;
            const f32x4 c0 = *(LAS f32x4*)(size_t)(ca + wid * 128), c1 = *(LAS f32x4*)(size_t)(ca + wid * 128 + 64), c2 = *(LAS f32x4*)(size_t)(ca + 512);
#pragma unroll
            for (int m = 0; m < 8; ++m) {
                const float nm = *(LAS float*)(size_t)(s1a + ((((m >> 1) ^ wid) << 1) + (m & 1)) * 256);
                acc[m][0] = c0 * nm; acc[m][1] = c1 * nm;
                if (m < 2) accx[m] = c2 * nm;
            }
        }
        PINACC();
        if (ph == 0) { lds_wait(fladdr, 1u); ALOAD(); STAMP(layer * 4 + 2, 1); }
        const u32 nxt = WS_WIN + (9 * pn + 2 * wid) * 18432;
        for (int j = 0; j < 3; ++j) {
            STEP(0, true, j == 2 ? nxt : so + 3072, 0);
            STEP(1, true, 0, 0);
            STEP(2, ph == 0 || j < 2, 0, 0);
        }
        asm volatile("s_nop 15" ::: "memory");
        STAMP(layer * 4 + 2, 2 + 2 * ph);
        if (ph == 1) {
            vB = lane * 16 + 65536;
            so = WS_Z + LSH * layer * ZB + (9 * pn + 2 * wid) * 65536;
            FENCE();
            early = __builtin_amdgcn_readfirstlane(ldsw(FL + 84));
            FENCE();
            if (early) BPRO(16);
        }
        float t1[8], t2[8];
        f32x4 rcs[8], cz[3];
        if (ph == 0) {
            lds_wait(FL + 32 + (lane % 3) * 4, 1u);
            const u32 ca = lds + K3_CS + g * 16;
            cz[0] = *(LAS f32x4*)(size_t)(ca + wid * 128); cz[1] = *(LAS f32x4*)(size_t)(ca + wid * 128 + 64); cz[2] = *(LAS f32x4*)(size_t)(ca + 512);
        }
#pragma unroll
        for (int m = 0; m < 8; ++m) rcs[m] = *(LAS f32x4*)(size_t)(s1a + ((((m >> 1) ^ wid) << 1) + (m & 1)) * 256);
#pragma unroll
        for (int m = 0; m < 8; ++m) {
            if (ph == 1 && m == 4 && !early) { FENCE(); early = __builtin_amdgcn_readfirstlane(ldsw(FL + 84)); FENCE(); if (early) BPRO(16); }
            constexpr int NP = 6;
            const int np = m < 2 ? 6 : 4;
            const f32x2 k1 = {rcs[m][1], rcs[m][1]}, k3 = {rcs[m][2], rcs[m][2]}, sg = {rcs[m][3], rcs[m][3]};
            f32x2 v[NP], e[NP];
#pragma unroll
            for (int i = 0; i < NP; ++i) if (i < np) {
                const f32x4 av = i < 4 ? acc[m][i >> 1] : accx[m & 1];
                v[i] = (f32x2){av[2 * (i & 1)], av[2 * (i & 1) + 1]};
                if (ph == 0) v[i] += (f32x2){cz[i >> 1][2 * (i & 1)], cz[i >> 1][2 * (i & 1) + 1]} * (f32x2){rcs[m][0], rcs[m][0]};
            }
#pragma unroll
            for (int i = 0; i < NP; ++i) if (i < np) e[i] = v[i] * v[i];
#pragma unroll
            for (int i = 0; i < NP; ++i) if (i < np) e[i] = e[i] * k3 + k1;
#pragma unroll
            for (int i = 0; i < NP; ++i) if (i < np) e[i] = e[i] * v[i];
            SB();
#pragma unroll
            for (int i = 0; i < NP; ++i) if (i < np) { e[i][0] = __builtin_amdgcn_exp2f(e[i][0]); e[i][1] = __builtin_amdgcn_exp2f(e[i][1]); }
            SB();
#pragma unroll
            for (int i = 0; i < NP; ++i) if (i < np) e[i] = e[i] * sg + sg;
            SB();
#pragma unroll
            for (int i = 0; i < NP; ++i) if (i < np) { e[i][0] = __builtin_amdgcn_rcpf(e[i][0]); e[i][1] = __builtin_amdgcn_rcpf(e[i][1]); }
            SB();
#pragma unroll
            for (int i = 0; i < NP; ++i) if (i < np) v[i] = v[i] * e[i];
#pragma unroll
            for (int i = 0; i < NP; ++i) if (i < np) yp[i < 4 ? 2 * m + (i >> 1) : 16 + m][i & 1] = pk2(v[i][0], v[i][1]);
            if (ph == 0) {
                f32x2 u1 = (v[0] + v[1]) + (v[2] + v[3]), u2 = v[0] * v[0], u3 = v[1] * v[1];
                u2 = v[2] * v[2] + u2; u3 = v[3] * v[3] + u3;
                if (m < 2) { u1 += v[4] + v[5]; u2 = v[4] * v[4] + u2; u3 = v[5] * v[5] + u3; }
                u2 += u3;
                t1[m] = u1[0] + u1[1]; t2[m] = u2[0] + u2[1];
            }
            PINV(yp[2 * m][0]); PINV(yp[2 * m][1]); PINV(yp[2 * m + 1][0]); PINV(yp[2 * m + 1][1]);
            if (m < 2) { PINV(yp[16 + m][0]); PINV(yp[16 + m][1]); }
            if (ph == 0) {
                const u32 row = (((((m >> 1) ^ wid) << 1) + (m & 1)) << 4) + c;
                u32x4 o = {yp[2 * m][0], yp[2 * m][1], yp[2 * m + 1][0], yp[2 * m + 1][1]};
                *(LAS u32x4*)(size_t)(lds + K3_PK + row * K3_PP + 64 * wid + 16 * g) = o;
                if (m < 2) { u32x2 ox = {yp[16 + m][0], yp[16 + m][1]}; *(LAS u32x2*)(size_t)(lds + K3_PK + row * K3_PP + 256 + 8 * g) = ox; }
            }
        }
        if (ph == 0) {
#pragma unroll
            for (int m = 0; m < 4; ++m) {
                auto r1 = __builtin_amdgcn_permlane32_swap(F2U(t1[m]), F2U(t1[m + 4]), false, false);
                auto r2 = __builtin_amdgcn_permlane32_swap(F2U(t2[m]), F2U(t2[m + 4]), false, false);
                t1[m] = U2F(r1[0]) + U2F(r1[1]); t2[m] = U2F(r2[0]) + U2F(r2[1]);
            }
#pragma unroll
            for (int m = 0; m < 2; ++m) {
                auto r1 = __builtin_amdgcn_permlane16_swap(F2U(t1[m]), F2U(t1[m + 2]), false, false);
                auto r2 = __builtin_amdgcn_permlane16_swap(F2U(t2[m]), F2U(t2[m + 2]), false, false);
                f32x2 o = {U2F(r1[0]) + U2F(r1[1]), U2F(r2[0]) + U2F(r2[1])};
                *(LAS f32x2*)(size_t)(lds + K3_WS + (wid * 128 + 32 * (g ^ wid) + 16 * m + c) * 8) = o;
            }
            FENCE();
            stsw(FL + 48 + wid * 4, 1u);
            FENCE();
        }
        STAMP(layer * 4 + 2, 3 + 2 * ph);
    }
    {
        float bv[8];
#pragma unroll
        for (int m = 0; m < 8; ++m) bv[m] = bflo(bsp[128 * pm + (((((m >> 1) ^ wid) << 1) + (m & 1)) << 4) + c]);
#pragma unroll
        for (int m = 0; m < 8; ++m) {
            acc[m][0] = acc[m][1] = (f32x4){bv[m], bv[m], bv[m], bv[m]};
            if (m < 2) accx[m] = acc[m][0];
        }
    }
#pragma unroll
    for (int q = 0; q < 4; ++q) { pa[0][q] = lds + (c >> 3) * 1024 + (c & 7) * 128 + ((g ^ ((c >> 1) & 7)) * 16) + ((q ^ wid) * 4096); pa[1][q] = pa[0][q] ^ 64; }
    if (!early) { lds_wait(FL + 84, 1u); BPRO(16); }
    lds_wait(fladdr, 19u);
    STAMP(layer * 4 + 2, 6);
    ALOAD();
    PINACC();
    for (int j = 0; j < 10; ++j) {
        STEP(0, true, so + BADV, BAUX);
        STEP(1, true, 0, BAUX);
        STEP(2, true, 0, BAUX);
    }
    if (BADV > 0) so -= 8192;
    STEP(0, true, so, 16);
    STEP(1, false, 0, 16);
    asm volatile("s_nop 15" ::: "memory");
    STAMP(layer * 4 + 2, 7);
    {
        const u32 hb = WS_H + LSH * layer * ZB + (8 * pm * 8 + (pn >> 1)) * 9216;
#pragma unroll
        for (int m = 0; m < 8; ++m) {
            const int mt = (((m >> 1) ^ wid) << 1) + (m & 1);
            u32x4 o;
            o[0] = pk2(bflo(yp[2 * m][0]) * acc[m][0][0], bfhi(yp[2 * m][0]) * acc[m][0][1]);
            o[1] = pk2(bflo(yp[2 * m][1]) * acc[m][0][2], bfhi(yp[2 * m][1]) * acc[m][0][3]);
            o[2] = pk2(bflo(yp[2 * m + 1][0]) * acc[m][1][0], bfhi(yp[2 * m + 1][0]) * acc[m][1][1]);
            o[3] = pk2(bflo(yp[2 * m + 1][1]) * acc[m][1][2], bfhi(yp[2 * m + 1][1]) * acc[m][1][3]);
            BST128(o, rs, vA, hb + mt * 73728 + ((pn & 1) * 4 + wid) * 1024, 16);
            if (m < 2) {
                u32x2 ox = {pk2(bflo(yp[16 + m][0]) * accx[m][0], bfhi(yp[16 + m][0]) * accx[m][1]), pk2(bflo(yp[16 + m][1]) * accx[m][2], bfhi(yp[16 + m][1]) * accx[m][3])};
                __builtin_amdgcn_raw_buffer_store_b64(ox, rs, vA, hb + mt * 73728 + 8192 + (pn & 1) * 8, 16);
            }
        }
        STAMP(layer * 4 + 2, 12);
        WAIT_V(0);
        u32 old = 0;
        if (lane == 0) old = __hip_atomic_fetch_add((LAS u32*)(size_t)(FL + 92), 1u, __ATOMIC_RELAXED, __HIP_MEMORY_SCOPE_WORKGROUP);
        if (__builtin_amdgcn_readfirstlane(old) == 3 && lane < 16)
            __hip_atomic_fetch_add((GAS u32*)(ws + WS_CNT + (layer * 16 + pm) * 4096 + lane * 256 + 128), 1u << pn, RLX_AGENT);
    }
    STAMP_OUT(layer * 4 + 2);
    STAMP(layer * 4 + 3, 0);
    {
        const int mt = 2 * pm + (MAP5 ? pn >> 3 : pn & 1), nt = MAP5 ? pn & 7 : pn >> 1;
        const u32 wb = WS_WOUT + (nt * 72 + 18 * wid) * 4608;
        const u32 vo4 = 4096 + g * 128 + (lane & 7) * 16;
        bf16x8 fa[4][4], fw[3][5], fl[5];
        f32x4 ac[4][5];
#pragma unroll
        for (int m = 0; m < 4; ++m)
#pragma unroll
            for (int n = 0; n < 5; ++n) ac[m][n] = (f32x4){0.f, 0.f, 0.f, 0.f};
        u32 abm[4];
#pragma unroll
        for (int m = 0; m < 4; ++m) abm[m] = WS_H + LSH * layer * ZB + ((4 * mt + (m ^ wid)) * 8 + 2 * wid) * 9216;
#define K5_LD(v, sof, aux) __builtin_bit_cast(bf16x8, __builtin_amdgcn_raw_buffer_load_b128(rs, v, sof, aux))
#define K5_LW(r, s) do { _Pragma("unroll") for (int n = 0; n < 4; ++n) fw[r][n] = K5_LD(vA + n * 1024, wb + (s) * 4608, 0); fw[r][4] = K5_LD(vo4, wb + (s) * 4608, 0); SB(); } while (0)
#define K5_LL(s) do { _Pragma("unroll") for (int n = 0; n < 4; ++n) fl[n] = LDSR(wl, (s) * 4608 + n * 1024); fl[4] = LDSR(wl4, (s) * 4608); SB(); } while (0)
#define K5_LA(r, s) do { _Pragma("unroll") for (int m = 0; m < 4; ++m) fa[r][m] = K5_LD(vA + ((s) & 3) * 1024, abm[m] + ((s) & ~3) * 1024, 16); SB(); } while (0)
#define K5_MM(W, r) do { _Pragma("unroll") for (int m = 0; m < 4; ++m) _Pragma("unroll") for (int n = 0; n < 5; ++n) ac[m][n] = MFMAB(W[n], fa[r][m], ac[m][n]); SB(); } while (0)
        PINV(vA);
        const u32 wl = lds + wid * 2 * STAGE + vA, wl4 = lds + wid * 2 * STAGE + vo4;
        K5_LW(0, 8); K5_LW(1, 9); K5_LW(2, 10);
        u32x4 res[2]; u32x2 res2;
        const u32 xoff = WS_X + (2 * layer + 1) * XB + (4 * mt + wid) * 18432;
        {
            const u32 need = 15u << (4 * wid);
            FENCE();
#pragma nounroll
            for (u32 n = 0; n < SPIN_CAP; ++n) { if ((__builtin_amdgcn_readfirstlane(ldsw(FL + 88)) & need) == need) break; }
            FENCE();
        }
        STAMP(layer * 4 + 3, 1);
        constexpr int ord[18] = {8, 0, 9, 1, 10, 2, 11, 3, 12, 4, 13, 5, 14, 6, 15, 7, 16, 17};
#pragma unroll
        for (int i = 0; i < 4; ++i) K5_LA(i, ord[i]);
        lds_wait(FL, 100u);
#pragma unroll
        for (int m = 0; m < 4; ++m)
#pragma unroll
            for (int n = 0; n < 5; ++n) PINV(ac[m][n]);
        asm volatile("s_nop 1");
#pragma unroll
        for (int i = 0; i < 18; ++i) {
            const int sl = ord[i];
#pragma unroll
            for (int m = 0; m < 4; ++m)
#pragma unroll
                for (int n = 0; n < 5; ++n) {
                    if (sl >= 8) MFS(ac[m][n], fw[(sl - 8) % 3][n], fa[i & 3][m]); else MFS(ac[m][n], fl[n], fa[i & 3][m]);
                    const int e = (n == 1 ? m : (m == 0 && n == 3 ? 4 : -1));
                    if (e >= 0) {
                        if (sl >= 8 && sl < 16) fl[e] = e < 4 ? LDSR(wl, (sl - 8) * 4608 + e * 1024) : LDSR(wl4, (sl - 8) * 4608);
                        if (sl < 7) fw[sl % 3][e] = e < 4 ? K5_LD(vA + e * 1024, wb + (sl + 11) * 4608, 0) : K5_LD(vo4, wb + (sl + 11) * 4608, 0);
                        if (sl == 7 && e == 0) res[0] = __builtin_amdgcn_raw_buffer_load_b128(rs, vA, xoff + 2304 * nt, 0);
                        if (sl == 7 && e == 1) res[1] = __builtin_amdgcn_raw_buffer_load_b128(rs, vA + 1024, xoff + 2304 * nt, 0);
                        if (sl == 7 && e == 2) res2 = __builtin_amdgcn_raw_buffer_load_b64(rs, (vA & 255) + ((vA >> 5) & 8) + 2048, xoff + 2304 * nt, 0);
                    }
                    if (n == 4 && i + 4 < 18) fa[i & 3][m] = K5_LD(vA + (ord[i + 4 < 18 ? i + 4 : 0] & 3) * 1024, abm[m] + (ord[i + 4 < 18 ? i + 4 : 0] & ~3) * 1024, 16);
                    SB();
                }
        }
        asm volatile("s_nop 15" ::: "memory");
        STAMP(layer * 4 + 3, 3);
        proj_tail(ac, res, res2, lds, 2 * STAGE, FL + 96, ws, out, 2 * layer + 2, mt, nt, wid, lane);
    }
    STAMP(layer * 4 + 3, 2);
    STAMP_OUT(layer * 4 + 3);
}

extern "C" __global__ void k9_end() {}

extern "C" void kernel_launch(void* const* d_in, const int* in_sizes, int n_in, void* d_out, int out_size, void* d_ws, size_t ws_size,
                              hipStream_t stream) {
    const u16* x = (const u16*)d_in[0];
    const u16* wqkv = (const u16*)d_in[1];
    const u16* wo = (const u16*)d_in[2];
    const u16* win = (const u16*)d_in[4];
    const u16* wsp = (const u16*)d_in[5];
    const u16* bsp = (const u16*)d_in[6];
    const u16* wout = (const u16*)d_in[7];
    char* ws = (char*)d_ws + WS_BASE(ws_size);
    u16* out = (u16*)d_out;
    (void)hipFuncSetAttribute((const void*)k1_attn, hipFuncAttributeMaxDynamicSharedMemorySize, K1_LDS);
    (void)hipFuncSetAttribute((const void*)k34_mlp, hipFuncAttributeMaxDynamicSharedMemorySize, K3_LDS);
    k0_prep<<<K0_GRID, 576, 0, stream>>>(x, wqkv, wo, win, wout, wsp, ws);
    for (int layer = 0; layer < 2; ++layer) {
        k1_attn<<<256, 512, K1_LDS, stream>>>(ws, layer);
        k34_mlp<<<256, 512, K3_LDS, stream>>>(ws, wsp, bsp, out, layer);
    }
}
```

```cpp
#include <hip/hip_runtime.h>
#include <hip/hip_bf16.h>
#include <stdint.h>

#define LAS __attribute__((address_space(3)))
#define GAS __attribute__((address_space(1)))
typedef unsigned short u16;
typedef unsigned u32;
typedef unsigned long long u64;
typedef short bf16x8 __attribute__((ext_vector_type(8)));
typedef float f32x2 __attribute__((ext_vector_type(2)));
typedef float f32x4 __attribute__((ext_vector_type(4)));
typedef unsigned u32x2 __attribute__((ext_vector_type(2)));
typedef unsigned u32x4 __attribute__((ext_vector_type(4)));
typedef __bf16 bf2_t __attribute__((ext_vector_type(2)));

#define ANY(p) (__builtin_amdgcn_ballot_w64(p) != 0)
#define U2F(x) __builtin_bit_cast(float, (u32)(x))
#define F2U(x) __builtin_bit_cast(u32, (float)(x))
#define WAIT_V(n) asm volatile("s_waitcnt vmcnt(%0)" ::"i"(n) : "memory")
#define WAIT_L0() asm volatile("s_waitcnt lgkmcnt(0)" ::: "memory")
#define FENCE() asm volatile("" ::: "memory")
#define SB() __builtin_amdgcn_sched_barrier(0)
#define BAR() do { FENCE(); __builtin_amdgcn_s_barrier(); FENCE(); } while (0)
#define WCNT(vm, lg) __builtin_amdgcn_s_waitcnt(((vm) & 15) | (7 << 4) | ((lg) << 8) | (((vm) >> 4) << 14))
#define MFMA(acc, x, y) asm volatile("v_mfma_f32_16x16x32_bf16 %0, %1, %2, %0" : "+v"(acc) : "v"(x), "v"(y))
#define MFMAB(x, y, c) __builtin_amdgcn_mfma_f32_16x16x32_bf16(x, y, c, 0, 0, 0)
#define BST128(o, rs, vo, so, aux) do { __builtin_amdgcn_raw_buffer_store_b128(o, rs, vo, so, aux); SB(); asm volatile("s_nop 1"); SB(); } while (0)
#define PINV(x) asm volatile("" : "+v"(x))
#define RLX_AGENT __ATOMIC_RELAXED, __HIP_MEMORY_SCOPE_AGENT

constexpr int S_ = 2048, D_ = 576, FH_ = 2304;
#define XDELAY1 18
#define XDELAY3 20
#define LGATE 12
#define LGATE2 14
#define POLLD 40
#define STAG 3
#define HPRIO 3
#define FDELAY 45
#define STAG2 60
#define MAP1 1
#define MAP3 1
#define CODETOUCH 1
#define WSPTOUCH 0
#define LSH 0
#define L2PF 0
#define ZSPLIT 10
#define ZGATE 8
#define QLATE 0
#define BADV 3072
#define AFAKE 0
#define BAUX 16
#define HDELAY 10
#define MAP5 1
#define WGATE 0
#define WGI 7
#define WEARLY 0
#define ODELAY 12
#define K0NT 1
#define NTL(p) __builtin_nontemporal_load(p)
#define TS3 2
#define TG0 6
#define TG1 6
#define HPW 0
#define HPS 4
constexpr u32 SENT = 0xFFFFFFFFu;
constexpr u32 SPIN_CAP = 1u << 16;

constexpr u32 WS_WQ = 0;
constexpr u32 WS_CSQ = WS_WQ + 16 * 129024;
constexpr u32 WS_WO = WS_CSQ + 18 * 8192;
constexpr u32 WS_WIN = WS_WO + 8 * 18 * 4608;
constexpr u32 WS_CSIN = WS_WIN + 288 * 18 * 1024;
constexpr u32 WS_WOUT = WS_CSIN + 2 * 4608 * 4;
constexpr u32 WS_X = WS_WOUT + 8 * 72 * 4608;
constexpr u32 XB = 128 * 18 * 1024, XSB = 2048 * 64, ZB = 144 * 64 * 1024;
constexpr u32 WS_XS = WS_X + 4 * XB;
constexpr u32 WS_O = WS_XS + 4 * XSB;
constexpr u32 WS_Z = WS_O + (1 + LSH) * XB;
constexpr u32 WS_H = WS_Z + (1 + LSH) * ZB;
constexpr u32 WS_KVP = WS_H + (1 + LSH) * ZB;
constexpr u32 WS_ZST = WS_KVP + 2 * 256 * 2816;
constexpr u32 WS_CNT = WS_ZST + 2 * 256 * 1024;
constexpr u32 WS_CNO = WS_CNT + 2 * 16 * 32 * 128;
constexpr u32 WS_END = WS_CNO + 2 * 16 * 16 * 128;
constexpr u32 WS_DBG = WS_END;
#define WSTOP 0
#define WS_BASE(n) (WSTOP ? (((n) - WS_END - 0x100000) & ~(size_t)0xfffff) : 0)

#define STAMP_DECL
#define STAMP(k, i) do { } while (0)
#define HST(i) do { } while (0)
#define HST_DECL
#define HST_OUT(k) do { } while (0)
#define STAMPV(k, i, v) do { } while (0)
#define STAMP_OUT(k) do { } while (0)

__device__ __forceinline__ u32 pk2(float lo, float hi) {
    f32x2 v = {lo, hi};
    bf2_t b = __builtin_convertvector(v, bf2_t);
    return __builtin_bit_cast(u32, b);
}
__device__ __forceinline__ float bflo(u32 w) { return U2F(w << 16); }
__device__ __forceinline__ float bfhi(u32 w) { return U2F(w & 0xffff0000u); }
__device__ __forceinline__ bf16x8 mk8(u32 a, u32 b, u32 c, u32 d) { u32x4 v = {a, b, c, d}; return __builtin_bit_cast(bf16x8, v); }
__device__ __forceinline__ float sfma(float a, float b, float c) { float r; asm("v_fma_f32 %0, %1, %2, %3" : "=v"(r) : "v"(a), "v"(b), "v"(c)); return r; }
__device__ __forceinline__ f32x4 sadd4(f32x4 a, f32x4 b) {
    float r0 = a[0], r1 = a[1], r2 = a[2], r3 = a[3];
    asm("v_add_f32 %0, %0, %4\n\tv_add_f32 %1, %1, %5\n\tv_add_f32 %2, %2, %6\n\tv_add_f32 %3, %3, %7" : "+v"(r0), "+v"(r1), "+v"(r2), "+v"(r3) : "v"(b[0]), "v"(b[1]), "v"(b[2]), "v"(b[3]));
    f32x4 r = {r0, r1, r2, r3};
    return r;
}
__device__ __forceinline__ f32x4 phi4(f32x4 t) {
    const f32x4 u = t * 1.44269504089f;
    f32x4 e, m;
#pragma unroll
    for (int r = 0; r < 4; ++r) { e[r] = __builtin_amdgcn_exp2f(__builtin_amdgcn_fmed3f(u[r], 0.f, -__builtin_inff())); m[r] = __builtin_amdgcn_fmed3f(t[r], 0.f, __builtin_inff()); }
    return (m + e) + 1e-6f;
}
__device__ __forceinline__ u32 ldsw(u32 a) { return *(volatile LAS u32*)(size_t)a; }
__device__ __forceinline__ void stsw(u32 a, u32 v) { *(volatile LAS u32*)(size_t)a = v; }
#define LDSR(addr, off) (*(LAS bf16x8*)((LAS char*)(size_t)(addr) + (off)))
__device__ __forceinline__ void lds_wait(u32 a, u32 target) {
    FENCE();
#pragma nounroll
    for (u32 n = 0; n < SPIN_CAP; ++n) { const u32 v = ldsw(a); if (!ANY(v < target)) break; }
    FENCE();
}
__device__ __forceinline__ void lds_wait_gt(u32 a, u32 low) {
    FENCE();
#pragma nounroll
    for (u32 n = 0; n < SPIN_CAP; ++n) { const u32 v = ldsw(a); if (!ANY(v <= low)) break; }
    FENCE();
}
__device__ __forceinline__ void lds_wait_sleep(u32 a, u32 target) {
    FENCE();
#pragma nounroll
    for (u32 n = 0; n < SPIN_CAP; ++n) { const u32 v = ldsw(a); if (!ANY(v < target)) break; __builtin_amdgcn_s_sleep(1); }
    FENCE();
}
#define DMA4(v0, v1, v2, v3, sbase, m0v)                                                                            \
    asm volatile("s_mov_b32 m0, %5\n\ts_nop 0\n\tglobal_load_lds_dwordx4 %0, %4\n\t"                                \
                 "global_load_lds_dwordx4 %1, %4 offset:1024\n\tglobal_load_lds_dwordx4 %2, %4 offset:2048\n\t"     \
                 "global_load_lds_dwordx4 %3, %4 offset:3072" ::"v"(v0), "v"(v1), "v"(v2), "v"(v3), "s"(sbase), "s"(m0v) : "memory")
#define DMA2(v0, v1, sbase, m0v)                                                                                    \
    asm volatile("s_mov_b32 m0, %3\n\ts_nop 0\n\tglobal_load_lds_dwordx4 %0, %2\n\t"                                \
                 "global_load_lds_dwordx4 %1, %2 offset:1024" ::"v"(v0), "v"(v1), "s"(sbase), "s"(m0v) : "memory")

template <int SLP>
__device__ __forceinline__ void touch_paced(const char* p, int n, u32 vo) {
    u32x4 sink = {0u, 0u, 0u, 0u};
#pragma nounroll
    for (int i = 0; i < n; ++i) {
        asm volatile("global_load_dwordx4 %0, %1, %2" : "+v"(sink) : "v"(vo), "s"(p + i * 1024));
        __builtin_amdgcn_s_sleep(SLP);
    }
    WAIT_V(0);
    PINV(sink);
}

__device__ __forceinline__ u32x4 tcol(const u32x2 (&ld)[8], int i) {
    const u32 sel = (i & 1) ? 0x07060302u : 0x05040100u;
    u32x4 o;
    o.x = __builtin_amdgcn_perm(ld[1][i >> 1], ld[0][i >> 1], sel);
    o.y = __builtin_amdgcn_perm(ld[3][i >> 1], ld[2][i >> 1], sel);
    o.z = __builtin_amdgcn_perm(ld[5][i >> 1], ld[4][i >> 1], sel);
    o.w = __builtin_amdgcn_perm(ld[7][i >> 1], ld[6][i >> 1], sel);
    return o;
}
__device__ __forceinline__ float sum8(u32x4 o) {
    return ((bflo(o.x) + bfhi(o.x)) + (bflo(o.y) + bfhi(o.y))) + ((bflo(o.z) + bfhi(o.z)) + (bflo(o.w) + bfhi(o.w)));
}

constexpr int K0_QKV = 72, K0_X = 64, K0_WIN = 144, K0_WO = 90, K0_GRID = K0_QKV + K0_X + K0_WIN + K0_WO;
__device__ __forceinline__ void k0_body(const u16* __restrict__ x, const u16* __restrict__ wqkv, const u16* __restrict__ wo,
                                        const u16* __restrict__ win, const u16* __restrict__ wout, const u16* __restrict__ wsp, char* __restrict__ ws) {
    __shared__ __attribute__((aligned(16))) char img[36864 + 576 * 8];
    float (*red)[64] = (float (*)[64])(img + 36864);
    const int tid = threadIdx.x, w = __builtin_amdgcn_readfirstlane(tid >> 6), lane = tid & 63, c = lane & 15, g = lane >> 4;
    int b = blockIdx.x;
    if (b < K0_QKV) {
        const int t = (b >> 3) % 3, kt = (b & 7) + 8 * (b / 24);
        if (kt >= 18) return;
        u32x2 ld[8];
#pragma unroll
        for (int j = 0; j < 8; ++j) ld[j] = NTL((const u32x2*)(wqkv + (size_t)(32 * kt + 8 * g + j) * 1728 + 576 * t + 64 * w + 4 * c));
        float* cst = (float*)(img + 36864);
#pragma unroll
        for (int i = 0; i < 4; ++i) {
            const int col = 64 * w + 4 * c + i, h = col / 18, d = col - 18 * h;
            const u32x4 o = tcol(ld, i);
            *(u32x4*)(img + (d < 16 ? h * 1024 + (d + 16 * g) * 16 : 32768 + (h >> 1) * 256 + g * 64 + (2 * (h & 1) + d - 16) * 16)) = o;
            float cs = sum8(o);
            cs += __shfl_xor(cs, 16); cs += __shfl_xor(cs, 32);
            if (g == 0) cst[(h >> 1) * 36 + (d < 16 ? (h & 1) * 16 + d : 32 + 2 * (h & 1) + d - 16)] = cs;
        }
        __syncthreads();
        const int rc = t == 1 ? 0 : (t == 2 ? 4 : 8);
        if (w < 8) {
#pragma unroll
            for (int k = 0; k < 4; ++k) {
                const int h = 4 * w + k;
                const u32 tile = t == 0 ? 92160 + (kt * 2 + (h & 1)) * 1024 : (kt * 5 + 2 * (t - 1) + (h & 1)) * 1024;
                *(u32x4*)(ws + WS_WQ + (h >> 1) * 129024 + tile + lane * 16) = *(const u32x4*)(img + h * 1024 + lane * 16);
            }
        } else {
#pragma unroll
            for (int k = 0; k < 4; ++k) {
                char* dst = ws + WS_WQ + (4 * k + g) * 129024 + (kt * 5 + 4) * 1024 + (c >> 2) * 256 + (rc + (c & 3)) * 16;
                *(u32x4*)dst = *(const u32x4*)(img + 32768 + k * 1024 + lane * 16);
                if (t == 0) *(u32x4*)(dst + 64) = (u32x4){0u, 0u, 0u, 0u};
            }
        }
        {
            const int p = tid / 36, e = tid - 36 * p;
            float* cq = (float*)(ws + WS_CSQ) + (kt * 16 + p) * 128;
            cq[e < 32 ? (t == 0 ? 80 : 32 * (t - 1)) + e : 64 + rc + e - 32] = cst[tid];
            if (t == 0 && e >= 32) cq[76 + e - 32] = 0.f;
        }
        return;
    }
    b -= K0_QKV;
    if (b < K0_X) {
        const int rl = tid / 18, t18 = tid - 18 * rl;
        u32x4 v[4];
#pragma unroll
        for (int i = 0; i < 4; ++i) v[i] = NTL((const u32x4*)(x + (size_t)(32 * b + rl) * 576 + (18 * i + t18) * 8));
        float s1 = 0.f, s2 = 0.f;
#pragma unroll
        for (int i = 0; i < 4; ++i) {
            const int ch = 18 * i + t18;
            *(u32x4*)(img + ((rl >> 4) * 18 + (ch >> 2)) * 1024 + ((rl & 15) + 16 * (ch & 3)) * 16) = v[i];
#pragma unroll
            for (int k = 0; k < 4; ++k) { const float a = bflo(v[i][k]), bb = bfhi(v[i][k]); s1 += a + bb; s2 += a * a + bb * bb; }
        }
        f32x2 o = {s1, s2};
        ((f32x2*)(img + 36864))[tid] = o;
        __syncthreads();
#pragma unroll
        for (int t = 0; t < 4; ++t) *(u32x4*)(ws + WS_X + b * 36864 + (w * 4 + t) * 1024 + lane * 16) = *(const u32x4*)(img + (w * 4 + t) * 1024 + lane * 16);
        if (tid < 256) {
            const int r = tid & 31, q = tid >> 5;
            const f32x2* pp = (const f32x2*)(img + 36864) + r * 18;
            o = pp[2 * q] + pp[2 * q + 1];
            if (q < 2) o += pp[16 + q];
            *(f32x2*)(ws + WS_XS + (q * 2048 + 32 * b + r) * 8) = o;
        }
        return;
    }
    b -= K0_X;
    if (b < K0_WIN) {
        const int hf = b & 1, cb = b >> 1, kt = 9 * hf + w;
        u32x2 ld[8];
#pragma unroll
        for (int j = 0; j < 8; ++j) ld[j] = NTL((const u32x2*)(win + (size_t)(32 * kt + 8 * g + j) * 4608 + 64 * cb + 4 * c));
#pragma unroll
        for (int i = 0; i < 4; ++i) {
            const u32x4 o = tcol(ld, i);
            *(u32x4*)(img + w * 4096 + (c >> 2) * 1024 + (4 * (c & 3) + i + 16 * g) * 16) = o;
            float cs = sum8(o);
            cs += __shfl_xor(cs, 16); cs += __shfl_xor(cs, 32);
            if (g == 0) red[w][4 * c + i] = cs;
        }
        FENCE();
#pragma unroll
        for (int t = 0; t < 4; ++t) *(u32x4*)(ws + WS_WIN + ((4 * cb + t) * 18 + kt) * 1024 + lane * 16) = *(const u32x4*)(img + w * 4096 + t * 1024 + lane * 16);
        __syncthreads();
        if (tid < 64) {
            float s = 0.f;
#pragma unroll
            for (int k = 0; k < 9; ++k) s += red[k][tid];
            ((float*)(ws + WS_CSIN))[hf * 4608 + 64 * cb + tid] = s;
        }
        return;
    }
    b -= K0_WIN;
    {
        const bool iso = b < 18;
        const int sl = iso ? b : b - 18, G = sl / 9, t = sl - 9 * G;
        const u16* src = iso ? wo : wout;
        const int col = 64 * w + 4 * c, nt = col / 72, cl = col - 72 * nt;
        const int r0 = iso ? (sl < 16 ? 36 * sl : 288 * (sl - 16) + 16) : (t < 8 ? 288 * G + 144 * (t >> 2) + 32 * (t & 3) : 288 * G + 128);
        const int gs = (iso && sl >= 16) ? 72 : 4, s4 = iso ? (sl < 16 ? 18 : 36) : (t < 8 ? 16 : 144), s2 = (iso && sl >= 16) ? 18 : 2;
        u32x2 ld[8];
#pragma unroll
        for (int j = 0; j < 8; ++j) ld[j] = NTL((const u32x2*)(src + (size_t)(r0 + gs * g + s4 * (j >> 2) + s2 * ((j >> 1) & 1) + (j & 1)) * 576 + col));
        const int n = cl < 64 ? 2 * (cl >> 5) + ((cl >> 2) & 1) : 4, ii = cl < 64 ? 4 * ((cl >> 3) & 3) : cl - 64;
#pragma unroll
        for (int i = 0; i < 4; ++i) {
            const u32 off = n < 4 ? n * 1024 + (ii + i + 16 * g) * 16 : 4096 + g * 128 + (ii + i) * 16;
            *(u32x4*)(img + nt * 4608 + off) = tcol(ld, i);
        }
        __syncthreads();
#pragma unroll
        for (int k = 0; k < 4; ++k) {
            const u32 i = (w * 4 + k) * 1024 + lane * 16, n2 = i / 4608, off = i - 4608 * n2;
            *(u32x4*)(ws + (iso ? WS_WO + (n2 * 18 + sl) * 4608 : WS_WOUT + (n2 * 72 + sl) * 4608) + off) = *(const u32x4*)(img + i);
        }
        return;
    }
}

__global__ __launch_bounds__(576) void k0_prep(const u16* __restrict__ x, const u16* __restrict__ wqkv, const u16* __restrict__ wo,
                                               const u16* __restrict__ win, const u16* __restrict__ wout, const u16* __restrict__ wsp, char* __restrict__ ws) {
    STAMP_DECL;
    STAMP(8, 0);
    const u32 gid = blockIdx.x * 576 + threadIdx.x;
    u32 warm = 0;
    if (WSPTOUCH && gid % 3 == 0 && gid < 3 * 65536) warm = *(const u32*)(wsp + (size_t)(gid / 3) * 64);
    if (blockIdx.x >= 50 && blockIdx.x < 66) {
        u64 pc; int dist;
        asm volatile("s_getpc_b64 %0\n\ts_mov_b32 %1, k9_end@rel32@lo+4" : "=&s"(pc), "=&s"(dist));
        if ((int)threadIdx.x * 128 < dist) warm |= *(const GAS u32*)(pc + threadIdx.x * 128);
    }
    if (gid < (WS_END - WS_KVP) / 16) {
        const auto rs = __builtin_amdgcn_make_buffer_rsrc(ws, 0, WS_END, 0x00020000);
        const u32 v = gid < (WS_CNT - WS_KVP) / 16 ? SENT : 0u;
        const u32x4 o = {v, v, v, v};
        BST128(o, rs, gid * 16, WS_KVP, 16);
    }
    k0_body(x, wqkv, wo, win, wout, wsp, ws);
    asm volatile("" :: "v"(warm));
    STAMP(8, 1);
    STAMP_OUT(11);
}

__device__ __forceinline__ void x_stats(const char* ws, int ver, int row, float& mean, float& var) {
    const f32x2* p = (const f32x2*)(ws + WS_XS + ver * XSB) + row;
    f32x2 v[8];
#pragma unroll
    for (int k = 0; k < 8; ++k) v[k] = p[k * 2048];
    const f32x2 sm = ((v[0] + v[1]) + (v[2] + v[3])) + ((v[4] + v[5]) + (v[6] + v[7]));
    const float s1 = sm[0], s2 = sm[1];
    mean = s1 * (1.f / 576.f);
    var = fmaxf(s2 * (1.f / 576.f) - mean * mean, 0.f);
}

__device__ __forceinline__ void proj_store(const f32x4 (&y)[5], char* ws, u16* out, int ver, int mt, int nt, int wid, int lane) {
    const u32 vo = lane * 16;
    const int c = lane & 15, g = lane >> 4;
    u32x4 o[2]; u32x2 o2;
#pragma unroll
    for (int i = 0; i < 2; ++i) o[i] = (u32x4){pk2(y[2 * i][0], y[2 * i][1]), pk2(y[2 * i][2], y[2 * i][3]), pk2(y[2 * i + 1][0], y[2 * i + 1][1]), pk2(y[2 * i + 1][2], y[2 * i + 1][3])};
    o2 = (u32x2){pk2(y[4][0], y[4][1]), pk2(y[4][2], y[4][3])};
    if (ver == 4) {
        u16* op = out + (size_t)(64 * mt + 16 * wid + c) * 576 + 72 * nt;
        *(u32x4*)(op + 8 * g) = o[0];
        *(u32x4*)(op + 32 + 8 * g) = o[1];
        if (g < 2) *(u32x2*)(op + 64 + 4 * g) = o2;
    } else {
        char* xp = ws + WS_X + ver * XB + (4 * mt + wid) * 18432 + 2304 * nt;
        *(u32x4*)(xp + vo) = o[0];
        *(u32x4*)(xp + vo + 1024) = o[1];
        if (g < 2) *(u32x2*)(xp + 2048 + c * 16 + g * 8) = o2; else o2 = (u32x2){0u, 0u};
        float s1 = 0.f, s2 = 0.f;
#pragma unroll
        for (int k = 0; k < 10; ++k) { const u32 wv = k < 8 ? o[k >> 2][k & 3] : o2[k & 1]; const float u = bflo(wv), v = bfhi(wv); s1 += u + v; s2 += u * u + v * v; }
        auto r1 = __builtin_amdgcn_permlane32_swap(F2U(s1), F2U(s2), false, false);
        const float t = U2F(r1[0]) + U2F(r1[1]);
        auto r2 = __builtin_amdgcn_permlane16_swap(F2U(t), F2U(t), false, false);
        const float tt = U2F(r2[0]) + U2F(r2[1]);
        if ((g & 1) == 0) *(float*)(ws + WS_XS + ver * XSB + (nt * 2048 + 64 * mt + 16 * wid + c) * 8 + (g >> 1) * 4) = tt;
    }
}

__device__ __forceinline__ void proj_tail(f32x4 (&ac)[4][5], const u32x4 (&res)[2], u32x2 res2, u32 lds, u32 wstride, u32 flags, char* ws, u16* out,
                                          int ver, int mt, int nt, int wid, int lane) {
    const u32 vo = lane * 16;
#pragma unroll
    for (int m = 1; m < 4; ++m)
#pragma unroll
        for (int n = 0; n < 5; ++n) *(LAS f32x4*)(size_t)(lds + wid * wstride + vo + ((m - 1) * 5 + n) * 1024) = ac[m][n];
    FENCE();
    stsw(flags + wid * 4, 1u);
#pragma unroll
    for (int i = 0; i < 4; ++i) {
        const u32 w0 = res[i >> 1][2 * (i & 1)], w1 = res[i >> 1][2 * (i & 1) + 1];
        ac[0][i] += (f32x4){bflo(w0), bfhi(w0), bflo(w1), bfhi(w1)};
        PINV(ac[0][i]);
    }
    ac[0][4] += (f32x4){bflo(res2[0]), bfhi(res2[0]), bflo(res2[1]), bfhi(res2[1])};
    PINV(ac[0][4]);
    lds_wait(flags + (lane & 3) * 4, 1u);
#pragma unroll
    for (int n = 0; n < 5; ++n)
        ac[0][n] = (ac[0][n] + *(LAS f32x4*)(size_t)(lds + (wid ^ 1) * wstride + vo + n * 1024)) +
                   (*(LAS f32x4*)(size_t)(lds + (wid ^ 2) * wstride + vo + (5 + n) * 1024) + *(LAS f32x4*)(size_t)(lds + (wid ^ 3) * wstride + vo + (10 + n) * 1024));
    proj_store(ac[0], ws, out, ver, mt, nt, wid, lane);
}

constexpr u32 K1_PW = 129024, K1_OP = K1_PW + 4 * 176 * 16, K1_ST = K1_OP + 1536, K1_CS = K1_ST + 1024, K1_FL = K1_CS + 512, K1_LDS = K1_FL + 128;

__global__ __launch_bounds__(512, 2) void k1_attn(char* __restrict__ ws, int layer) {
    extern __shared__ __attribute__((aligned(16))) char shm_[];
    const u32 lds = (u32)(size_t)(LAS char*)shm_;
    const int tid = threadIdx.x, wid = __builtin_amdgcn_readfirstlane(tid >> 6), lane = tid & 63, c = lane & 15, g = lane >> 4;
    const int xcd = blockIdx.x & 7, bi = blockIdx.x >> 3;
    const int pm = MAP1 ? (xcd & 3) * 4 + (bi & 3) : (xcd & 1) * 8 + (bi & 7), p = MAP1 ? (xcd >> 2) * 8 + (bi >> 2) : (xcd >> 1) * 4 + (bi >> 3);
    const u32 FL = lds + K1_FL;
    STAMP_DECL;
    if (wid == 4) {
        const char* src = ws + WS_WQ + p * 129024;
        const u32 vo = lane * 16;
#define K1_U10(u) do { DMA4(vo, vo, vo, vo, src + (u) * 10240, lds + (u) * 10240); DMA4(vo, vo, vo, vo, src + (u) * 10240 + 4096, lds + (u) * 10240 + 4096); \
                       DMA2(vo, vo, src + (u) * 10240 + 8192, lds + (u) * 10240 + 8192); } while (0)
#define K1_U12(u) do { DMA4(vo, vo, vo, vo, src + 92160 + (u) * 12288, lds + 92160 + (u) * 12288); DMA4(vo, vo, vo, vo, src + 92160 + (u) * 12288 + 4096, lds + 92160 + (u) * 12288 + 4096); \
                       DMA4(vo, vo, vo, vo, src + 92160 + (u) * 12288 + 8192, lds + 92160 + (u) * 12288 + 8192); } while (0)
        K1_U10(0);
        BAR();
        K1_U10(1); K1_U10(2);
#pragma unroll
        for (int u = 3; u < 9; ++u) { WAIT_V(20); stsw(FL, u - 2); FENCE(); K1_U10(u); }
        WAIT_V(20); stsw(FL, 7); FENCE(); K1_U12(0);
        WAIT_V(22); stsw(FL, 8); FENCE(); K1_U12(1);
        WAIT_V(24); stsw(FL, 9); FENCE(); K1_U12(2);
        WAIT_V(24); stsw(FL, 10); FENCE();
        WAIT_V(12); stsw(FL, 11); FENCE();
        WAIT_V(0); stsw(FL, 12);
        lds_wait_sleep(FL + 16 + (lane & 3) * 4, 2u);
        src = ws + WS_WO + (p & 7) * 82944;
#pragma unroll
        for (int u = 0; u < 8; ++u) { K1_U10(u); if (u >= 4) WAIT_V(40); }
        asm volatile("s_mov_b32 m0, %2\n\ts_nop 0\n\tglobal_load_lds_dwordx4 %0, %1" ::"v"(vo), "s"(src + 81920), "s"(lds + 81920) : "memory");
        WAIT_V(0); stsw(FL, 13);
        return;
    }
    if (wid > 4) {
        const int ht = tid - 320;
        if (ht >= 128 && ht < 160) stsw(FL + (ht - 128) * 4, 0u);
        WAIT_L0();
        BAR();
        if (wid != 7) {
            float mean, var;
            x_stats(ws, 2 * layer, 128 * pm + ht, mean, var);
            const float r = __builtin_amdgcn_rsqf(var + 1e-5f);
            *(LAS float*)(size_t)(lds + K1_ST + ht * 4) = r;
            *(LAS float*)(size_t)(lds + K1_ST + 512 + ht * 4) = -mean * r;
        } else {
            const f32x2* cp = (const f32x2*)(ws + WS_CSQ) + p * 64 + min(lane, 55);
            f32x2 q[18];
#pragma unroll
            for (int k = 0; k < 18; ++k) q[k] = cp[k * 1024];
            float q0 = q[0][0], q1 = q[0][1];
#pragma unroll
            for (int k = 1; k < 18; ++k) { q0 = sfma(q[k][0], 1.f, q0); q1 = sfma(q[k][1], 1.f, q1); }
            *(LAS f32x2*)(size_t)(lds + K1_CS + lane * 8) = (f32x2){q0, q1};
        }
        FENCE();
        stsw(FL + 4 + (wid - 5) * 4, 1u);
        __builtin_amdgcn_s_setprio(3);
        lds_wait_sleep(FL + 16 + (lane & 3) * 4, 1u);
        STAMP(layer * 4 + 0, 7);
        const int it = ht < 173 ? ht : 172;
        f32x4 v = *(LAS f32x4*)(size_t)(lds + K1_PW + it * 16);
#pragma unroll
        for (int k = 1; k < 4; ++k) v = sadd4(v, *(LAS f32x4*)(size_t)(lds + K1_PW + k * 2816 + it * 16));
        const auto rs = __builtin_amdgcn_make_buffer_rsrc(ws, 0, WS_END, 0x00020000);
        const u32 gbase = WS_KVP + (layer * 16 + p) * 16 * 2816;
        {
            u32x4 o = __builtin_bit_cast(u32x4, v);
#pragma unroll
            for (int k = 0; k < 4; ++k) o[k] = o[k] == SENT ? 0x7fc00000u : o[k];
            if (ht < 173) BST128(o, rs, it * 16, gbase + pm * 2816, 16);
        }
        u32x4 t[16];
        STAMP(layer * 4 + 0, 8); STAMP(layer * 4 + 0, 9);
        __builtin_amdgcn_s_sleep(XDELAY1);
#pragma unroll
        for (int k = 0; k < 16; ++k) t[k] = __builtin_amdgcn_raw_buffer_load_b128(rs, it * 16, gbase + k * 2816, 16);
        u32 mx = 0, spins = 0;
#pragma unroll
        for (int k = 0; k < 16; ++k) mx = max(max(mx, max(t[k][0], t[k][1])), max(t[k][2], t[k][3]));
        if (__builtin_expect(ANY(mx == SENT), 0)) {
#pragma nounroll
            for (spins = 1; spins < SPIN_CAP; ++spins) {
                FENCE();
                u32 miss = 0;
#pragma unroll
                for (int k = 0; k < 16; ++k) miss |= ANY(max(max(t[k][0], t[k][1]), max(t[k][2], t[k][3])) == SENT) ? 1u << k : 0u;
                if (!miss) break;
#pragma unroll
                for (int k = 0; k < 16; ++k) if (miss >> k & 1) t[k] = __builtin_amdgcn_raw_buffer_load_b128(rs, it * 16, gbase + k * 2816, 16);
            }
        }
        STAMP(layer * 4 + 0, 10); STAMPV(layer * 4 + 0, 15, spins);
        v = __builtin_bit_cast(f32x4, t[0]);
#pragma unroll
        for (int k = 1; k < 16; ++k) v = sadd4(v, __builtin_bit_cast(f32x4, t[k]));
        u32x2 lh = {pk2(v[0], v[1]), pk2(v[2], v[3])};
        if (ht >= 173) lh = (u32x2){0u, 0u};
        *(LAS u32x2*)(size_t)(lds + K1_OP + ht * 8) = lh;
        FENCE();
        stsw(FL + 32 + (wid - 5) * 4, 1u);
        if (wid != 5) return;
        STAMP_OUT(layer * 4 + 0);
        lds_wait_sleep(FL + 48, 4u);
        __builtin_amdgcn_s_sleep(ODELAY);
        GAS u32* om = (GAS u32*)(ws + WS_CNO + ((layer * 16 + pm) * 16 + p) * 128);
#pragma nounroll
        for (u32 n = 0; n < SPIN_CAP; ++n) { if (__builtin_amdgcn_readfirstlane(__hip_atomic_load(om, RLX_AGENT)) == 0xffffu) break; }
        FENCE();
        stsw(FL + 52, 1u);
        return;
    }
    STAMP(layer * 4 + 0, 0);

    const auto rs = __builtin_amdgcn_make_buffer_rsrc(ws, 0, WS_END, 0x00020000);
    const u32 xb = WS_X + 2 * layer * XB + (8 * pm + 2 * wid) * 18432, vo = lane * 16;
    bf16x8 xa[2][18];
#define K1_LX(m, kt) xa[m][kt] = __builtin_bit_cast(bf16x8, __builtin_amdgcn_raw_buffer_load_b128(rs, vo, xb + ((m) * 18 + (kt)) * 1024, 0))
#pragma unroll
    for (int kt = 0; kt < 6; ++kt) { K1_LX(0, kt); SB(); K1_LX(1, kt); SB(); }
    f32x4 acc[2][5];
#pragma unroll
    for (int f = 0; f < 5; ++f) { acc[0][f] = acc[1][f] = (f32x4){0.f, 0.f, 0.f, 0.f}; PINV(acc[0][f]); PINV(acc[1][f]); }
    u32 wb0 = lds + lane * 16, wb1 = wb0 + 61440, wb2 = wb0 + 92160;
    PINV(wb0); PINV(wb1); PINV(wb2);
#define K1_W1(kt, f) ((kt) < 12 ? LDSR(wb0, ((kt) * 5 + (f)) * 1024) : LDSR(wb1, (((kt) - 12) * 5 + (f)) * 1024))
    BAR();
    lds_wait(FL, 1u);
    STAMP(layer * 4 + 0, 1);
    bf16x8 wf[2][5];
#pragma unroll
    for (int f = 0; f < 5; ++f) { wf[0][f] = K1_W1(0, f); SB(); }
#pragma unroll
    for (int f = 0; f < 5; ++f) { wf[1][f] = K1_W1(1, f); SB(); }
    u32 fl = 0;
#pragma unroll
    for (int kt = 0; kt < 18; ++kt) {
        if ((kt & 1) == 0 && kt + 2 < 18) { if (__builtin_expect(ANY(fl < (u32)(kt / 2 + 2)), 0)) lds_wait(FL, kt / 2 + 2); FENCE(); }
#pragma unroll
        for (int f = 0; f < 5; ++f) {
            MFMA(acc[0][f], xa[0][kt], wf[kt & 1][f]);
            if (f == 1 && kt + 6 < 18) K1_LX(0, kt + 6);
            if (f == 3 && kt + 6 < 18) K1_LX(1, kt + 6);
            if (f == 0 && (kt & 1) && kt < 16) { FENCE(); fl = ldsw(FL); FENCE(); }
            SB();
            MFMA(acc[1][f], xa[1][kt], wf[kt & 1][f]);
            if (kt + 2 < 18) wf[kt & 1][f] = K1_W1(kt + 2, f);
            SB();
        }
    }
    asm volatile("s_nop 15" ::: "memory");
    STAMP(layer * 4 + 0, 2);
    lds_wait(FL + 4 + (lane % 3) * 4, 1u);
    {
        u32 op[5][4], rk[4];
        float cs1[5];
#pragma unroll
        for (int f = 0; f < 5; ++f) cs1[f] = *(LAS float*)(size_t)(lds + K1_CS + f * 64 + c * 4);
#pragma unroll
        for (int m = 0; m < 2; ++m) {
            const f32x4 rr = *(LAS f32x4*)(size_t)(lds + K1_ST + (32 * wid + 16 * m + 4 * g) * 4);
            const f32x4 mm = *(LAS f32x4*)(size_t)(lds + K1_ST + 512 + (32 * wid + 16 * m + 4 * g) * 4);
#pragma unroll
            for (int f = 0; f < 5; ++f) {
                f32x4 v = rr * acc[m][f] + mm * cs1[f];
                if (f < 2) v = phi4(v);
                if (f == 4) { const f32x4 u = phi4(v); rk[2 * m] = pk2(u[0], u[1]); rk[2 * m + 1] = pk2(u[2], u[3]); }
                op[f][2 * m] = pk2(v[0], v[1]); op[f][2 * m + 1] = pk2(v[2], v[3]);
                if (f == 4 && c >= 12) { op[f][2 * m] = 0x3f803f80u; op[f][2 * m + 1] = 0x3f803f80u; }
            }
        }
        const bf16x8 KAa = mk8(op[0][0], op[0][1], op[0][2], op[0][3]), KAb = mk8(op[1][0], op[1][1], op[1][2], op[1][3]);
        const bf16x8 VBa = mk8(op[2][0], op[2][1], op[2][2], op[2][3]), VBb = mk8(op[3][0], op[3][1], op[3][2], op[3][3]);
        const bf16x8 RV = mk8(op[4][0], op[4][1], op[4][2], op[4][3]), RK = mk8(rk[0], rk[1], rk[2], rk[3]);
        const f32x4 z4 = {0.f, 0.f, 0.f, 0.f};
        const f32x4 T1a = MFMAB(KAa, VBa, z4), T1b = MFMAB(KAb, VBb, z4), T2a = MFMAB(KAa, RV, z4), T2b = MFMAB(KAb, RV, z4);
        const f32x4 T3a = MFMAB(RK, VBa, z4), T3b = MFMAB(RK, VBb, z4), T4 = MFMAB(RK, RV, z4);
        const u32 pw = lds + K1_PW + wid * 2816;
        *(LAS f32x4*)(size_t)(pw + lane * 16) = T1a;
        *(LAS f32x4*)(size_t)(pw + 1024 + lane * 16) = T1b;
        const f32x4 u3 = {T3a[0], T3a[1], T3b[2], T3b[3]};
        *(LAS f32x4*)(size_t)(pw + (g == 0 ? 128 + c : 175) * 16) = u3;
        *(LAS f32x4*)(size_t)(pw + (c == 12 ? 152 + g : ((c & 14) == 4 ? 144 + (c - 4) * 4 + g : 175)) * 16) = T2a;
        *(LAS f32x4*)(size_t)(pw + (c == 12 ? 164 + g : ((c & 14) == 6 ? 156 + (c - 6) * 4 + g : 175)) * 16) = T2b;
        *(LAS f32x4*)(size_t)(pw + (lane == 12 ? 172 : ((lane & 60) == 4 ? 164 + lane : 175)) * 16) = T4;
        FENCE();
        stsw(FL + 16 + wid * 4, 1u);
    }
    STAMP(layer * 4 + 0, 3);
    f32x4 qa[2][3];
#pragma unroll
    for (int f = 0; f < 3; ++f) { qa[0][f] = qa[1][f] = (f32x4){0.f, 0.f, 0.f, 0.f}; PINV(qa[0][f]); PINV(qa[1][f]); }
#define K1_W2(kt, f) ((f) < 2 ? LDSR(wb2, ((kt) * 2 + (f)) * 1024) : K1_W1(kt, 4))
    lds_wait(FL, 10u);
    bf16x8 wq[3][3];
#pragma unroll
    for (int k = 0; k < 3; ++k)
#pragma unroll
        for (int f = 0; f < 3; ++f) { wq[k][f] = K1_W2(k, f); SB(); }
#pragma unroll
    for (int kt = 0; kt < 18; ++kt) {
        if (kt == 3) lds_wait(FL, 11u);
        if (kt == 9) lds_wait(FL, 12u);
#pragma unroll
        for (int f = 0; f < 3; ++f) {
            MFMA(qa[0][f], wq[kt % 3][f], xa[0][kt]);
            SB();
            MFMA(qa[1][f], wq[kt % 3][f], xa[1][kt]);
            if (kt + 3 < 18) wq[kt % 3][f] = K1_W2(kt + 3, f);
            SB();
        }
    }
    asm volatile("s_nop 15" ::: "memory");
    STAMP(layer * 4 + 0, 4);
    stsw(FL + 16 + wid * 4, 2u);
    f32x4 cs2[3];
#pragma unroll
    for (int f = 0; f < 3; ++f) cs2[f] = *(LAS f32x4*)(size_t)(lds + K1_CS + (f < 2 ? 5 + f : 4) * 64 + g * 16);
    bf16x8 PQ[2][2];
#pragma unroll
    for (int m = 0; m < 2; ++m) {
        const float rr = *(LAS float*)(size_t)(lds + K1_ST + (32 * wid + 16 * m + c) * 4), mm = *(LAS float*)(size_t)(lds + K1_ST + 512 + (32 * wid + 16 * m + c) * 4);
        u32 w2[3][2];
#pragma unroll
        for (int f = 0; f < 3; ++f) {
            const f32x4 v = phi4(qa[m][f] * rr + cs2[f] * mm);
            w2[f][0] = pk2(v[0], v[1]); w2[f][1] = pk2(v[2], v[3]);
        }
        if (g != 2) { w2[2][0] = 0u; w2[2][1] = 0u; }
        PQ[m][0] = mk8(w2[0][0], w2[0][1], w2[2][0], w2[2][1]);
        PQ[m][1] = mk8(w2[1][0], w2[1][1], w2[2][0], w2[2][1]);
        PINV(PQ[m][0]); PINV(PQ[m][1]);
    }
    u32 ga[6];
    ga[0] = lane; ga[1] = g == 2 ? 128 + c : 175;
    ga[2] = c < 2 ? 144 + 4 * c + g : ((c & 3) == 2 ? 152 + g : 175);
    ga[3] = (c & 14) == 4 ? 156 + 4 * (c - 4) + g : ((c & 3) == 3 ? 164 + g : 175);
    ga[4] = g != 2 ? 175 : (c < 2 ? 168 + c : ((c & 3) == 2 ? 172 : 175));
    ga[5] = g != 2 ? 175 : ((c & 14) == 4 ? 166 + c : ((c & 3) == 3 ? 172 : 175));
#pragma unroll
    for (int i = 0; i < 6; ++i) { ga[i] = lds + K1_OP + ga[i] * 8; PINV(ga[i]); }
    const u32 ob = WS_O + LSH * layer * XB + (8 * pm + 2 * wid) * 18432;
    u32 orem = (16 + (p >> 3)) * 1024 + (c + 16 * ((p & 7) >> 1)) * 16 + (p & 1) * 8 + g * 4;
    PINV(orem);
    lds_wait(FL + 32 + (lane % 3) * 4, 1u);
    STAMP(layer * 4 + 0, 5);
    {
        const u32x2 l0 = *(LAS u32x2*)(size_t)ga[0], l1 = *(LAS u32x2*)(size_t)(ga[0] + 512), h01 = *(LAS u32x2*)(size_t)ga[1];
        const u32x2 l2 = *(LAS u32x2*)(size_t)ga[2], l3 = *(LAS u32x2*)(size_t)ga[3];
        const u32 h2 = *(LAS u32*)(size_t)ga[4], h3 = *(LAS u32*)(size_t)(ga[5] + 4);
        const bf16x8 A1a = mk8(l0[0], l0[1], h01[0], 0u), A1b = mk8(l1[0], l1[1], 0u, h01[1]), A2a = mk8(l2[0], l2[1], h2, 0u), A2b = mk8(l3[0], l3[1], 0u, h3);
        const f32x4 z4 = {0.f, 0.f, 0.f, 0.f};
        f32x4 O1a[2], O1b[2], O2[2];
#pragma unroll
        for (int m = 0; m < 2; ++m) O2[m] = MFMAB(A2a, PQ[m][0], z4);
#pragma unroll
        for (int m = 0; m < 2; ++m) O2[m] = MFMAB(A2b, PQ[m][1], O2[m]);
#pragma unroll
        for (int m = 0; m < 2; ++m) { O1a[m] = MFMAB(A1a, PQ[m][0], z4); O1b[m] = MFMAB(A1b, PQ[m][1], z4); }
#pragma unroll
        for (int m = 0; m < 2; ++m) {
            const float ia = __builtin_amdgcn_rcpf(O2[m][2] + 1e-6f), ib = __builtin_amdgcn_rcpf(O2[m][3] + 1e-6f);
            const f32x4 ya = O1a[m] * ia, yb = O1b[m] * ib;
            const u32x4 o = {pk2(ya[0], ya[1]), pk2(ya[2], ya[3]), pk2(yb[0], yb[1]), pk2(yb[2], yb[3])};
            BST128(o, rs, vo, ob + m * 18432 + p * 1024, 16);
            const float ir = g == 0 ? ia : ib;
            if (g < 2) __builtin_amdgcn_raw_buffer_store_b32(pk2(O2[m][0] * ir, O2[m][1] * ir), rs, orem, ob + m * 18432, 16);
        }
    }
    STAMP(layer * 4 + 0, 6);
    STAMP_OUT(layer * 4 + 0);
    WAIT_V(0);
    {
        u32 old = 0;
        if (lane == 0) old = __hip_atomic_fetch_add((LAS u32*)(size_t)(FL + 48), 1u, __ATOMIC_RELAXED, __HIP_MEMORY_SCOPE_WORKGROUP);
        if (__builtin_amdgcn_readfirstlane(old) == 3 && lane < 16)
            __hip_atomic_fetch_add((GAS u32*)(ws + WS_CNO + ((layer * 16 + pm) * 16 + lane) * 128), 1u << p, RLX_AGENT);
    }
    STAMP(layer * 4 + 1, 0);
    const int mt = 2 * pm + (p >> 3), nt = p & 7;
    const u32 vo4 = 4096 + g * 128 + (lane & 7) * 16;
#define K2_LD(v, sof, aux) __builtin_bit_cast(bf16x8, __builtin_amdgcn_raw_buffer_load_b128(rs, v, sof, aux))
    bf16x8 fw[6][5], fa[18];
    f32x4 ac[5];
    {
        const u32 xo = WS_X + 2 * layer * XB + (4 * mt + wid) * 18432 + 2304 * nt;
        const u32x4 r0 = __builtin_amdgcn_raw_buffer_load_b128(rs, vo, xo, 0), r1 = __builtin_amdgcn_raw_buffer_load_b128(rs, vo + 1024, xo, 0);
        const u32x2 r2 = __builtin_amdgcn_raw_buffer_load_b64(rs, 2048 + c * 16 + (g & 1) * 8, xo, 0);
        ac[0] = (f32x4){bflo(r0[0]), bfhi(r0[0]), bflo(r0[1]), bfhi(r0[1])}; ac[1] = (f32x4){bflo(r0[2]), bfhi(r0[2]), bflo(r0[3]), bfhi(r0[3])};
        ac[2] = (f32x4){bflo(r1[0]), bfhi(r1[0]), bflo(r1[1]), bfhi(r1[1])}; ac[3] = (f32x4){bflo(r1[2]), bfhi(r1[2]), bflo(r1[3]), bfhi(r1[3])};
        ac[4] = (f32x4){bflo(r2[0]), bfhi(r2[0]), bflo(r2[1]), bfhi(r2[1])};
    }
    u32 wl[2] = {lds + vo, lds + vo + 41472}, wl4[2] = {lds + vo4, lds + vo4 + 41472};
    PINV(wl[0]); PINV(wl[1]); PINV(wl4[0]); PINV(wl4[1]);
#define K2_W(s, n) ((n) < 4 ? LDSR(wl[(s) / 9], ((s) % 9) * 4608 + (n) * 1024) : LDSR(wl4[(s) / 9], ((s) % 9) * 4608))
    lds_wait(FL, 13u);
#pragma unroll
    for (int sl = 0; sl < 6; ++sl)
#pragma unroll
        for (int n = 0; n < 5; ++n) { fw[sl][n] = K2_W(sl, n); SB(); }
#pragma unroll
    for (int n = 0; n < 5; ++n) PINV(ac[n]);
    lds_wait(FL + 52, 1u);
    STAMP(layer * 4 + 1, 1);
#pragma unroll
    for (int sl = 0; sl < 18; ++sl) { fa[sl] = K2_LD(vo + (sl & 3) * 1024, WS_O + LSH * layer * XB + ((4 * mt + wid) * 18 + (sl & ~3)) * 1024, 16); SB(); }
#pragma unroll
    for (int sl = 0; sl < 18; ++sl)
#pragma unroll
        for (int n = 0; n < 5; ++n) {
            MFMA(ac[n], fw[sl % 6][n], fa[sl]);
            SB();
            if (sl + 6 < 18) fw[sl % 6][n] = K2_W(sl + 6, n);
            SB();
        }
    asm volatile("s_nop 15" ::: "memory");
    STAMP(layer * 4 + 1, 3);
    proj_store(ac, ws, nullptr, 2 * layer + 1, mt, nt, wid, lane);
    STAMP(layer * 4 + 1, 2);
    STAMP_OUT(layer * 4 + 1);
}

constexpr u32 STAGE = 18432, NS = 6;
constexpr u32 K3_PK = NS * STAGE, K3_PP = 304, K3_WS = K3_PK + 128 * K3_PP, K3_S1 = K3_WS + 4096, K3_S2 = K3_S1 + 2048, K3_CS = K3_S2 + 1024, K3_FL = K3_CS + 1280, K3_LDS = K3_FL + 128;

__global__ __launch_bounds__(512, 2) void k34_mlp(char* __restrict__ ws, const u16* __restrict__ wsp, const u16* __restrict__ bsp, u16* __restrict__ out, int layer) {
    extern __shared__ __attribute__((aligned(16))) char shm_[];
    const u32 lds = (u32)(size_t)(LAS char*)shm_;
    const int tid = threadIdx.x, wid = __builtin_amdgcn_readfirstlane(tid >> 6), lane = tid & 63, c = lane & 15, g = lane >> 4;
    const int xcd = blockIdx.x & 7, bi = blockIdx.x >> 3;
    const int pm = MAP3 ? (xcd & 3) * 4 + (bi & 3) : (xcd & 1) * 8 + (bi & 7), pn = MAP3 ? (xcd >> 2) * 8 + (bi >> 2) : (xcd >> 1) * 4 + (bi >> 3);
    const u32 FL = lds + K3_FL;
    STAMP_DECL;
    const auto rs = __builtin_amdgcn_make_buffer_rsrc(ws, 0, WS_END, 0x00020000);
    if (wid == 4) {
        u32 ao[16];
        const u32 vo = lane * 16;
#pragma unroll
        for (int q = 0; q < 16; ++q) { ao[q] = vo + (q >> 1) * 18432 - (q & 2) * 1024 + 4096; PINV(ao[q]); }
        const char* ap = ws + WS_X + (2 * layer + 1) * XB + 8 * pm * 18432 - 4096;
        const char* xp = ws + WS_WIN + (144 + 9 * pn + 8) * 18432;
        u32 slot = 0;
#define K3_ISSUE() do { const u32 dst = lds + slot * STAGE;                                                          \
            DMA4(ao[0], ao[1], ao[2], ao[3], ap, dst); DMA4(ao[4], ao[5], ao[6], ao[7], ap, dst + 4096);             \
            DMA4(ao[8], ao[9], ao[10], ao[11], ap, dst + 8192); DMA4(ao[12], ao[13], ao[14], ao[15], ap, dst + 12288); } while (0)
#define K3_SEG(s0, s1, GATE) for (int s = (s0); s < (s1) + 3; ++s) {                                                 \
            if (s >= (s0) + 3) {                                                                                     \
                if (s <= (s1)) WAIT_V(36); else if (s == (s1) + 1) WAIT_V(18); else WAIT_V(0);                       \
                stsw(FL, s - 2); FENCE(); }                                                                          \
            if (s == 1) BAR();                                                                                       \
            if (s == 2) { WAIT_V(18); stsw(FL, 1u); FENCE(); }                                                       \
            if (s < (s1)) {                                                                                          \
                if (s >= (int)NS) lds_wait_sleep(FL + 16 + (lane & 3) * 4, GATE);                                    \
                if (s == 9) { ap -= 9 * 2048; xp = ws + WS_WIN + (9 * pn + 8) * 18432; }                             \
                K3_ISSUE();                                                                                          \
                DMA2(vo, vo, xp, lds + slot * STAGE + 16384);                                                        \
                xp += 2048; ap += 2048;                                                                              \
                slot = slot + 1 == NS ? 0 : slot + 1; } }
        K3_SEG(0, ZSPLIT, s - NS + 1);
        K3_SEG(ZSPLIT, 18, s >= 15 ? LGATE : max(ZGATE, s - (int)NS + 1));
#pragma unroll
        for (int q = 0; q < 16; ++q) { ao[q] = (q * 8 + (lane >> 3)) * 4096 + (((lane & 7) ^ ((q & 1) * 4 + g)) * 16) - (q & 3) * 1024 + 4096; PINV(ao[q]); }
        ap = (const char*)wsp + (size_t)pm * 128 * 4096 - 4096;
        lds_wait_sleep(FL + 16 + (lane & 3) * 4, LGATE2);
        for (int s = 18; s < 53; ++s) {
            if (s >= 21) {
                if (s <= 50) WAIT_V(32); else if (s == 51) WAIT_V(16); else WAIT_V(0);
                stsw(FL, s - 2);
                FENCE();
            }
            if (s < 50) {
                lds_wait_sleep(FL + 16 + (lane & 3) * 4, s - NS + 1);
                K3_ISSUE();
                ap += AFAKE ? 0 : (s == 33 ? 128 - 15 * 256 : 256);
                slot = slot + 1 == NS ? 0 : slot + 1;
            }
        }
        lds_wait_sleep(FL + 80, 2u);
        for (int i = 0; i < 8; ++i) {
            const int r = i < 2 ? 6 + i : (i < 6 ? i : i - 6);
            if (i >= 2) lds_wait_sleep(FL + 16 + (lane & 3) * 4, 43 + i);
            if (WGATE && i == WGI) lds_wait_sleep(FL + 92, WGATE);
            const char* wp = ws + WS_WOUT + ((MAP5 ? pn & 7 : pn >> 1) * 72 + 18 * (r >> 1) + 4 * (r & 1)) * 4608;
            const u32 dst = lds + r * STAGE;
            DMA4(vo, vo, vo, vo, wp, dst); DMA4(vo, vo, vo, vo, wp + 4096, dst + 4096); DMA4(vo, vo, vo, vo, wp + 8192, dst + 8192);
            DMA4(vo, vo, vo, vo, wp + 12288, dst + 12288); DMA2(vo, vo, wp + 16384, dst + 16384);
            if (i >= 2) WAIT_V(36);
        }
        WAIT_V(0);
        stsw(FL, 100u);
        return;
    }
    if (wid > 4) {
        const int ht = tid - 320;
        if (ht >= 128 && ht < 160) stsw(FL + (ht - 128) * 4, ht == 129 ? 18u : 0u);
        WAIT_L0();
        BAR();
        if (wid != 7) {
            float mean, var;
            x_stats(ws, 2 * layer + 1, 128 * pm + ht, mean, var);
            const float r = __builtin_amdgcn_rsqf(var + 1e-5f);
            f32x4 o = {-mean, -2.302208198f * r, -0.1029432397f * r * r * r, (var + 1e-5f) * r};
            *(LAS f32x4*)(size_t)(lds + K3_S1 + ht * 16) = o;
        } else {
            const int i = min(lane, 35);
            const f32x4* cp = (const f32x4*)(ws + WS_CSIN) + 36 * pn + i;
            const f32x4 a0 = cp[576], a1 = cp[576 + 1152], b0 = cp[0], b1 = cp[1152];
            *(LAS f32x4*)(size_t)(lds + K3_CS + i * 16) = a0 + a1;
            *(LAS f32x4*)(size_t)(lds + K3_CS + 640 + i * 16) = b0 + b1;
        }
        FENCE();
        stsw(FL + 32 + (wid - 5) * 4, 1u);
        u32 vo = lane * 16;
        PINV(vo);
        GAS u32* flg = (GAS u32*)(ws + WS_CNT + (layer * 16 + pn) * 4096);
        HST_DECL;
        if (wid != 5) {
            const int h = wid - 6;
            u32 ta = lds + K3_PK + (32 * h + 8 * g + (c >> 2)) * K3_PP + 16 * (c & 3), tx = ta + 256 - 8 * (c & 3), s2r = lds + K3_S2 + 128 * h + 32 * g;
            PINV(ta); PINV(tx); PINV(s2r);
            const u32 zob = WS_Z + LSH * layer * ZB + (9 * pn * 64 + 2 * pm + h) * 1024;
            if (layer == 0) {
                if (h == 1) lds_wait_sleep(FL + 16 + (lane & 3) * 4, TG1); else lds_wait_sleep(FL + 16 + (lane & 3) * 4, TG0);
                touch_paced<TS3>((const char*)wsp + blockIdx.x * 32768 + h * 16384, 16, vo);
            }
            lds_wait_sleep(FL + 48 + (lane & 3) * 4, 1u);
            HST(0);
            if (pm < HPRIO) __builtin_amdgcn_s_setprio(3);
            float zf[18][8];
#define K3_TR(fi, A, O) do { u32x2 t0_, t1_, t2_, t3_;                                                               \
                asm volatile("ds_read_b64_tr_b16 %0, %4 offset:%5\n\tds_read_b64_tr_b16 %1, %4 offset:%6\n\tds_read_b64_tr_b16 %2, %4 offset:%7\n\t" \
                             "ds_read_b64_tr_b16 %3, %4 offset:%8\n\ts_waitcnt lgkmcnt(0)" : "=&v"(t0_), "=&v"(t1_), "=&v"(t2_), "=&v"(t3_) \
                             : "v"(A), "i"(O), "i"((O) + 4 * K3_PP), "i"((O) + 64 * K3_PP), "i"((O) + 68 * K3_PP) : "memory"); \
                const u32 w_[8] = {t0_[0], t0_[1], t1_[0], t1_[1], t2_[0], t2_[1], t3_[0], t3_[1]};                  \
                _Pragma("unroll") for (int i_ = 0; i_ < 8; ++i_) { zf[2 * (fi) + (i_ >> 2)][2 * (i_ & 3)] = bflo(w_[i_]); zf[2 * (fi) + (i_ >> 2)][2 * (i_ & 3) + 1] = bfhi(w_[i_]); \
                    PINV(zf[2 * (fi) + (i_ >> 2)][2 * (i_ & 3)]); PINV(zf[2 * (fi) + (i_ >> 2)][2 * (i_ & 3) + 1]); } } while (0)
            K3_TR(0, ta, 0); K3_TR(1, ta, 8); K3_TR(2, ta, 64); K3_TR(3, ta, 72); K3_TR(4, ta, 128); K3_TR(5, ta, 136); K3_TR(6, ta, 192); K3_TR(7, ta, 200);
            K3_TR(8, tx, 0);
            HST(1);
            lds_wait_sleep(FL + 64, 1u);
            for (int i = 2; i < pm; ++i) __builtin_amdgcn_s_sleep(STAG);
            HST(2);
            f32x4 sv[2][4];
#pragma unroll
            for (int q = 0; q < 2; ++q)
#pragma unroll
                for (int k = 0; k < 4; ++k) sv[q][k] = *(LAS f32x4*)(size_t)(s2r + (k >> 1) * 512 + 256 * q + (k & 1) * 16);
#pragma unroll
            for (int ii = 0; ii < 18; ++ii) {
                const int q = ii / 9, i = 2 * (ii % 9) + q;
                u32x4 o;
                asm("v_fma_f32 %4, %4, %12, %20\n\tv_fma_f32 %5, %5, %13, %21\n\tv_fma_f32 %6, %6, %14, %22\n\tv_fma_f32 %7, %7, %15, %23\n\t"
                    "v_fma_f32 %8, %8, %16, %24\n\tv_fma_f32 %9, %9, %17, %25\n\tv_fma_f32 %10, %10, %18, %26\n\tv_fma_f32 %11, %11, %19, %27\n\t"
                    "v_cvt_pk_bf16_f32 %0, %4, %5\n\tv_cvt_pk_bf16_f32 %1, %6, %7\n\tv_cvt_pk_bf16_f32 %2, %8, %9\n\tv_cvt_pk_bf16_f32 %3, %10, %11"
                    : "=&v"(o[0]), "=&v"(o[1]), "=&v"(o[2]), "=&v"(o[3]), "+v"(zf[i][0]), "+v"(zf[i][1]), "+v"(zf[i][2]), "+v"(zf[i][3]), "+v"(zf[i][4]), "+v"(zf[i][5]), "+v"(zf[i][6]), "+v"(zf[i][7])
                    : "v"(sv[q][0][0]), "v"(sv[q][0][1]), "v"(sv[q][0][2]), "v"(sv[q][0][3]), "v"(sv[q][1][0]), "v"(sv[q][1][1]), "v"(sv[q][1][2]), "v"(sv[q][1][3]),
                      "v"(sv[q][2][0]), "v"(sv[q][2][1]), "v"(sv[q][2][2]), "v"(sv[q][2][3]), "v"(sv[q][3][0]), "v"(sv[q][3][1]), "v"(sv[q][3][2]), "v"(sv[q][3][3]));
                __builtin_amdgcn_raw_buffer_store_b128(o, rs, vo, zob + q * 32768 + (i >> 1) * 65536, 16);
                SB();
                if (ii % 9 == 8) {
                    if (q == 0) HST(3);
                    WAIT_V(0);
                    if (q == 0) HST(4);
                    u32 old = 0;
                    if (lane == 0) old = __hip_atomic_fetch_add((LAS u32*)(size_t)(FL + 76 + 4 * q), 1u, __ATOMIC_RELAXED, __HIP_MEMORY_SCOPE_WORKGROUP);
                    if (__builtin_amdgcn_readfirstlane(old) == 1 && lane < 16)
                        __hip_atomic_fetch_add(flg + lane * 64, 1u << (16 * q + pm), RLX_AGENT);
                    if (q == 0) { __builtin_amdgcn_s_setprio(0); __builtin_amdgcn_s_sleep(STAG2); }
                    SB();
                }
            }
            if (HPW && layer == 0) {
                lds_wait_sleep(FL + 16 + (lane & 3) * 4, HPW);
                const u32x4 z4 = {0u, 0u, 0u, 0u};
#pragma nounroll
                for (int i = 0; i < 16; ++i) {
                    BST128(z4, rs, vo, WS_H + ((8 * pm + 4 * h + (i >> 2)) * 8 + (pn >> 1)) * 9216 + ((pn & 1) * 4 + (i & 3)) * 1024, 16);
                    __builtin_amdgcn_s_sleep(HPS);
                }
                WAIT_V(0);
            }
            stsw(FL + 112 + 4 * h, 1u);
            HST(5);
            if (wid == 6) HST_OUT(9 + layer);
            return;
        }
        u32 wsa = lds + K3_WS + lane * 16, s2w = lds + K3_S2 + lane * 8;
        PINV(wsa); PINV(s2w);
        const u32 zsb = WS_ZST + (layer * 16 + pm) * 16384;
        lds_wait_sleep(FL + 16 + (lane & 3) * 4, 9u);
        __builtin_amdgcn_s_sleep(POLLD);
        __builtin_amdgcn_s_setprio(3);
        lds_wait(FL + 48 + (lane & 3) * 4, 1u);
        STAMP(layer * 4 + 2, 8);
        {
            const f32x4 st = sadd4(sadd4(*(LAS f32x4*)(size_t)wsa, *(LAS f32x4*)(size_t)(wsa + 1024)), sadd4(*(LAS f32x4*)(size_t)(wsa + 2048), *(LAS f32x4*)(size_t)(wsa + 3072)));
            u32x4 o = __builtin_bit_cast(u32x4, st);
#pragma unroll
            for (int k = 0; k < 4; ++k) o[k] = o[k] == SENT ? 0x7fc00000u : o[k];
            BST128(o, rs, vo, zsb + pn * 1024, 16);
            __builtin_amdgcn_s_sleep(XDELAY3);
            u32x4 t[16];
#pragma unroll
            for (int k = 0; k < 16; ++k) t[k] = __builtin_amdgcn_raw_buffer_load_b128(rs, vo + (k & 3) * 1024, zsb + (k >> 2) * 4096, 16);
            u32 mx = 0;
#pragma unroll
            for (int k = 0; k < 16; ++k) mx = max(max(mx, max(t[k][0], t[k][1])), max(t[k][2], t[k][3]));
            u32 spins = 0;
            if (__builtin_expect(ANY(mx == SENT), 0)) {
#pragma nounroll
                for (spins = 1; spins < SPIN_CAP; ++spins) {
                    FENCE();
                    u32 miss = 0;
#pragma unroll
                    for (int k = 0; k < 16; ++k) miss |= ANY(max(max(t[k][0], t[k][1]), max(t[k][2], t[k][3])) == SENT) ? 1u << k : 0u;
                    if (!miss) break;
#pragma unroll
                    for (int k = 0; k < 16; ++k) if (miss >> k & 1) t[k] = __builtin_amdgcn_raw_buffer_load_b128(rs, vo + (k & 3) * 1024, zsb + (k >> 2) * 4096, 16);
                }
            }
            f32x4 sa[4];
#pragma unroll
            for (int k = 0; k < 4; ++k)
                sa[k] = sadd4(sadd4(__builtin_bit_cast(f32x4, t[4 * k]), __builtin_bit_cast(f32x4, t[4 * k + 1])), sadd4(__builtin_bit_cast(f32x4, t[4 * k + 2]), __builtin_bit_cast(f32x4, t[4 * k + 3])));
            const f32x4 ss = sadd4(sadd4(sa[0], sa[1]), sadd4(sa[2], sa[3]));
            f32x2 rr, mr;
#pragma unroll
            for (int k = 0; k < 2; ++k) {
                const float m2 = sfma(ss[2 * k], 1.f / 2304.f, 0.f);
                float v2 = fmaxf(sfma(-m2, m2, sfma(ss[2 * k + 1], 1.f / 2304.f, 0.f)), 0.f) + 1e-5f;
                PINV(v2);
                rr[k] = __builtin_amdgcn_rsqf(v2);
                float t2 = -m2 * rr[k];
                PINV(t2);
                mr[k] = t2;
            }
            *(LAS f32x2*)(size_t)s2w = rr;
            *(LAS f32x2*)(size_t)(s2w + 512) = mr;
            FENCE();
            stsw(FL + 64, 1u);
            FENCE();
        }
        STAMP(layer * 4 + 2, 9);
        STAMP(layer * 4 + 2, 10);
        u32 ready = 0;
#define K3_NEED(n) do { _Pragma("nounroll") for (u32 spins_ = 0; ready < (u32)(n) && spins_ < SPIN_CAP; ++spins_) {   \
            const u32 v_ = __builtin_amdgcn_readfirstlane(__hip_atomic_load(flg + pm * 64, RLX_AGENT));                                               \
            ready = __builtin_ctzll(~(u64)v_); } FENCE(); } while (0)
        __builtin_amdgcn_s_sleep(FDELAY);
        K3_NEED(3);
        stsw(FL + 84, 1u);
        FENCE();
        STAMP(layer * 4 + 2, 11);
        STAMP_OUT(layer * 4 + 2);
        __builtin_amdgcn_s_setprio(0);
        const u32 zx = WS_Z + LSH * layer * ZB + (9 * pn + 8) * 65536;
        u32x4 xr[4][2];
#pragma unroll
        for (int k = 0; k < 3; ++k) {
            xr[k][0] = __builtin_amdgcn_raw_buffer_load_b128(rs, vo, zx + k * 2048, 16);
            xr[k][1] = __builtin_amdgcn_raw_buffer_load_b128(rs, vo + 1024, zx + k * 2048, 16);
            SB();
        }
        u32 slot = 0;
        for (int s0 = 0; s0 < 32; s0 += 4) {
#pragma unroll
            for (int k = 0; k < 4; ++k) {
                const int s = s0 + k;
                lds_wait_sleep(FL + 16 + (lane & 3) * 4, 18 + s - NS + 1);
                *(LAS u32x4*)(size_t)(lds + slot * STAGE + 16384 + lane * 16) = xr[k][0];
                *(LAS u32x4*)(size_t)(lds + slot * STAGE + 17408 + lane * 16) = xr[k][1];
                FENCE();
                stsw(FL + 4, 18 + s + 1);
                FENCE();
                const u32 sn = s + 3 < 32 ? s + 3 : 31;
                K3_NEED(sn + 1);
                xr[(k + 3) & 3][0] = __builtin_amdgcn_raw_buffer_load_b128(rs, vo, zx + sn * 2048, 16);
                xr[(k + 3) & 3][1] = __builtin_amdgcn_raw_buffer_load_b128(rs, vo + 1024, zx + sn * 2048, 16);
                slot = slot + 1 == NS ? 0 : slot + 1;
            }
        }
        lds_wait_sleep(FL + 92, 4u);
        __builtin_amdgcn_s_sleep(HDELAY);
        GAS u32* hm = (GAS u32*)(ws + WS_CNT + (layer * 16 + pm) * 4096 + pn * 256 + 128);
#pragma nounroll
        for (u32 spins = 0; spins < SPIN_CAP; ++spins) {
            const u32 v = __builtin_amdgcn_readfirstlane(__hip_atomic_load(hm, RLX_AGENT));
            FENCE();
            stsw(FL + 88, v);
            if (v == 0xffffu) break;
        }
        return;
    }
    STAMP(layer * 4 + 2, 0);
    u32 vA = lane * 16, vB = lane * 16 + 18432;
    u32 so = WS_WIN + (144 + 9 * pn + 2 * wid) * 18432;
    bf16x8 bb[6][2];
#define BLD(voff, imm, aux) __builtin_bit_cast(bf16x8, __builtin_amdgcn_raw_buffer_load_b128(rs, (voff) + (imm), so, aux))
#define BPRO(aux) do { _Pragma("unroll") for (int r = 0; r < 5; ++r) { if (r == 3) so += 3072;                       \
        bb[r][0] = BLD(vA, (r % 3) * 1024, aux); SB(); bb[r][1] = BLD(vB, (r % 3) * 1024, aux); SB(); } } while (0)
    BPRO(0);
    u32 pa[2][4];
#pragma unroll
    for (int q = 0; q < 4; ++q) { pa[0][q] = lds + lane * 16 + ((q ^ wid) * 4096); pa[1][q] = pa[0][q] + 1024; }
    u32 px = lds + 16384 + lane * 16;
    u32 myfree = FL + 16 + wid * 4, fladdr = FL + (lane & 1) * 4, cnt = 0;
    PINV(cnt); PINV(myfree); PINV(fladdr);
    int dstep = 3 * STAGE;
    f32x4 acc[8][2], accx[2];
    bf16x8 a[8], xf;
#define ALOAD() do { a[0] = LDSR(pa[0][0], 0); SB(); xf = LDSR(px, 0); SB();                                         \
        _Pragma("unroll") for (int m = 1; m < 8; ++m) { a[m] = LDSR(pa[0][m >> 1], (m & 1) * 2048); SB(); } } while (0)
#define MFS(acc, x, y) do { MFMA(acc, x, y); SB(); } while (0)
#define HALF(r, RD, kn, sf, rl, imm, aux, E0, E1, E2, E3, E4, E5) do {                                               \
        WCNT(8, 5); MFS(acc[0][0], bb[r][0], a[0]); E0; SB();                                                       \
        MFS(acc[0][1], bb[r][1], a[0]); E1; SB();                                                                   \
        MFS(accx[0], xf, a[0]); if (RD) a[0] = LDSR(pa[kn][0], (sf)); SB();                                         \
        MFS(accx[1], xf, a[1]); if (RD) xf = LDSR(px, (sf) + (kn) * 1024); SB();                                    \
        MFS(acc[1][1], bb[r][1], a[1]); bb[rl][0] = BLD(vA, imm, aux); SB();                                        \
        MFS(acc[1][0], bb[r][0], a[1]); if (RD) a[1] = LDSR(pa[kn][0], (sf) + 2048); SB();                          \
        MFS(acc[2][0], bb[r][0], a[2]); E2; SB();                                                                   \
        MFS(acc[2][1], bb[r][1], a[2]); if (RD) a[2] = LDSR(pa[kn][1], (sf)); SB();                                 \
        WCNT(63, 5); MFS(acc[3][1], bb[r][1], a[3]); SB();                                                          \
        MFS(acc[3][0], bb[r][0], a[3]); if (RD) a[3] = LDSR(pa[kn][1], (sf) + 2048); SB();                          \
        MFS(acc[4][0], bb[r][0], a[4]); bb[rl][1] = BLD(vB, imm, aux); SB();                                        \
        MFS(acc[4][1], bb[r][1], a[4]); if (RD) a[4] = LDSR(pa[kn][2], (sf)); SB();                                 \
        MFS(acc[5][1], bb[r][1], a[5]); E3; SB();                                                                   \
        MFS(acc[5][0], bb[r][0], a[5]); if (RD) a[5] = LDSR(pa[kn][2], (sf) + 2048); SB();                          \
        MFS(acc[6][0], bb[r][0], a[6]); E4; SB();                                                                   \
        MFS(acc[6][1], bb[r][1], a[6]); if (RD) a[6] = LDSR(pa[kn][3], (sf)); SB();                                 \
        MFS(acc[7][1], bb[r][1], a[7]); E5; SB();                                                                   \
        MFS(acc[7][0], bb[r][0], a[7]); if (RD) a[7] = LDSR(pa[kn][3], (sf) + 2048); SB(); } while (0)
#define STEP(u, chk, nso, aux) do { u32 fl_; u64 msk_;                                                               \
        HALF(2 * (u), 1, 1, (u) * STAGE, (2 * (u) + 5) % 6, ((2 * (u) + 5) % 3) * 1024, aux,                         \
             { if ((u) == 2) so += 3072; }, { cnt += 1; PINV(cnt); }, { FENCE(); fl_ = ldsw(fladdr); FENCE(); },     \
             { if ((u) == 2) { pa[0][0] += dstep; pa[0][1] += dstep; PINV(pa[0][0]); PINV(pa[0][1]); } },            \
             { if ((u) == 2) { pa[0][2] += dstep; pa[0][3] += dstep; PINV(pa[0][2]); PINV(pa[0][3]); } },            \
             { asm volatile("v_cmp_le_u32 %0, %1, %2" : "=s"(msk_) : "v"(fl_), "v"(cnt)); if ((u) == 2) { px += dstep; PINV(px); } }); \
        HALF(2 * (u) + 1, 1, 0, (((u) + 1) % 3) * STAGE, (2 * (u) + 6) % 6, ((2 * (u) + 6) % 3) * 1024, aux,         \
             { if (__builtin_expect(msk_ != 0, 0)) { if (chk) lds_wait_gt(fladdr, cnt); } FENCE(); }, { if ((u) == 0) so = (nso); }, \
             { FENCE(); stsw(myfree, cnt); FENCE(); },                                                               \
             { if ((u) == 2) { pa[1][0] += dstep; pa[1][1] += dstep; PINV(pa[1][0]); PINV(pa[1][1]); } },            \
             { if ((u) == 2) { pa[1][2] += dstep; pa[1][3] += dstep; PINV(pa[1][2]); PINV(pa[1][3]); } },            \
             { if ((u) == 2) dstep = -dstep; }); } while (0)
#define PINACC() do { _Pragma("unroll") for (int m = 0; m < 8; ++m) { PINV(acc[m][0]); PINV(acc[m][1]); } PINV(accx[0]); PINV(accx[1]); asm volatile("s_nop 1"); } while (0)
    BAR();
    u32 yp[18][2], early = 0;
    const u32 s1a = lds + K3_S1 + c * 16;
#pragma unroll
    for (int ph = 0; ph < 2; ++ph) {
        if (ph == 0) {
#pragma unroll
            for (int m = 0; m < 8; ++m) { acc[m][0] = acc[m][1] = (f32x4){0.f, 0.f, 0.f, 0.f}; if (m < 2) accx[m] = acc[m][0]; }
        } else {
            const u32 ca = lds + K3_CS + ph * 640 + g * 16;
            const f32x4 c0 = *(LAS f32x4*)(size_t)(ca + wid * 128), c1 = *(LAS f32x4*)(size_t)(ca + wid * 128 + 64), c2 = *(LAS f32x4*)(size_t)(ca + 512);
#pragma unroll
            for (int m = 0; m < 8; ++m) {
                const float nm = *(LAS float*)(size_t)(s1a + ((((m >> 1) ^ wid) << 1) + (m & 1)) * 256);
                acc[m][0] = c0 * nm; acc[m][1] = c1 * nm;
                if (m < 2) accx[m] = c2 * nm;
            }
        }
        PINACC();
        if (ph == 0) { lds_wait(fladdr, 1u); ALOAD(); STAMP(layer * 4 + 2, 1); }
        const u32 nxt = WS_WIN + (9 * pn + 2 * wid) * 18432;
        for (int j = 0; j < 3; ++j) {
            STEP(0, true, j == 2 ? nxt : so + 3072, 0);
            STEP(1, true, 0, 0);
            STEP(2, ph == 0 || j < 2, 0, 0);
        }
        asm volatile("s_nop 15" ::: "memory");
        STAMP(layer * 4 + 2, 2 + 2 * ph);
        if (ph == 1) {
            vB = lane * 16 + 65536;
            so = WS_Z + LSH * layer * ZB + (9 * pn + 2 * wid) * 65536;
            FENCE();
            early = __builtin_amdgcn_readfirstlane(ldsw(FL + 84));
            FENCE();
            if (early) BPRO(16);
        }
        float t1[8], t2[8];
        f32x4 rcs[8], cz[3];
        if (ph == 0) {
            lds_wait(FL + 32 + (lane % 3) * 4, 1u);
            const u32 ca = lds + K3_CS + g * 16;
            cz[0] = *(LAS f32x4*)(size_t)(ca + wid * 128); cz[1] = *(LAS f32x4*)(size_t)(ca + wid * 128 + 64); cz[2] = *(LAS f32x4*)(size_t)(ca + 512);
        }
#pragma unroll
        for (int m = 0; m < 8; ++m) rcs[m] = *(LAS f32x4*)(size_t)(s1a + ((((m >> 1) ^ wid) << 1) + (m & 1)) * 256);
#pragma unroll
        for (int m = 0; m < 8; ++m) {
            if (ph == 1 && m == 4 && !early) { FENCE(); early = __builtin_amdgcn_readfirstlane(ldsw(FL + 84)); FENCE(); if (early) BPRO(16); }
            constexpr int NP = 6;
            const int np = m < 2 ? 6 : 4;
            const f32x2 k1 = {rcs[m][1], rcs[m][1]}, k3 = {rcs[m][2], rcs[m][2]}, sg = {rcs[m][3], rcs[m][3]};
            f32x2 v[NP], e[NP];
#pragma unroll
            for (int i = 0; i < NP; ++i) if (i < np) {
                const f32x4 av = i < 4 ? acc[m][i >> 1] : accx[m & 1];
                v[i] = (f32x2){av[2 * (i & 1)], av[2 * (i & 1) + 1]};
                if (ph == 0) v[i] += (f32x2){cz[i >> 1][2 * (i & 1)], cz[i >> 1][2 * (i & 1) + 1]} * (f32x2){rcs[m][0], rcs[m][0]};
            }
#pragma unroll
            for (int i = 0; i < NP; ++i) if (i < np) e[i] = v[i] * v[i];
#pragma unroll
            for (int i = 0; i < NP; ++i) if (i < np) e[i] = e[i] * k3 + k1;
#pragma unroll
            for (int i = 0; i < NP; ++i) if (i < np) e[i] = e[i] * v[i];
            SB();
#pragma unroll
            for (int i = 0; i < NP; ++i) if (i < np) { e[i][0] = __builtin_amdgcn_exp2f(e[i][0]); e[i][1] = __builtin_amdgcn_exp2f(e[i][1]); }
            SB();
#pragma unroll
            for (int i = 0; i < NP; ++i) if (i < np) e[i] = e[i] * sg + sg;
            SB();
#pragma unroll
            for (int i = 0; i < NP; ++i) if (i < np) { e[i][0] = __builtin_amdgcn_rcpf(e[i][0]); e[i][1] = __builtin_amdgcn_rcpf(e[i][1]); }
            SB();
#pragma unroll
            for (int i = 0; i < NP; ++i) if (i < np) v[i] = v[i] * e[i];
#pragma unroll
            for (int i = 0; i < NP; ++i) if (i < np) yp[i < 4 ? 2 * m + (i >> 1) : 16 + m][i & 1] = pk2(v[i][0], v[i][1]);
            if (ph == 0) {
                f32x2 u1 = (v[0] + v[1]) + (v[2] + v[3]), u2 = v[0] * v[0], u3 = v[1] * v[1];
                u2 = v[2] * v[2] + u2; u3 = v[3] * v[3] + u3;
                if (m < 2) { u1 += v[4] + v[5]; u2 = v[4] * v[4] + u2; u3 = v[5] * v[5] + u3; }
                u2 += u3;
                t1[m] = u1[0] + u1[1]; t2[m] = u2[0] + u2[1];
            }
            PINV(yp[2 * m][0]); PINV(yp[2 * m][1]); PINV(yp[2 * m + 1][0]); PINV(yp[2 * m + 1][1]);
            if (m < 2) { PINV(yp[16 + m][0]); PINV(yp[16 + m][1]); }
            if (ph == 0) {
                const u32 row = (((((m >> 1) ^ wid) << 1) + (m & 1)) << 4) + c;
                u32x4 o = {yp[2 * m][0], yp[2 * m][1], yp[2 * m + 1][0], yp[2 * m + 1][1]};
                *(LAS u32x4*)(size_t)(lds + K3_PK + row * K3_PP + 64 * wid + 16 * g) = o;
                if (m < 2) { u32x2 ox = {yp[16 + m][0], yp[16 + m][1]}; *(LAS u32x2*)(size_t)(lds + K3_PK + row * K3_PP + 256 + 8 * g) = ox; }
            }
        }
        if (ph == 0) {
#pragma unroll
            for (int m = 0; m < 4; ++m) {
                auto r1 = __builtin_amdgcn_permlane32_swap(F2U(t1[m]), F2U(t1[m + 4]), false, false);
                auto r2 = __builtin_amdgcn_permlane32_swap(F2U(t2[m]), F2U(t2[m + 4]), false, false);
                t1[m] = U2F(r1[0]) + U2F(r1[1]); t2[m] = U2F(r2[0]) + U2F(r2[1]);
            }
#pragma unroll
            for (int m = 0; m < 2; ++m) {
                auto r1 = __builtin_amdgcn_permlane16_swap(F2U(t1[m]), F2U(t1[m + 2]), false, false);
                auto r2 = __builtin_amdgcn_permlane16_swap(F2U(t2[m]), F2U(t2[m + 2]), false, false);
                f32x2 o = {U2F(r1[0]) + U2F(r1[1]), U2F(r2[0]) + U2F(r2[1])};
                *(LAS f32x2*)(size_t)(lds + K3_WS + (wid * 128 + 32 * (g ^ wid) + 16 * m + c) * 8) = o;
            }
            FENCE();
            stsw(FL + 48 + wid * 4, 1u);
            FENCE();
        }
        STAMP(layer * 4 + 2, 3 + 2 * ph);
    }
    {
        float bv[8];
#pragma unroll
        for (int m = 0; m < 8; ++m) bv[m] = bflo(bsp[128 * pm + (((((m >> 1) ^ wid) << 1) + (m & 1)) << 4) + c]);
#pragma unroll
        for (int m = 0; m < 8; ++m) {
            acc[m][0] = acc[m][1] = (f32x4){bv[m], bv[m], bv[m], bv[m]};
            if (m < 2) accx[m] = acc[m][0];
        }
    }
#pragma unroll
    for (int q = 0; q < 4; ++q) { pa[0][q] = lds + (c >> 3) * 1024 + (c & 7) * 128 + ((g ^ ((c >> 1) & 7)) * 16) + ((q ^ wid) * 4096); pa[1][q] = pa[0][q] ^ 64; }
    if (!early) { lds_wait(FL + 84, 1u); BPRO(16); }
    lds_wait(fladdr, 19u);
    STAMP(layer * 4 + 2, 6);
    ALOAD();
    PINACC();
    for (int j = 0; j < 10; ++j) {
        STEP(0, true, so + BADV, BAUX);
        STEP(1, true, 0, BAUX);
        STEP(2, true, 0, BAUX);
    }
    if (BADV > 0) so -= 8192;
    STEP(0, true, so, 16);
    STEP(1, false, 0, 16);
    asm volatile("s_nop 15" ::: "memory");
    STAMP(layer * 4 + 2, 7);
    {
        lds_wait(FL + 112 + (lane & 1) * 4, 1u);
        const u32 hb = WS_H + LSH * layer * ZB + (8 * pm * 8 + (pn >> 1)) * 9216;
#pragma unroll
        for (int m = 0; m < 8; ++m) {
            const int mt = (((m >> 1) ^ wid) << 1) + (m & 1);
            u32x4 o;
            o[0] = pk2(bflo(yp[2 * m][0]) * acc[m][0][0], bfhi(yp[2 * m][0]) * acc[m][0][1]);
            o[1] = pk2(bflo(yp[2 * m][1]) * acc[m][0][2], bfhi(yp[2 * m][1]) * acc[m][0][3]);
            o[2] = pk2(bflo(yp[2 * m + 1][0]) * acc[m][1][0], bfhi(yp[2 * m + 1][0]) * acc[m][1][1]);
            o[3] = pk2(bflo(yp[2 * m + 1][1]) * acc[m][1][2], bfhi(yp[2 * m + 1][1]) * acc[m][1][3]);
            BST128(o, rs, vA, hb + mt * 73728 + ((pn & 1) * 4 + wid) * 1024, 16);
            if (m < 2) {
                u32x2 ox = {pk2(bflo(yp[16 + m][0]) * accx[m][0], bfhi(yp[16 + m][0]) * accx[m][1]), pk2(bflo(yp[16 + m][1]) * accx[m][2], bfhi(yp[16 + m][1]) * accx[m][3])};
                __builtin_amdgcn_raw_buffer_store_b64(ox, rs, vA, hb + mt * 73728 + 8192 + (pn & 1) * 8, 16);
            }
        }
        STAMP(layer * 4 + 2, 12);
        WAIT_V(0);
        u32 old = 0;
        if (lane == 0) old = __hip_atomic_fetch_add((LAS u32*)(size_t)(FL + 92), 1u, __ATOMIC_RELAXED, __HIP_MEMORY_SCOPE_WORKGROUP);
        if (__builtin_amdgcn_readfirstlane(old) == 3 && lane < 16)
            __hip_atomic_fetch_add((GAS u32*)(ws + WS_CNT + (layer * 16 + pm) * 4096 + lane * 256 + 128), 1u << pn, RLX_AGENT);
    }
    STAMP_OUT(layer * 4 + 2);
    STAMP(layer * 4 + 3, 0);
    {
        const int mt = 2 * pm + (MAP5 ? pn >> 3 : pn & 1), nt = MAP5 ? pn & 7 : pn >> 1;
        const u32 wb = WS_WOUT + (nt * 72 + 18 * wid) * 4608;
        const u32 vo4 = 4096 + g * 128 + (lane & 7) * 16;
        bf16x8 fa[4][4], fw[3][5], fl[5];
        f32x4 ac[4][5];
#pragma unroll
        for (int m = 0; m < 4; ++m)
#pragma unroll
            for (int n = 0; n < 5; ++n) ac[m][n] = (f32x4){0.f, 0.f, 0.f, 0.f};
        u32 abm[4];
#pragma unroll
        for (int m = 0; m < 4; ++m) abm[m] = WS_H + LSH * layer * ZB + ((4 * mt + (m ^ wid)) * 8 + 2 * wid) * 9216;
#define K5_LD(v, sof, aux) __builtin_bit_cast(bf16x8, __builtin_amdgcn_raw_buffer_load_b128(rs, v, sof, aux))
#define K5_LW(r, s) do { _Pragma("unroll") for (int n = 0; n < 4; ++n) fw[r][n] = K5_LD(vA + n * 1024, wb + (s) * 4608, 0); fw[r][4] = K5_LD(vo4, wb + (s) * 4608, 0); SB(); } while (0)
#define K5_LL(s) do { _Pragma("unroll") for (int n = 0; n < 4; ++n) fl[n] = LDSR(wl, (s) * 4608 + n * 1024); fl[4] = LDSR(wl4, (s) * 4608); SB(); } while (0)
#define K5_LA(r, s) do { _Pragma("unroll") for (int m = 0; m < 4; ++m) fa[r][m] = K5_LD(vA + ((s) & 3) * 1024, abm[m] + ((s) & ~3) * 1024, 16); SB(); } while (0)
#define K5_MM(W, r) do { _Pragma("unroll") for (int m = 0; m < 4; ++m) _Pragma("unroll") for (int n = 0; n < 5; ++n) ac[m][n] = MFMAB(W[n], fa[r][m], ac[m][n]); SB(); } while (0)
        PINV(vA);
        const u32 wl = lds + wid * 2 * STAGE + vA, wl4 = lds + wid * 2 * STAGE + vo4;
        K5_LW(0, 8); K5_LW(1, 9); K5_LW(2, 10);
        u32x4 res[2]; u32x2 res2;
        const u32 xoff = WS_X + (2 * layer + 1) * XB + (4 * mt + wid) * 18432;
        {
            const u32 need = 15u << (4 * wid);
            FENCE();
#pragma nounroll
            for (u32 n = 0; n < SPIN_CAP; ++n) { if ((__builtin_amdgcn_readfirstlane(ldsw(FL + 88)) & need) == need) break; }
            FENCE();
        }
        STAMP(layer * 4 + 3, 1);
        constexpr int ord[18] = {8, 0, 9, 1, 10, 2, 11, 3, 12, 4, 13, 5, 14, 6, 15, 7, 16, 17};
#pragma unroll
        for (int i = 0; i < 4; ++i) K5_LA(i, ord[i]);
        lds_wait(FL, 100u);
#pragma unroll
        for (int m = 0; m < 4; ++m)
#pragma unroll
            for (int n = 0; n < 5; ++n) PINV(ac[m][n]);
        asm volatile("s_nop 1");
#pragma unroll
        for (int i = 0; i < 18; ++i) {
            const int sl = ord[i];
#pragma unroll
            for (int m = 0; m < 4; ++m)
#pragma unroll
                for (int n = 0; n < 5; ++n) {
                    if (sl >= 8) MFS(ac[m][n], fw[(sl - 8) % 3][n], fa[i & 3][m]); else MFS(ac[m][n], fl[n], fa[i & 3][m]);
                    const int e = (n == 1 ? m : (m == 0 && n == 3 ? 4 : -1));
                    if (e >= 0) {
                        if (sl >= 8 && sl < 16) fl[e] = e < 4 ? LDSR(wl, (sl - 8) * 4608 + e * 1024) : LDSR(wl4, (sl - 8) * 4608);
                        if (sl < 7) fw[sl % 3][e] = e < 4 ? K5_LD(vA + e * 1024, wb + (sl + 11) * 4608, 0) : K5_LD(vo4, wb + (sl + 11) * 4608, 0);
                        if (sl == 7 && e == 0) res[0] = __builtin_amdgcn_raw_buffer_load_b128(rs, vA, xoff + 2304 * nt, 0);
                        if (sl == 7 && e == 1) res[1] = __builtin_amdgcn_raw_buffer_load_b128(rs, vA + 1024, xoff + 2304 * nt, 0);
                        if (sl == 7 && e == 2) res2 = __builtin_amdgcn_raw_buffer_load_b64(rs, (vA & 255) + ((vA >> 5) & 8) + 2048, xoff + 2304 * nt, 0);
                    }
                    if (n == 4 && i + 4 < 18) fa[i & 3][m] = K5_LD(vA + (ord[i + 4 < 18 ? i + 4 : 0] & 3) * 1024, abm[m] + (ord[i + 4 < 18 ? i + 4 : 0] & ~3) * 1024, 16);
                    SB();
                }
        }
        asm volatile("s_nop 15" ::: "memory");
        STAMP(layer * 4 + 3, 3);
        proj_tail(ac, res, res2, lds, 2 * STAGE, FL + 96, ws, out, 2 * layer + 2, mt, nt, wid, lane);
    }
    STAMP(layer * 4 + 3, 2);
    STAMP_OUT(layer * 4 + 3);
}

extern "C" __global__ void k9_end() {}

extern "C" void kernel_launch(void* const* d_in, const int* in_sizes, int n_in, void* d_out, int out_size, void* d_ws, size_t ws_size,
                              hipStream_t stream) {
    const u16* x = (const u16*)d_in[0];
    const u16* wqkv = (const u16*)d_in[1];
    const u16* wo = (const u16*)d_in[2];
    const u16* win = (const u16*)d_in[4];
    const u16* wsp = (const u16*)d_in[5];
    const u16* bsp = (const u16*)d_in[6];
    const u16* wout = (const u16*)d_in[7];
    char* ws = (char*)d_ws + WS_BASE(ws_size);
    u16* out = (u16*)d_out;
    (void)hipFuncSetAttribute((const void*)k1_attn, hipFuncAttributeMaxDynamicSharedMemorySize, K1_LDS);
    (void)hipFuncSetAttribute((const void*)k34_mlp, hipFuncAttributeMaxDynamicSharedMemorySize, K3_LDS);
    k0_prep<<<K0_GRID, 576, 0, stream>>>(x, wqkv, wo, win, wout, wsp, ws);
    for (int layer = 0; layer < 2; ++layer) {
        k1_attn<<<256, 512, K1_LDS, stream>>>(ws, layer);
        k34_mlp<<<256, 512, K3_LDS, stream>>>(ws, wsp, bsp, out, layer);
    }
}
```

```cpp
#include <hip/hip_runtime.h>
#include <hip/hip_bf16.h>
#include <stdint.h>

#define LAS __attribute__((address_space(3)))
#define GAS __attribute__((address_space(1)))
typedef unsigned short u16;
typedef unsigned u32;
typedef unsigned long long u64;
typedef short bf16x8 __attribute__((ext_vector_type(8)));
typedef float f32x2 __attribute__((ext_vector_type(2)));
typedef float f32x4 __attribute__((ext_vector_type(4)));
typedef unsigned u32x2 __attribute__((ext_vector_type(2)));
typedef unsigned u32x4 __attribute__((ext_vector_type(4)));
typedef __bf16 bf2_t __attribute__((ext_vector_type(2)));

#define ANY(p) (__builtin_amdgcn_ballot_w64(p) != 0)
#define U2F(x) __builtin_bit_cast(float, (u32)(x))
#define F2U(x) __builtin_bit_cast(u32, (float)(x))
#define WAIT_V(n) asm volatile("s_waitcnt vmcnt(%0)" ::"i"(n) : "memory")
#define WAIT_L0() asm volatile("s_waitcnt lgkmcnt(0)" ::: "memory")
#define FENCE() asm volatile("" ::: "memory")
#define SB() __builtin_amdgcn_sched_barrier(0)
#define BAR() do { FENCE(); __builtin_amdgcn_s_barrier(); FENCE(); } while (0)
#define WCNT(vm, lg) __builtin_amdgcn_s_waitcnt(((vm) & 15) | (7 << 4) | ((lg) << 8) | (((vm) >> 4) << 14))
#define MFMA(acc, x, y) asm volatile("v_mfma_f32_16x16x32_bf16 %0, %1, %2, %0" : "+v"(acc) : "v"(x), "v"(y))
#define MFMAB(x, y, c) __builtin_amdgcn_mfma_f32_16x16x32_bf16(x, y, c, 0, 0, 0)
#define BST128(o, rs, vo, so, aux) do { __builtin_amdgcn_raw_buffer_store_b128(o, rs, vo, so, aux); SB(); asm volatile("s_nop 1"); SB(); } while (0)
#define PINV(x) asm volatile("" : "+v"(x))
#define RLX_AGENT __ATOMIC_RELAXED, __HIP_MEMORY_SCOPE_AGENT

constexpr int S_ = 2048, D_ = 576, FH_ = 2304;
#define XDELAY1 18
#define XDELAY3 20
#define LGATE 12
#define LGATE2 14
#define POLLD 40
#define STAG 3
#define HPRIO 3
#define FDELAY 45
#define STAG2 60
#define MAP1 1
#define MAP3 1
#define CODETOUCH 1
#define WSPTOUCH 0
#define LSH 0
#define L2PF 0
#define ZSPLIT 10
#define ZGATE 8
#define QLATE 0
#define BADV 3072
#define AFAKE 0
#define BAUX 16
#define HDELAY 10
#define MAP5 1
#define WGATE 0
#define WGI 7
#define WEARLY 0
#define ODELAY 12
#define K0NT 1
#define NTL(p) __builtin_nontemporal_load(p)
#define TS3 2
#define TG0 6
#define TG1 6
#define HPW 0
#define HPS 4
constexpr u32 SENT = 0xFFFFFFFFu;
constexpr u32 SPIN_CAP = 1u << 16;

constexpr u32 WS_WQ = 0;
constexpr u32 WS_CSQ = WS_WQ + 16 * 129024;
constexpr u32 WS_WO = WS_CSQ + 18 * 8192;
constexpr u32 WS_WIN = WS_WO + 8 * 18 * 4608;
constexpr u32 WS_CSIN = WS_WIN + 288 * 18 * 1024;
constexpr u32 WS_WOUT = WS_CSIN + 2 * 4608 * 4;
constexpr u32 WS_X = WS_WOUT + 8 * 72 * 4608;
constexpr u32 XB = 128 * 18 * 1024, XSB = 2048 * 64, ZB = 144 * 64 * 1024;
constexpr u32 WS_XS = WS_X + 4 * XB;
constexpr u32 WS_O = WS_XS + 4 * XSB;
constexpr u32 WS_Z = WS_O + (1 + LSH) * XB;
constexpr u32 WS_H = WS_Z + (1 + LSH) * ZB;
constexpr u32 WS_KVP = WS_H + (1 + LSH) * ZB;
constexpr u32 WS_ZST = WS_KVP + 2 * 256 * 2816;
constexpr u32 WS_CNT = WS_ZST + 2 * 256 * 1024;
constexpr u32 WS_CNO = WS_CNT + 2 * 16 * 32 * 128;
constexpr u32 WS_END = WS_CNO + 2 * 16 * 16 * 128;
constexpr u32 WS_DBG = WS_END;
#define WSTOP 0
#define WS_BASE(n) (WSTOP ? (((n) - WS_END - 0x100000) & ~(size_t)0xfffff) : 0)

#define STAMP_DECL
#define STAMP(k, i) do { } while (0)
#define HST(i) do { } while (0)
#define HST_DECL
#define HST_OUT(k) do { } while (0)
#define STAMPV(k, i, v) do { } while (0)
#define STAMP_OUT(k) do { } while (0)

__device__ __forceinline__ u32 pk2(float lo, float hi) {
    f32x2 v = {lo, hi};
    bf2_t b = __builtin_convertvector(v, bf2_t);
    return __builtin_bit_cast(u32, b);
}
__device__ __forceinline__ float bflo(u32 w) { return U2F(w << 16); }
__device__ __forceinline__ float bfhi(u32 w) { return U2F(w & 0xffff0000u); }
__device__ __forceinline__ bf16x8 mk8(u32 a, u32 b, u32 c, u32 d) { u32x4 v = {a, b, c, d}; return __builtin_bit_cast(bf16x8, v); }
__device__ __forceinline__ float sfma(float a, float b, float c) { float r; asm("v_fma_f32 %0, %1, %2, %3" : "=v"(r) : "v"(a), "v"(b), "v"(c)); return r; }
__device__ __forceinline__ f32x4 sadd4(f32x4 a, f32x4 b) {
    float r0 = a[0], r1 = a[1], r2 = a[2], r3 = a[3];
    asm("v_add_f32 %0, %0, %4\n\tv_add_f32 %1, %1, %5\n\tv_add_f32 %2, %2, %6\n\tv_add_f32 %3, %3, %7" : "+v"(r0), "+v"(r1), "+v"(r2), "+v"(r3) : "v"(b[0]), "v"(b[1]), "v"(b[2]), "v"(b[3]));
    f32x4 r = {r0, r1, r2, r3};
    return r;
}
__device__ __forceinline__ f32x4 phi4(f32x4 t) {
    const f32x4 u = t * 1.44269504089f;
    f32x4 e, m;
#pragma unroll
    for (int r = 0; r < 4; ++r) { e[r] = __builtin_amdgcn_exp2f(__builtin_amdgcn_fmed3f(u[r], 0.f, -__builtin_inff())); m[r] = __builtin_amdgcn_fmed3f(t[r], 0.f, __builtin_inff()); }
    return (m + e) + 1e-6f;
}
__device__ __forceinline__ u32 ldsw(u32 a) { return *(volatile LAS u32*)(size_t)a; }
__device__ __forceinline__ void stsw(u32 a, u32 v) { *(volatile LAS u32*)(size_t)a = v; }
#define LDSR(addr, off) (*(LAS bf16x8*)((LAS char*)(size_t)(addr) + (off)))
__device__ __forceinline__ void lds_wait(u32 a, u32 target) {
    FENCE();
#pragma nounroll
    for (u32 n = 0; n < SPIN_CAP; ++n) { const u32 v = ldsw(a); if (!ANY(v < target)) break; }
    FENCE();
}
__device__ __forceinline__ void lds_wait_gt(u32 a, u32 low) {
    FENCE();
#pragma nounroll
    for (u32 n = 0; n < SPIN_CAP; ++n) { const u32 v = ldsw(a); if (!ANY(v <= low)) break; }
    FENCE();
}
__device__ __forceinline__ void lds_wait_sleep(u32 a, u32 target) {
    FENCE();
#pragma nounroll
    for (u32 n = 0; n < SPIN_CAP; ++n) { const u32 v = ldsw(a); if (!ANY(v < target)) break; __builtin_amdgcn_s_sleep(1); }
    FENCE();
}
#define DMA4(v0, v1, v2, v3, sbase, m0v)                                                                            \
    asm volatile("s_mov_b32 m0, %5\n\ts_nop 0\n\tglobal_load_lds_dwordx4 %0, %4\n\t"                                \
                 "global_load_lds_dwordx4 %1, %4 offset:1024\n\tglobal_load_lds_dwordx4 %2, %4 offset:2048\n\t"     \
                 "global_load_lds_dwordx4 %3, %4 offset:3072" ::"v"(v0), "v"(v1), "v"(v2), "v"(v3), "s"(sbase), "s"(m0v) : "memory")
#define DMA2(v0, v1, sbase, m0v)                                                                                    \
    asm volatile("s_mov_b32 m0, %3\n\ts_nop 0\n\tglobal_load_lds_dwordx4 %0, %2\n\t"                                \
                 "global_load_lds_dwordx4 %1, %2 offset:1024" ::"v"(v0), "v"(v1), "s"(sbase), "s"(m0v) : "memory")

template <int SLP>
__device__ __forceinline__ void touch_paced(const char* p, int n, u32 vo) {
    u32x4 sink = {0u, 0u, 0u, 0u};
#pragma nounroll
    for (int i = 0; i < n; ++i) {
        asm volatile("global_load_dwordx4 %0, %1, %2" : "+v"(sink) : "v"(vo), "s"(p + i * 1024));
        __builtin_amdgcn_s_sleep(SLP);
    }
    WAIT_V(0);
    PINV(sink);
}

__device__ __forceinline__ u32x4 tcol(const u32x2 (&ld)[8], int i) {
    const u32 sel = (i & 1) ? 0x07060302u : 0x05040100u;
    u32x4 o;
    o.x = __builtin_amdgcn_perm(ld[1][i >> 1], ld[0][i >> 1], sel);
    o.y = __builtin_amdgcn_perm(ld[3][i >> 1], ld[2][i >> 1], sel);
    o.z = __builtin_amdgcn_perm(ld[5][i >> 1], ld[4][i >> 1], sel);
    o.w = __builtin_amdgcn_perm(ld[7][i >> 1], ld[6][i >> 1], sel);
    return o;
}
__device__ __forceinline__ float sum8(u32x4 o) {
    return ((bflo(o.x) + bfhi(o.x)) + (bflo(o.y) + bfhi(o.y))) + ((bflo(o.z) + bfhi(o.z)) + (bflo(o.w) + bfhi(o.w)));
}

constexpr int K0_QKV = 72, K0_X = 64, K0_WIN = 144, K0_WO = 90, K0_GRID = K0_QKV + K0_X + K0_WIN + K0_WO;
__device__ __forceinline__ void k0_body(const u16* __restrict__ x, const u16* __restrict__ wqkv, const u16* __restrict__ wo,
                                        const u16* __restrict__ win, const u16* __restrict__ wout, const u16* __restrict__ wsp, char* __restrict__ ws) {
    __shared__ __attribute__((aligned(16))) char img[36864 + 576 * 8];
    float (*red)[64] = (float (*)[64])(img + 36864);
    const int tid = threadIdx.x, w = __builtin_amdgcn_readfirstlane(tid >> 6), lane = tid & 63, c = lane & 15, g = lane >> 4;
    int b = blockIdx.x;
    if (b < K0_QKV) {
        const int t = (b >> 3) % 3, kt = (b & 7) + 8 * (b / 24);
        if (kt >= 18) return;
        u32x2 ld[8];
#pragma unroll
        for (int j = 0; j < 8; ++j) ld[j] = NTL((const u32x2*)(wqkv + (size_t)(32 * kt + 8 * g + j) * 1728 + 576 * t + 64 * w + 4 * c));
        float* cst = (float*)(img + 36864);
#pragma unroll
        for (int i = 0; i < 4; ++i) {
            const int col = 64 * w + 4 * c + i, h = col / 18, d = col - 18 * h;
            const u32x4 o = tcol(ld, i);
            *(u32x4*)(img + (d < 16 ? h * 1024 + (d + 16 * g) * 16 : 32768 + (h >> 1) * 256 + g * 64 + (2 * (h & 1) + d - 16) * 16)) = o;
            float cs = sum8(o);
            cs += __shfl_xor(cs, 16); cs += __shfl_xor(cs, 32);
            if (g == 0) cst[(h >> 1) * 36 + (d < 16 ? (h & 1) * 16 + d : 32 + 2 * (h & 1) + d - 16)] = cs;
        }
        __syncthreads();
        const int rc = t == 1 ? 0 : (t == 2 ? 4 : 8);
        if (w < 8) {
#pragma unroll
            for (int k = 0; k < 4; ++k) {
                const int h = 4 * w + k;
                const u32 tile = t == 0 ? 92160 + (kt * 2 + (h & 1)) * 1024 : (kt * 5 + 2 * (t - 1) + (h & 1)) * 1024;
                *(u32x4*)(ws + WS_WQ + (h >> 1) * 129024 + tile + lane * 16) = *(const u32x4*)(img + h * 1024 + lane * 16);
            }
        } else {
#pragma unroll
            for (int k = 0; k < 4; ++k) {
                char* dst = ws + WS_WQ + (4 * k + g) * 129024 + (kt * 5 + 4) * 1024 + (c >> 2) * 256 + (rc + (c & 3)) * 16;
                *(u32x4*)dst = *(const u32x4*)(img + 32768 + k * 1024 + lane * 16);
                if (t == 0) *(u32x4*)(dst + 64) = (u32x4){0u, 0u, 0u, 0u};
            }
        }
        {
            const int p = tid / 36, e = tid - 36 * p;
            float* cq = (float*)(ws + WS_CSQ) + (kt * 16 + p) * 128;
            cq[e < 32 ? (t == 0 ? 80 : 32 * (t - 1)) + e : 64 + rc + e - 32] = cst[tid];
            if (t == 0 && e >= 32) cq[76 + e - 32] = 0.f;
        }
        return;
    }
    b -= K0_QKV;
    if (b < K0_X) {
        const int rl = tid / 18, t18 = tid - 18 * rl;
        u32x4 v[4];
#pragma unroll
        for (int i = 0; i < 4; ++i) v[i] = NTL((const u32x4*)(x + (size_t)(32 * b + rl) * 576 + (18 * i + t18) * 8));
        float s1 = 0.f, s2 = 0.f;
#pragma unroll
        for (int i = 0; i < 4; ++i) {
            const int ch = 18 * i + t18;
            *(u32x4*)(img + ((rl >> 4) * 18 + (ch >> 2)) * 1024 + ((rl & 15) + 16 * (ch & 3)) * 16) = v[i];
#pragma unroll
            for (int k = 0; k < 4; ++k) { const float a = bflo(v[i][k]), bb = bfhi(v[i][k]); s1 += a + bb; s2 += a * a + bb * bb; }
        }
        f32x2 o = {s1, s2};
        ((f32x2*)(img + 36864))[tid] = o;
        __syncthreads();
#pragma unroll
        for (int t = 0; t < 4; ++t) *(u32x4*)(ws + WS_X + b * 36864 + (w * 4 + t) * 1024 + lane * 16) = *(const u32x4*)(img + (w * 4 + t) * 1024 + lane * 16);
        if (tid < 256) {
            const int r = tid & 31, q = tid >> 5;
            const f32x2* pp = (const f32x2*)(img + 36864) + r * 18;
            o = pp[2 * q] + pp[2 * q + 1];
            if (q < 2) o += pp[16 + q];
            *(f32x2*)(ws + WS_XS + (q * 2048 + 32 * b + r) * 8) = o;
        }
        return;
    }
    b -= K0_X;
    if (b < K0_WIN) {
        const int hf = b & 1, cb = b >> 1, kt = 9 * hf + w;
        u32x2 ld[8];
#pragma unroll
        for (int j = 0; j < 8; ++j) ld[j] = NTL((const u32x2*)(win + (size_t)(32 * kt + 8 * g + j) * 4608 + 64 * cb + 4 * c));
#pragma unroll
        for (int i = 0; i < 4; ++i) {
            const u32x4 o = tcol(ld, i);
            *(u32x4*)(img + w * 4096 + (c >> 2) * 1024 + (4 * (c & 3) + i + 16 * g) * 16) = o;
            float cs = sum8(o);
            cs += __shfl_xor(cs, 16); cs += __shfl_xor(cs, 32);
            if (g == 0) red[w][4 * c + i] = cs;
        }
        FENCE();
#pragma unroll
        for (int t = 0; t < 4; ++t) *(u32x4*)(ws + WS_WIN + ((4 * cb + t) * 18 + kt) * 1024 + lane * 16) = *(const u32x4*)(img + w * 4096 + t * 1024 + lane * 16);
        __syncthreads();
        if (tid < 64) {
            float s = 0.f;
#pragma unroll
            for (int k = 0; k < 9; ++k) s += red[k][tid];
            ((float*)(ws + WS_CSIN))[hf * 4608 + 64 * cb + tid] = s;
        }
        return;
    }
    b -= K0_WIN;
    {
        const bool iso = b < 18;
        const int sl = iso ? b : b - 18, G = sl / 9, t = sl - 9 * G;
        const u16* src = iso ? wo : wout;
        const int col = 64 * w + 4 * c, nt = col / 72, cl = col - 72 * nt;
        const int r0 = iso ? (sl < 16 ? 36 * sl : 288 * (sl - 16) + 16) : (t < 8 ? 288 * G + 144 * (t >> 2) + 32 * (t & 3) : 288 * G + 128);
        const int gs = (iso && sl >= 16) ? 72 : 4, s4 = iso ? (sl < 16 ? 18 : 36) : (t < 8 ? 16 : 144), s2 = (iso && sl >= 16) ? 18 : 2;
        u32x2 ld[8];
#pragma unroll
        for (int j = 0; j < 8; ++j) ld[j] = NTL((const u32x2*)(src + (size_t)(r0 + gs * g + s4 * (j >> 2) + s2 * ((j >> 1) & 1) + (j & 1)) * 576 + col));
        const int n = cl < 64 ? 2 * (cl >> 5) + ((cl >> 2) & 1) : 4, ii = cl < 64 ? 4 * ((cl >> 3) & 3) : cl - 64;
#pragma unroll
        for (int i = 0; i < 4; ++i) {
            const u32 off = n < 4 ? n * 1024 + (ii + i + 16 * g) * 16 : 4096 + g * 128 + (ii + i) * 16;
            *(u32x4*)(img + nt * 4608 + off) = tcol(ld, i);
        }
        __syncthreads();
#pragma unroll
        for (int k = 0; k < 4; ++k) {
            const u32 i = (w * 4 + k) * 1024 + lane * 16, n2 = i / 4608, off = i - 4608 * n2;
            *(u32x4*)(ws + (iso ? WS_WO + (n2 * 18 + sl) * 4608 : WS_WOUT + (n2 * 72 + sl) * 4608) + off) = *(const u32x4*)(img + i);
        }
        return;
    }
}

__global__ __launch_bounds__(576) void k0_prep(const u16* __restrict__ x, const u16* __restrict__ wqkv, const u16* __restrict__ wo,
                                               const u16* __restrict__ win, const u16* __restrict__ wout, const u16* __restrict__ wsp, char* __restrict__ ws, const u16* out) {
    STAMP_DECL;
    STAMP(8, 0);
    const u32 gid = blockIdx.x * 576 + threadIdx.x;
    u32 warm = 0;
    if (WSPTOUCH && gid % 3 == 0 && gid < 3 * 65536) warm = *(const u32*)(wsp + (size_t)(gid / 3) * 64);
    if (blockIdx.x >= 50 && blockIdx.x < 66) {
        u64 pc; int dist;
        asm volatile("s_getpc_b64 %0\n\ts_mov_b32 %1, k9_end@rel32@lo+4" : "=&s"(pc), "=&s"(dist));
        if ((int)threadIdx.x * 128 < dist) warm |= *(const GAS u32*)(pc + threadIdx.x * 128);
    }
    if (blockIdx.x >= 72 && blockIdx.x < 104) {
        const u32 o = threadIdx.x * 81920u;
        if (o < WS_END) warm |= *(const u32*)(ws + o + 64);
        if (threadIdx.x < 128) warm |= *(const u32*)((const char*)wsp + threadIdx.x * 65536);
        if (threadIdx.x >= 128 && threadIdx.x < 164) warm |= *(const u32*)((const char*)out + (threadIdx.x - 128) * 65536);
    }
    if (gid < (WS_END - WS_KVP) / 16) {
        const auto rs = __builtin_amdgcn_make_buffer_rsrc(ws, 0, WS_END, 0x00020000);
        const u32 v = gid < (WS_CNT - WS_KVP) / 16 ? SENT : 0u;
        const u32x4 o = {v, v, v, v};
        BST128(o, rs, gid * 16, WS_KVP, 16);
    }
    k0_body(x, wqkv, wo, win, wout, wsp, ws);
    asm volatile("" :: "v"(warm));
    STAMP(8, 1);
    STAMP_OUT(11);
}

__device__ __forceinline__ void x_stats(const char* ws, int ver, int row, float& mean, float& var) {
    const f32x2* p = (const f32x2*)(ws + WS_XS + ver * XSB) + row;
    f32x2 v[8];
#pragma unroll
    for (int k = 0; k < 8; ++k) v[k] = p[k * 2048];
    const f32x2 sm = ((v[0] + v[1]) + (v[2] + v[3])) + ((v[4] + v[5]) + (v[6] + v[7]));
    const float s1 = sm[0], s2 = sm[1];
    mean = s1 * (1.f / 576.f);
    var = fmaxf(s2 * (1.f / 576.f) - mean * mean, 0.f);
}

__device__ __forceinline__ void proj_store(const f32x4 (&y)[5], char* ws, u16* out, int ver, int mt, int nt, int wid, int lane) {
    const u32 vo = lane * 16;
    const int c = lane & 15, g = lane >> 4;
    u32x4 o[2]; u32x2 o2;
#pragma unroll
    for (int i = 0; i < 2; ++i) o[i] = (u32x4){pk2(y[2 * i][0], y[2 * i][1]), pk2(y[2 * i][2], y[2 * i][3]), pk2(y[2 * i + 1][0], y[2 * i + 1][1]), pk2(y[2 * i + 1][2], y[2 * i + 1][3])};
    o2 = (u32x2){pk2(y[4][0], y[4][1]), pk2(y[4][2], y[4][3])};
    if (ver == 4) {
        u16* op = out + (size_t)(64 * mt + 16 * wid + c) * 576 + 72 * nt;
        *(u32x4*)(op + 8 * g) = o[0];
        *(u32x4*)(op + 32 + 8 * g) = o[1];
        if (g < 2) *(u32x2*)(op + 64 + 4 * g) = o2;
    } else {
        char* xp = ws + WS_X + ver * XB + (4 * mt + wid) * 18432 + 2304 * nt;
        *(u32x4*)(xp + vo) = o[0];
        *(u32x4*)(xp + vo + 1024) = o[1];
        if (g < 2) *(u32x2*)(xp + 2048 + c * 16 + g * 8) = o2; else o2 = (u32x2){0u, 0u};
        float s1 = 0.f, s2 = 0.f;
#pragma unroll
        for (int k = 0; k < 10; ++k) { const u32 wv = k < 8 ? o[k >> 2][k & 3] : o2[k & 1]; const float u = bflo(wv), v = bfhi(wv); s1 += u + v; s2 += u * u + v * v; }
        auto r1 = __builtin_amdgcn_permlane32_swap(F2U(s1), F2U(s2), false, false);
        const float t = U2F(r1[0]) + U2F(r1[1]);
        auto r2 = __builtin_amdgcn_permlane16_swap(F2U(t), F2U(t), false, false);
        const float tt = U2F(r2[0]) + U2F(r2[1]);
        if ((g & 1) == 0) *(float*)(ws + WS_XS + ver * XSB + (nt * 2048 + 64 * mt + 16 * wid + c) * 8 + (g >> 1) * 4) = tt;
    }
}

__device__ __forceinline__ void proj_tail(f32x4 (&ac)[4][5], const u32x4 (&res)[2], u32x2 res2, u32 lds, u32 wstride, u32 flags, char* ws, u16* out,
                                          int ver, int mt, int nt, int wid, int lane) {
    const u32 vo = lane * 16;
#pragma unroll
    for (int m = 1; m < 4; ++m)
#pragma unroll
        for (int n = 0; n < 5; ++n) *(LAS f32x4*)(size_t)(lds + wid * wstride + vo + ((m - 1) * 5 + n) * 1024) = ac[m][n];
    FENCE();
    stsw(flags + wid * 4, 1u);
#pragma unroll
    for (int i = 0; i < 4; ++i) {
        const u32 w0 = res[i >> 1][2 * (i & 1)], w1 = res[i >> 1][2 * (i & 1) + 1];
        ac[0][i] += (f32x4){bflo(w0), bfhi(w0), bflo(w1), bfhi(w1)};
        PINV(ac[0][i]);
    }
    ac[0][4] += (f32x4){bflo(res2[0]), bfhi(res2[0]), bflo(res2[1]), bfhi(res2[1])};
    PINV(ac[0][4]);
    lds_wait(flags + (lane & 3) * 4, 1u);
#pragma unroll
    for (int n = 0; n < 5; ++n)
        ac[0][n] = (ac[0][n] + *(LAS f32x4*)(size_t)(lds + (wid ^ 1) * wstride + vo + n * 1024)) +
                   (*(LAS f32x4*)(size_t)(lds + (wid ^ 2) * wstride + vo + (5 + n) * 1024) + *(LAS f32x4*)(size_t)(lds + (wid ^ 3) * wstride + vo + (10 + n) * 1024));
    proj_store(ac[0], ws, out, ver, mt, nt, wid, lane);
}

constexpr u32 K1_PW = 129024, K1_OP = K1_PW + 4 * 176 * 16, K1_ST = K1_OP + 1536, K1_CS = K1_ST + 1024, K1_FL = K1_CS + 512, K1_LDS = K1_FL + 128;

__global__ __launch_bounds__(512, 2) void k1_attn(char* __restrict__ ws, int layer) {
    extern __shared__ __attribute__((aligned(16))) char shm_[];
    const u32 lds = (u32)(size_t)(LAS char*)shm_;
    const int tid = threadIdx.x, wid = __builtin_amdgcn_readfirstlane(tid >> 6), lane = tid & 63, c = lane & 15, g = lane >> 4;
    const int xcd = blockIdx.x & 7, bi = blockIdx.x >> 3;
    const int pm = MAP1 ? (xcd & 3) * 4 + (bi & 3) : (xcd & 1) * 8 + (bi & 7), p = MAP1 ? (xcd >> 2) * 8 + (bi >> 2) : (xcd >> 1) * 4 + (bi >> 3);
    const u32 FL = lds + K1_FL;
    STAMP_DECL;
    if (wid == 4) {
        const char* src = ws + WS_WQ + p * 129024;
        const u32 vo = lane * 16;
#define K1_U10(u) do { DMA4(vo, vo, vo, vo, src + (u) * 10240, lds + (u) * 10240); DMA4(vo, vo, vo, vo, src + (u) * 10240 + 4096, lds + (u) * 10240 + 4096); \
                       DMA2(vo, vo, src + (u) * 10240 + 8192, lds + (u) * 10240 + 8192); } while (0)
#define K1_U12(u) do { DMA4(vo, vo, vo, vo, src + 92160 + (u) * 12288, lds + 92160 + (u) * 12288); DMA4(vo, vo, vo, vo, src + 92160 + (u) * 12288 + 4096, lds + 92160 + (u) * 12288 + 4096); \
                       DMA4(vo, vo, vo, vo, src + 92160 + (u) * 12288 + 8192, lds + 92160 + (u) * 12288 + 8192); } while (0)
        K1_U10(0);
        BAR();
        K1_U10(1); K1_U10(2);
#pragma unroll
        for (int u = 3; u < 9; ++u) { WAIT_V(20); stsw(FL, u - 2); FENCE(); K1_U10(u); }
        WAIT_V(20); stsw(FL, 7); FENCE(); K1_U12(0);
        WAIT_V(22); stsw(FL, 8); FENCE(); K1_U12(1);
        WAIT_V(24); stsw(FL, 9); FENCE(); K1_U12(2);
        WAIT_V(24); stsw(FL, 10); FENCE();
        WAIT_V(12); stsw(FL, 11); FENCE();
        WAIT_V(0); stsw(FL, 12);
        lds_wait_sleep(FL + 16 + (lane & 3) * 4, 2u);
        src = ws + WS_WO + (p & 7) * 82944;
#pragma unroll
        for (int u = 0; u < 8; ++u) { K1_U10(u); if (u >= 4) WAIT_V(40); }
        asm volatile("s_mov_b32 m0, %2\n\ts_nop 0\n\tglobal_load_lds_dwordx4 %0, %1" ::"v"(vo), "s"(src + 81920), "s"(lds + 81920) : "memory");
        WAIT_V(0); stsw(FL, 13);
        return;
    }
    if (wid > 4) {
        const int ht = tid - 320;
        if (ht >= 128 && ht < 160) stsw(FL + (ht - 128) * 4, 0u);
        WAIT_L0();
        BAR();
        if (wid != 7) {
            float mean, var;
            x_stats(ws, 2 * layer, 128 * pm + ht, mean, var);
            const float r = __builtin_amdgcn_rsqf(var + 1e-5f);
            *(LAS float*)(size_t)(lds + K1_ST + ht * 4) = r;
            *(LAS float*)(size_t)(lds + K1_ST + 512 + ht * 4) = -mean * r;
        } else {
            const f32x2* cp = (const f32x2*)(ws + WS_CSQ) + p * 64 + min(lane, 55);
            f32x2 q[18];
#pragma unroll
            for (int k = 0; k < 18; ++k) q[k] = cp[k * 1024];
            float q0 = q[0][0], q1 = q[0][1];
#pragma unroll
            for (int k = 1; k < 18; ++k) { q0 = sfma(q[k][0], 1.f, q0); q1 = sfma(q[k][1], 1.f, q1); }
            *(LAS f32x2*)(size_t)(lds + K1_CS + lane * 8) = (f32x2){q0, q1};
        }
        FENCE();
        stsw(FL + 4 + (wid - 5) * 4, 1u);
        __builtin_amdgcn_s_setprio(3);
        lds_wait_sleep(FL + 16 + (lane & 3) * 4, 1u);
        STAMP(layer * 4 + 0, 7);
        const int it = ht < 173 ? ht : 172;
        f32x4 v = *(LAS f32x4*)(size_t)(lds + K1_PW + it * 16);
#pragma unroll
        for (int k = 1; k < 4; ++k) v = sadd4(v, *(LAS f32x4*)(size_t)(lds + K1_PW + k * 2816 + it * 16));
        const auto rs = __builtin_amdgcn_make_buffer_rsrc(ws, 0, WS_END, 0x00020000);
        const u32 gbase = WS_KVP + (layer * 16 + p) * 16 * 2816;
        {
            u32x4 o = __builtin_bit_cast(u32x4, v);
#pragma unroll
            for (int k = 0; k < 4; ++k) o[k] = o[k] == SENT ? 0x7fc00000u : o[k];
            if (ht < 173) BST128(o, rs, it * 16, gbase + pm * 2816, 16);
        }
        u32x4 t[16];
        STAMP(layer * 4 + 0, 8); STAMP(layer * 4 + 0, 9);
        __builtin_amdgcn_s_sleep(XDELAY1);
#pragma unroll
        for (int k = 0; k < 16; ++k) t[k] = __builtin_amdgcn_raw_buffer_load_b128(rs, it * 16, gbase + k * 2816, 16);
        u32 mx = 0, spins = 0;
#pragma unroll
        for (int k = 0; k < 16; ++k) mx = max(max(mx, max(t[k][0], t[k][1])), max(t[k][2], t[k][3]));
        if (__builtin_expect(ANY(mx == SENT), 0)) {
#pragma nounroll
            for (spins = 1; spins < SPIN_CAP; ++spins) {
                FENCE();
                u32 miss = 0;
#pragma unroll
                for (int k = 0; k < 16; ++k) miss |= ANY(max(max(t[k][0], t[k][1]), max(t[k][2], t[k][3])) == SENT) ? 1u << k : 0u;
                if (!miss) break;
#pragma unroll
                for (int k = 0; k < 16; ++k) if (miss >> k & 1) t[k] = __builtin_amdgcn_raw_buffer_load_b128(rs, it * 16, gbase + k * 2816, 16);
            }
        }
        STAMP(layer * 4 + 0, 10); STAMPV(layer * 4 + 0, 15, spins);
        v = __builtin_bit_cast(f32x4, t[0]);
#pragma unroll
        for (int k = 1; k < 16; ++k) v = sadd4(v, __builtin_bit_cast(f32x4, t[k]));
        u32x2 lh = {pk2(v[0], v[1]), pk2(v[2], v[3])};
        if (ht >= 173) lh = (u32x2){0u, 0u};
        *(LAS u32x2*)(size_t)(lds + K1_OP + ht * 8) = lh;
        FENCE();
        stsw(FL + 32 + (wid - 5) * 4, 1u);
        if (wid != 5) return;
        STAMP_OUT(layer * 4 + 0);
        lds_wait_sleep(FL + 48, 4u);
        __builtin_amdgcn_s_sleep(ODELAY);
        GAS u32* om = (GAS u32*)(ws + WS_CNO + ((layer * 16 + pm) * 16 + p) * 128);
#pragma nounroll
        for (u32 n = 0; n < SPIN_CAP; ++n) { if (__builtin_amdgcn_readfirstlane(__hip_atomic_load(om, RLX_AGENT)) == 0xffffu) break; }
        FENCE();
        stsw(FL + 52, 1u);
        return;
    }
    STAMP(layer * 4 + 0, 0);

    const auto rs = __builtin_amdgcn_make_buffer_rsrc(ws, 0, WS_END, 0x00020000);
    const u32 xb = WS_X + 2 * layer * XB + (8 * pm + 2 * wid) * 18432, vo = lane * 16;
    bf16x8 xa[2][18];
#define K1_LX(m, kt) xa[m][kt] = __builtin_bit_cast(bf16x8, __builtin_amdgcn_raw_buffer_load_b128(rs, vo, xb + ((m) * 18 + (kt)) * 1024, 0))
#pragma unroll
    for (int kt = 0; kt < 6; ++kt) { K1_LX(0, kt); SB(); K1_LX(1, kt); SB(); }
    f32x4 acc[2][5];
#pragma unroll
    for (int f = 0; f < 5; ++f) { acc[0][f] = acc[1][f] = (f32x4){0.f, 0.f, 0.f, 0.f}; PINV(acc[0][f]); PINV(acc[1][f]); }
    u32 wb0 = lds + lane * 16, wb1 = wb0 + 61440, wb2 = wb0 + 92160;
    PINV(wb0); PINV(wb1); PINV(wb2);
#define K1_W1(kt, f) ((kt) < 12 ? LDSR(wb0, ((kt) * 5 + (f)) * 1024) : LDSR(wb1, (((kt) - 12) * 5 + (f)) * 1024))
    BAR();
    lds_wait(FL, 1u);
    STAMP(layer * 4 + 0, 1);
    bf16x8 wf[2][5];
#pragma unroll
    for (int f = 0; f < 5; ++f) { wf[0][f] = K1_W1(0, f); SB(); }
#pragma unroll
    for (int f = 0; f < 5; ++f) { wf[1][f] = K1_W1(1, f); SB(); }
    u32 fl = 0;
#pragma unroll
    for (int kt = 0; kt < 18; ++kt) {
        if ((kt & 1) == 0 && kt + 2 < 18) { if (__builtin_expect(ANY(fl < (u32)(kt / 2 + 2)), 0)) lds_wait(FL, kt / 2 + 2); FENCE(); }
#pragma unroll
        for (int f = 0; f < 5; ++f) {
            MFMA(acc[0][f], xa[0][kt], wf[kt & 1][f]);
            if (f == 1 && kt + 6 < 18) K1_LX(0, kt + 6);
            if (f == 3 && kt + 6 < 18) K1_LX(1, kt + 6);
            if (f == 0 && (kt & 1) && kt < 16) { FENCE(); fl = ldsw(FL); FENCE(); }
            SB();
            MFMA(acc[1][f], xa[1][kt], wf[kt & 1][f]);
            if (kt + 2 < 18) wf[kt & 1][f] = K1_W1(kt + 2, f);
            SB();
        }
    }
    asm volatile("s_nop 15" ::: "memory");
    STAMP(layer * 4 + 0, 2);
    lds_wait(FL + 4 + (lane % 3) * 4, 1u);
    {
        u32 op[5][4], rk[4];
        float cs1[5];
#pragma unroll
        for (int f = 0; f < 5; ++f) cs1[f] = *(LAS float*)(size_t)(lds + K1_CS + f * 64 + c * 4);
#pragma unroll
        for (int m = 0; m < 2; ++m) {
            const f32x4 rr = *(LAS f32x4*)(size_t)(lds + K1_ST + (32 * wid + 16 * m + 4 * g) * 4);
            const f32x4 mm = *(LAS f32x4*)(size_t)(lds + K1_ST + 512 + (32 * wid + 16 * m + 4 * g) * 4);
#pragma unroll
            for (int f = 0; f < 5; ++f) {
                f32x4 v = rr * acc[m][f] + mm * cs1[f];
                if (f < 2) v = phi4(v);
                if (f == 4) { const f32x4 u = phi4(v); rk[2 * m] = pk2(u[0], u[1]); rk[2 * m + 1] = pk2(u[2], u[3]); }
                op[f][2 * m] = pk2(v[0], v[1]); op[f][2 * m + 1] = pk2(v[2], v[3]);
                if (f == 4 && c >= 12) { op[f][2 * m] = 0x3f803f80u; op[f][2 * m + 1] = 0x3f803f80u; }
            }
        }
        const bf16x8 KAa = mk8(op[0][0], op[0][1], op[0][2], op[0][3]), KAb = mk8(op[1][0], op[1][1], op[1][2], op[1][3]);
        const bf16x8 VBa = mk8(op[2][0], op[2][1], op[2][2], op[2][3]), VBb = mk8(op[3][0], op[3][1], op[3][2], op[3][3]);
        const bf16x8 RV = mk8(op[4][0], op[4][1], op[4][2], op[4][3]), RK = mk8(rk[0], rk[1], rk[2], rk[3]);
        const f32x4 z4 = {0.f, 0.f, 0.f, 0.f};
        const f32x4 T1a = MFMAB(KAa, VBa, z4), T1b = MFMAB(KAb, VBb, z4), T2a = MFMAB(KAa, RV, z4), T2b = MFMAB(KAb, RV, z4);
        const f32x4 T3a = MFMAB(RK, VBa, z4), T3b = MFMAB(RK, VBb, z4), T4 = MFMAB(RK, RV, z4);
        const u32 pw = lds + K1_PW + wid * 2816;
        *(LAS f32x4*)(size_t)(pw + lane * 16) = T1a;
        *(LAS f32x4*)(size_t)(pw + 1024 + lane * 16) = T1b;
        const f32x4 u3 = {T3a[0], T3a[1], T3b[2], T3b[3]};
        *(LAS f32x4*)(size_t)(pw + (g == 0 ? 128 + c : 175) * 16) = u3;
        *(LAS f32x4*)(size_t)(pw + (c == 12 ? 152 + g : ((c & 14) == 4 ? 144 + (c - 4) * 4 + g : 175)) * 16) = T2a;
        *(LAS f32x4*)(size_t)(pw + (c == 12 ? 164 + g : ((c & 14) == 6 ? 156 + (c - 6) * 4 + g : 175)) * 16) = T2b;
        *(LAS f32x4*)(size_t)(pw + (lane == 12 ? 172 : ((lane & 60) == 4 ? 164 + lane : 175)) * 16) = T4;
        FENCE();
        stsw(FL + 16 + wid * 4, 1u);
    }
    STAMP(layer * 4 + 0, 3);
    f32x4 qa[2][3];
#pragma unroll
    for (int f = 0; f < 3; ++f) { qa[0][f] = qa[1][f] = (f32x4){0.f, 0.f, 0.f, 0.f}; PINV(qa[0][f]); PINV(qa[1][f]); }
#define K1_W2(kt, f) ((f) < 2 ? LDSR(wb2, ((kt) * 2 + (f)) * 1024) : K1_W1(kt, 4))
    lds_wait(FL, 10u);
    bf16x8 wq[3][3];
#pragma unroll
    for (int k = 0; k < 3; ++k)
#pragma unroll
        for (int f = 0; f < 3; ++f) { wq[k][f] = K1_W2(k, f); SB(); }
#pragma unroll
    for (int kt = 0; kt < 18; ++kt) {
        if (kt == 3) lds_wait(FL, 11u);
        if (kt == 9) lds_wait(FL, 12u);
#pragma unroll
        for (int f = 0; f < 3; ++f) {
            MFMA(qa[0][f], wq[kt % 3][f], xa[0][kt]);
            SB();
            MFMA(qa[1][f], wq[kt % 3][f], xa[1][kt]);
            if (kt + 3 < 18) wq[kt % 3][f] = K1_W2(kt + 3, f);
            SB();
        }
    }
    asm volatile("s_nop 15" ::: "memory");
    STAMP(layer * 4 + 0, 4);
    stsw(FL + 16 + wid * 4, 2u);
    f32x4 cs2[3];
#pragma unroll
    for (int f = 0; f < 3; ++f) cs2[f] = *(LAS f32x4*)(size_t)(lds + K1_CS + (f < 2 ? 5 + f : 4) * 64 + g * 16);
    bf16x8 PQ[2][2];
#pragma unroll
    for (int m = 0; m < 2; ++m) {
        const float rr = *(LAS float*)(size_t)(lds + K1_ST + (32 * wid + 16 * m + c) * 4), mm = *(LAS float*)(size_t)(lds + K1_ST + 512 + (32 * wid + 16 * m + c) * 4);
        u32 w2[3][2];
#pragma unroll
        for (int f = 0; f < 3; ++f) {
            const f32x4 v = phi4(qa[m][f] * rr + cs2[f] * mm);
            w2[f][0] = pk2(v[0], v[1]); w2[f][1] = pk2(v[2], v[3]);
        }
        if (g != 2) { w2[2][0] = 0u; w2[2][1] = 0u; }
        PQ[m][0] = mk8(w2[0][0], w2[0][1], w2[2][0], w2[2][1]);
        PQ[m][1] = mk8(w2[1][0], w2[1][1], w2[2][0], w2[2][1]);
        PINV(PQ[m][0]); PINV(PQ[m][1]);
    }
    u32 ga[6];
    ga[0] = lane; ga[1] = g == 2 ? 128 + c : 175;
    ga[2] = c < 2 ? 144 + 4 * c + g : ((c & 3) == 2 ? 152 + g : 175);
    ga[3] = (c & 14) == 4 ? 156 + 4 * (c - 4) + g : ((c & 3) == 3 ? 164 + g : 175);
    ga[4] = g != 2 ? 175 : (c < 2 ? 168 + c : ((c & 3) == 2 ? 172 : 175));
    ga[5] = g != 2 ? 175 : ((c & 14) == 4 ? 166 + c : ((c & 3) == 3 ? 172 : 175));
#pragma unroll
    for (int i = 0; i < 6; ++i) { ga[i] = lds + K1_OP + ga[i] * 8; PINV(ga[i]); }
    const u32 ob = WS_O + LSH * layer * XB + (8 * pm + 2 * wid) * 18432;
    u32 orem = (16 + (p >> 3)) * 1024 + (c + 16 * ((p & 7) >> 1)) * 16 + (p & 1) * 8 + g * 4;
    PINV(orem);
    lds_wait(FL + 32 + (lane % 3) * 4, 1u);
    STAMP(layer * 4 + 0, 5);
    {
        const u32x2 l0 = *(LAS u32x2*)(size_t)ga[0], l1 = *(LAS u32x2*)(size_t)(ga[0] + 512), h01 = *(LAS u32x2*)(size_t)ga[1];
        const u32x2 l2 = *(LAS u32x2*)(size_t)ga[2], l3 = *(LAS u32x2*)(size_t)ga[3];
        const u32 h2 = *(LAS u32*)(size_t)ga[4], h3 = *(LAS u32*)(size_t)(ga[5] + 4);
        const bf16x8 A1a = mk8(l0[0], l0[1], h01[0], 0u), A1b = mk8(l1[0], l1[1], 0u, h01[1]), A2a = mk8(l2[0], l2[1], h2, 0u), A2b = mk8(l3[0], l3[1], 0u, h3);
        const f32x4 z4 = {0.f, 0.f, 0.f, 0.f};
        f32x4 O1a[2], O1b[2], O2[2];
#pragma unroll
        for (int m = 0; m < 2; ++m) O2[m] = MFMAB(A2a, PQ[m][0], z4);
#pragma unroll
        for (int m = 0; m < 2; ++m) O2[m] = MFMAB(A2b, PQ[m][1], O2[m]);
#pragma unroll
        for (int m = 0; m < 2; ++m) { O1a[m] = MFMAB(A1a, PQ[m][0], z4); O1b[m] = MFMAB(A1b, PQ[m][1], z4); }
#pragma unroll
        for (int m = 0; m < 2; ++m) {
            const float ia = __builtin_amdgcn_rcpf(O2[m][2] + 1e-6f), ib = __builtin_amdgcn_rcpf(O2[m][3] + 1e-6f);
            const f32x4 ya = O1a[m] * ia, yb = O1b[m] * ib;
            const u32x4 o = {pk2(ya[0], ya[1]), pk2(ya[2], ya[3]), pk2(yb[0], yb[1]), pk2(yb[2], yb[3])};
            BST128(o, rs, vo, ob + m * 18432 + p * 1024, 16);
            const float ir = g == 0 ? ia : ib;
            if (g < 2) __builtin_amdgcn_raw_buffer_store_b32(pk2(O2[m][0] * ir, O2[m][1] * ir), rs, orem, ob + m * 18432, 16);
        }
    }
    STAMP(layer * 4 + 0, 6);
    STAMP_OUT(layer * 4 + 0);
    WAIT_V(0);
    {
        u32 old = 0;
        if (lane == 0) old = __hip_atomic_fetch_add((LAS u32*)(size_t)(FL + 48), 1u, __ATOMIC_RELAXED, __HIP_MEMORY_SCOPE_WORKGROUP);
        if (__builtin_amdgcn_readfirstlane(old) == 3 && lane < 16)
            __hip_atomic_fetch_add((GAS u32*)(ws + WS_CNO + ((layer * 16 + pm) * 16 + lane) * 128), 1u << p, RLX_AGENT);
    }
    STAMP(layer * 4 + 1, 0);
    const int mt = 2 * pm + (p >> 3), nt = p & 7;
    const u32 vo4 = 4096 + g * 128 + (lane & 7) * 16;
#define K2_LD(v, sof, aux) __builtin_bit_cast(bf16x8, __builtin_amdgcn_raw_buffer_load_b128(rs, v, sof, aux))
    bf16x8 fw[6][5], fa[18];
    f32x4 ac[5];
    {
        const u32 xo = WS_X + 2 * layer * XB + (4 * mt + wid) * 18432 + 2304 * nt;
        const u32x4 r0 = __builtin_amdgcn_raw_buffer_load_b128(rs, vo, xo, 0), r1 = __builtin_amdgcn_raw_buffer_load_b128(rs, vo + 1024, xo, 0);
        const u32x2 r2 = __builtin_amdgcn_raw_buffer_load_b64(rs, 2048 + c * 16 + (g & 1) * 8, xo, 0);
        ac[0] = (f32x4){bflo(r0[0]), bfhi(r0[0]), bflo(r0[1]), bfhi(r0[1])}; ac[1] = (f32x4){bflo(r0[2]), bfhi(r0[2]), bflo(r0[3]), bfhi(r0[3])};
        ac[2] = (f32x4){bflo(r1[0]), bfhi(r1[0]), bflo(r1[1]), bfhi(r1[1])}; ac[3] = (f32x4){bflo(r1[2]), bfhi(r1[2]), bflo(r1[3]), bfhi(r1[3])};
        ac[4] = (f32x4){bflo(r2[0]), bfhi(r2[0]), bflo(r2[1]), bfhi(r2[1])};
    }
    u32 wl[2] = {lds + vo, lds + vo + 41472}, wl4[2] = {lds + vo4, lds + vo4 + 41472};
    PINV(wl[0]); PINV(wl[1]); PINV(wl4[0]); PINV(wl4[1]);
#define K2_W(s, n) ((n) < 4 ? LDSR(wl[(s) / 9], ((s) % 9) * 4608 + (n) * 1024) : LDSR(wl4[(s) / 9], ((s) % 9) * 4608))
    lds_wait(FL, 13u);
#pragma unroll
    for (int sl = 0; sl < 6; ++sl)
#pragma unroll
        for (int n = 0; n < 5; ++n) { fw[sl][n] = K2_W(sl, n); SB(); }
#pragma unroll
    for (int n = 0; n < 5; ++n) PINV(ac[n]);
    lds_wait(FL + 52, 1u);
    STAMP(layer * 4 + 1, 1);
#pragma unroll
    for (int sl = 0; sl < 18; ++sl) { fa[sl] = K2_LD(vo + (sl & 3) * 1024, WS_O + LSH * layer * XB + ((4 * mt + wid) * 18 + (sl & ~3)) * 1024, 16); SB(); }
#pragma unroll
    for (int sl = 0; sl < 18; ++sl)
#pragma unroll
        for (int n = 0; n < 5; ++n) {
            MFMA(ac[n], fw[sl % 6][n], fa[sl]);
            SB();
            if (sl + 6 < 18) fw[sl % 6][n] = K2_W(sl + 6, n);
            SB();
        }
    asm volatile("s_nop 15" ::: "memory");
    STAMP(layer * 4 + 1, 3);
    proj_store(ac, ws, nullptr, 2 * layer + 1, mt, nt, wid, lane);
    STAMP(layer * 4 + 1, 2);
    STAMP_OUT(layer * 4 + 1);
}

constexpr u32 STAGE = 18432, NS = 6;
constexpr u32 K3_PK = NS * STAGE, K3_PP = 304, K3_WS = K3_PK + 128 * K3_PP, K3_S1 = K3_WS + 4096, K3_S2 = K3_S1 + 2048, K3_CS = K3_S2 + 1024, K3_FL = K3_CS + 1280, K3_LDS = K3_FL + 128;

__global__ __launch_bounds__(512, 2) void k34_mlp(char* __restrict__ ws, const u16* __restrict__ wsp, const u16* __restrict__ bsp, u16* __restrict__ out, int layer) {
    extern __shared__ __attribute__((aligned(16))) char shm_[];
    const u32 lds = (u32)(size_t)(LAS char*)shm_;
    const int tid = threadIdx.x, wid = __builtin_amdgcn_readfirstlane(tid >> 6), lane = tid & 63, c = lane & 15, g = lane >> 4;
    const int xcd = blockIdx.x & 7, bi = blockIdx.x >> 3;
    const int pm = MAP3 ? (xcd & 3) * 4 + (bi & 3) : (xcd & 1) * 8 + (bi & 7), pn = MAP3 ? (xcd >> 2) * 8 + (bi >> 2) : (xcd >> 1) * 4 + (bi >> 3);
    const u32 FL = lds + K3_FL;
    STAMP_DECL;
    const auto rs = __builtin_amdgcn_make_buffer_rsrc(ws, 0, WS_END, 0x00020000);
    if (wid == 4) {
        u32 ao[16];
        const u32 vo = lane * 16;
#pragma unroll
        for (int q = 0; q < 16; ++q) { ao[q] = vo + (q >> 1) * 18432 - (q & 2) * 1024 + 4096; PINV(ao[q]); }
        const char* ap = ws + WS_X + (2 * layer + 1) * XB + 8 * pm * 18432 - 4096;
        const char* xp = ws + WS_WIN + (144 + 9 * pn + 8) * 18432;
        u32 slot = 0;
#define K3_ISSUE() do { const u32 dst = lds + slot * STAGE;                                                          \
            DMA4(ao[0], ao[1], ao[2], ao[3], ap, dst); DMA4(ao[4], ao[5], ao[6], ao[7], ap, dst + 4096);             \
            DMA4(ao[8], ao[9], ao[10], ao[11], ap, dst + 8192); DMA4(ao[12], ao[13], ao[14], ao[15], ap, dst + 12288); } while (0)
#define K3_SEG(s0, s1, GATE) for (int s = (s0); s < (s1) + 3; ++s) {                                                 \
            if (s >= (s0) + 3) {                                                                                     \
                if (s <= (s1)) WAIT_V(36); else if (s == (s1) + 1) WAIT_V(18); else WAIT_V(0);                       \
                stsw(FL, s - 2); FENCE(); }                                                                          \
            if (s == 1) BAR();                                                                                       \
            if (s == 2) { WAIT_V(18); stsw(FL, 1u); FENCE(); }                                                       \
            if (s < (s1)) {                                                                                          \
                if (s >= (int)NS) lds_wait_sleep(FL + 16 + (lane & 3) * 4, GATE);                                    \
                if (s == 9) { ap -= 9 * 2048; xp = ws + WS_WIN + (9 * pn + 8) * 18432; }                             \
                K3_ISSUE();                                                                                          \
                DMA2(vo, vo, xp, lds + slot * STAGE + 16384);                                                        \
                xp += 2048; ap += 2048;                                                                              \
                slot = slot + 1 == NS ? 0 : slot + 1; } }
        K3_SEG(0, ZSPLIT, s - NS + 1);
        K3_SEG(ZSPLIT, 18, s >= 15 ? LGATE : max(ZGATE, s - (int)NS + 1));
#pragma unroll
        for (int q = 0; q < 16; ++q) { ao[q] = (q * 8 + (lane >> 3)) * 4096 + (((lane & 7) ^ ((q & 1) * 4 + g)) * 16) - (q & 3) * 1024 + 4096; PINV(ao[q]); }
        ap = (const char*)wsp + (size_t)pm * 128 * 4096 - 4096;
        lds_wait_sleep(FL + 16 + (lane & 3) * 4, LGATE2);
        for (int s = 18; s < 53; ++s) {
            if (s >= 21) {
                if (s <= 50) WAIT_V(32); else if (s == 51) WAIT_V(16); else WAIT_V(0);
                stsw(FL, s - 2);
                FENCE();
            }
            if (s < 50) {
                lds_wait_sleep(FL + 16 + (lane & 3) * 4, s - NS + 1);
                K3_ISSUE();
                ap += AFAKE ? 0 : (s == 33 ? 128 - 15 * 256 : 256);
                slot = slot + 1 == NS ? 0 : slot + 1;
            }
        }
        lds_wait_sleep(FL + 80, 2u);
        for (int i = 0; i < 8; ++i) {
            const int r = i < 2 ? 6 + i : (i < 6 ? i : i - 6);
            if (i >= 2) lds_wait_sleep(FL + 16 + (lane & 3) * 4, 43 + i);
            if (WGATE && i == WGI) lds_wait_sleep(FL + 92, WGATE);
            const char* wp = ws + WS_WOUT + ((MAP5 ? pn & 7 : pn >> 1) * 72 + 18 * (r >> 1) + 4 * (r & 1)) * 4608;
            const u32 dst = lds + r * STAGE;
            DMA4(vo, vo, vo, vo, wp, dst); DMA4(vo, vo, vo, vo, wp + 4096, dst + 4096); DMA4(vo, vo, vo, vo, wp + 8192, dst + 8192);
            DMA4(vo, vo, vo, vo, wp + 12288, dst + 12288); DMA2(vo, vo, wp + 16384, dst + 16384);
            if (i >= 2) WAIT_V(36);
        }
        WAIT_V(0);
        stsw(FL, 100u);
        return;
    }
    if (wid > 4) {
        const int ht = tid - 320;
        if (ht >= 128 && ht < 160) stsw(FL + (ht - 128) * 4, ht == 129 ? 18u : 0u);
        WAIT_L0();
        BAR();
        if (wid != 7) {
            float mean, var;
            x_stats(ws, 2 * layer + 1, 128 * pm + ht, mean, var);
            const float r = __builtin_amdgcn_rsqf(var + 1e-5f);
            f32x4 o = {-mean, -2.302208198f * r, -0.1029432397f * r * r * r, (var + 1e-5f) * r};
            *(LAS f32x4*)(size_t)(lds + K3_S1 + ht * 16) = o;
        } else {
            const int i = min(lane, 35);
            const f32x4* cp = (const f32x4*)(ws + WS_CSIN) + 36 * pn + i;
            const f32x4 a0 = cp[576], a1 = cp[576 + 1152], b0 = cp[0], b1 = cp[1152];
            *(LAS f32x4*)(size_t)(lds + K3_CS + i * 16) = a0 + a1;
            *(LAS f32x4*)(size_t)(lds + K3_CS + 640 + i * 16) = b0 + b1;
        }
        FENCE();
        stsw(FL + 32 + (wid - 5) * 4, 1u);
        u32 vo = lane * 16;
        PINV(vo);
        GAS u32* flg = (GAS u32*)(ws + WS_CNT + (layer * 16 + pn) * 4096);
        HST_DECL;
        if (wid != 5) {
            const int h = wid - 6;
            u32 ta = lds + K3_PK + (32 * h + 8 * g + (c >> 2)) * K3_PP + 16 * (c & 3), tx = ta + 256 - 8 * (c & 3), s2r = lds + K3_S2 + 128 * h + 32 * g;
            PINV(ta); PINV(tx); PINV(s2r);
            const u32 zob = WS_Z + LSH * layer * ZB + (9 * pn * 64 + 2 * pm + h) * 1024;
            if (layer == 0) {
                if (h == 1) lds_wait_sleep(FL + 16 + (lane & 3) * 4, TG1); else lds_wait_sleep(FL + 16 + (lane & 3) * 4, TG0);
                touch_paced<TS3>((const char*)wsp + blockIdx.x * 32768 + h * 16384, 16, vo);
            }
            lds_wait_sleep(FL + 48 + (lane & 3) * 4, 1u);
            HST(0);
            if (pm < HPRIO) __builtin_amdgcn_s_setprio(3);
            float zf[18][8];
#define K3_TR(fi, A, O) do { u32x2 t0_, t1_, t2_, t3_;                                                               \
                asm volatile("ds_read_b64_tr_b16 %0, %4 offset:%5\n\tds_read_b64_tr_b16 %1, %4 offset:%6\n\tds_read_b64_tr_b16 %2, %4 offset:%7\n\t" \
                             "ds_read_b64_tr_b16 %3, %4 offset:%8\n\ts_waitcnt lgkmcnt(0)" : "=&v"(t0_), "=&v"(t1_), "=&v"(t2_), "=&v"(t3_) \
                             : "v"(A), "i"(O), "i"((O) + 4 * K3_PP), "i"((O) + 64 * K3_PP), "i"((O) + 68 * K3_PP) : "memory"); \
                const u32 w_[8] = {t0_[0], t0_[1], t1_[0], t1_[1], t2_[0], t2_[1], t3_[0], t3_[1]};                  \
                _Pragma("unroll") for (int i_ = 0; i_ < 8; ++i_) { zf[2 * (fi) + (i_ >> 2)][2 * (i_ & 3)] = bflo(w_[i_]); zf[2 * (fi) + (i_ >> 2)][2 * (i_ & 3) + 1] = bfhi(w_[i_]); \
                    PINV(zf[2 * (fi) + (i_ >> 2)][2 * (i_ & 3)]); PINV(zf[2 * (fi) + (i_ >> 2)][2 * (i_ & 3) + 1]); } } while (0)
            K3_TR(0, ta, 0); K3_TR(1, ta, 8); K3_TR(2, ta, 64); K3_TR(3, ta, 72); K3_TR(4, ta, 128); K3_TR(5, ta, 136); K3_TR(6, ta, 192); K3_TR(7, ta, 200);
            K3_TR(8, tx, 0);
            HST(1);
            lds_wait_sleep(FL + 64, 1u);
            for (int i = 2; i < pm; ++i) __builtin_amdgcn_s_sleep(STAG);
            HST(2);
            f32x4 sv[2][4];
#pragma unroll
            for (int q = 0; q < 2; ++q)
#pragma unroll
                for (int k = 0; k < 4; ++k) sv[q][k] = *(LAS f32x4*)(size_t)(s2r + (k >> 1) * 512 + 256 * q + (k & 1) * 16);
#pragma unroll
            for (int ii = 0; ii < 18; ++ii) {
                const int q = ii / 9, i = 2 * (ii % 9) + q;
                u32x4 o;
                asm("v_fma_f32 %4, %4, %12, %20\n\tv_fma_f32 %5, %5, %13, %21\n\tv_fma_f32 %6, %6, %14, %22\n\tv_fma_f32 %7, %7, %15, %23\n\t"
                    "v_fma_f32 %8, %8, %16, %24\n\tv_fma_f32 %9, %9, %17, %25\n\tv_fma_f32 %10, %10, %18, %26\n\tv_fma_f32 %11, %11, %19, %27\n\t"
                    "v_cvt_pk_bf16_f32 %0, %4, %5\n\tv_cvt_pk_bf16_f32 %1, %6, %7\n\tv_cvt_pk_bf16_f32 %2, %8, %9\n\tv_cvt_pk_bf16_f32 %3, %10, %11"
                    : "=&v"(o[0]), "=&v"(o[1]), "=&v"(o[2]), "=&v"(o[3]), "+v"(zf[i][0]), "+v"(zf[i][1]), "+v"(zf[i][2]), "+v"(zf[i][3]), "+v"(zf[i][4]), "+v"(zf[i][5]), "+v"(zf[i][6]), "+v"(zf[i][7])
                    : "v"(sv[q][0][0]), "v"(sv[q][0][1]), "v"(sv[q][0][2]), "v"(sv[q][0][3]), "v"(sv[q][1][0]), "v"(sv[q][1][1]), "v"(sv[q][1][2]), "v"(sv[q][1][3]),
                      "v"(sv[q][2][0]), "v"(sv[q][2][1]), "v"(sv[q][2][2]), "v"(sv[q][2][3]), "v"(sv[q][3][0]), "v"(sv[q][3][1]), "v"(sv[q][3][2]), "v"(sv[q][3][3]));
                __builtin_amdgcn_raw_buffer_store_b128(o, rs, vo, zob + q * 32768 + (i >> 1) * 65536, 16);
                SB();
                if (ii % 9 == 8) {
                    if (q == 0) HST(3);
                    WAIT_V(0);
                    if (q == 0) HST(4);
                    u32 old = 0;
                    if (lane == 0) old = __hip_atomic_fetch_add((LAS u32*)(size_t)(FL + 76 + 4 * q), 1u, __ATOMIC_RELAXED, __HIP_MEMORY_SCOPE_WORKGROUP);
                    if (__builtin_amdgcn_readfirstlane(old) == 1 && lane < 16)
                        __hip_atomic_fetch_add(flg + lane * 64, 1u << (16 * q + pm), RLX_AGENT);
                    if (q == 0) { __builtin_amdgcn_s_setprio(0); __builtin_amdgcn_s_sleep(STAG2); }
                    SB();
                }
            }
            if (HPW && layer == 0) {
                lds_wait_sleep(FL + 16 + (lane & 3) * 4, HPW);
                const u32x4 z4 = {0u, 0u, 0u, 0u};
#pragma nounroll
                for (int i = 0; i < 16; ++i) {
                    BST128(z4, rs, vo, WS_H + ((8 * pm + 4 * h + (i >> 2)) * 8 + (pn >> 1)) * 9216 + ((pn & 1) * 4 + (i & 3)) * 1024, 16);
                    __builtin_amdgcn_s_sleep(HPS);
                }
                WAIT_V(0);
            }
            stsw(FL + 112 + 4 * h, 1u);
            HST(5);
            if (wid == 6) HST_OUT(9 + layer);
            return;
        }
        u32 wsa = lds + K3_WS + lane * 16, s2w = lds + K3_S2 + lane * 8;
        PINV(wsa); PINV(s2w);
        const u32 zsb = WS_ZST + (layer * 16 + pm) * 16384;
        lds_wait_sleep(FL + 16 + (lane & 3) * 4, 9u);
        __builtin_amdgcn_s_sleep(POLLD);
        __builtin_amdgcn_s_setprio(3);
        lds_wait(FL + 48 + (lane & 3) * 4, 1u);
        STAMP(layer * 4 + 2, 8);
        {
            const f32x4 st = sadd4(sadd4(*(LAS f32x4*)(size_t)wsa, *(LAS f32x4*)(size_t)(wsa + 1024)), sadd4(*(LAS f32x4*)(size_t)(wsa + 2048), *(LAS f32x4*)(size_t)(wsa + 3072)));
            u32x4 o = __builtin_bit_cast(u32x4, st);
#pragma unroll
            for (int k = 0; k < 4; ++k) o[k] = o[k] == SENT ? 0x7fc00000u : o[k];
            BST128(o, rs, vo, zsb + pn * 1024, 16);
            __builtin_amdgcn_s_sleep(XDELAY3);
            u32x4 t[16];
#pragma unroll
            for (int k = 0; k < 16; ++k) t[k] = __builtin_amdgcn_raw_buffer_load_b128(rs, vo + (k & 3) * 1024, zsb + (k >> 2) * 4096, 16);
            u32 mx = 0;
#pragma unroll
            for (int k = 0; k < 16; ++k) mx = max(max(mx, max(t[k][0], t[k][1])), max(t[k][2], t[k][3]));
            u32 spins = 0;
            if (__builtin_expect(ANY(mx == SENT), 0)) {
#pragma nounroll
                for (spins = 1; spins < SPIN_CAP; ++spins) {
                    FENCE();
                    u32 miss = 0;
#pragma unroll
                    for (int k = 0; k < 16; ++k) miss |= ANY(max(max(t[k][0], t[k][1]), max(t[k][2], t[k][3])) == SENT) ? 1u << k : 0u;
                    if (!miss) break;
#pragma unroll
                    for (int k = 0; k < 16; ++k) if (miss >> k & 1) t[k] = __builtin_amdgcn_raw_buffer_load_b128(rs, vo + (k & 3) * 1024, zsb + (k >> 2) * 4096, 16);
                }
            }
            f32x4 sa[4];
#pragma unroll
            for (int k = 0; k < 4; ++k)
                sa[k] = sadd4(sadd4(__builtin_bit_cast(f32x4, t[4 * k]), __builtin_bit_cast(f32x4, t[4 * k + 1])), sadd4(__builtin_bit_cast(f32x4, t[4 * k + 2]), __builtin_bit_cast(f32x4, t[4 * k + 3])));
            const f32x4 ss = sadd4(sadd4(sa[0], sa[1]), sadd4(sa[2], sa[3]));
            f32x2 rr, mr;
#pragma unroll
            for (int k = 0; k < 2; ++k) {
                const float m2 = sfma(ss[2 * k], 1.f / 2304.f, 0.f);
                float v2 = fmaxf(sfma(-m2, m2, sfma(ss[2 * k + 1], 1.f / 2304.f, 0.f)), 0.f) + 1e-5f;
                PINV(v2);
                rr[k] = __builtin_amdgcn_rsqf(v2);
                float t2 = -m2 * rr[k];
                PINV(t2);
                mr[k] = t2;
            }
            *(LAS f32x2*)(size_t)s2w = rr;
            *(LAS f32x2*)(size_t)(s2w + 512) = mr;
            FENCE();
            stsw(FL + 64, 1u);
            FENCE();
        }
        STAMP(layer * 4 + 2, 9);
        STAMP(layer * 4 + 2, 10);
        u32 ready = 0;
#define K3_NEED(n) do { _Pragma("nounroll") for (u32 spins_ = 0; ready < (u32)(n) && spins_ < SPIN_CAP; ++spins_) {   \
            const u32 v_ = __builtin_amdgcn_readfirstlane(__hip_atomic_load(flg + pm * 64, RLX_AGENT));                                               \
            ready = __builtin_ctzll(~(u64)v_); } FENCE(); } while (0)
        __builtin_amdgcn_s_sleep(FDELAY);
        K3_NEED(3);
        stsw(FL + 84, 1u);
        FENCE();
        STAMP(layer * 4 + 2, 11);
        STAMP_OUT(layer * 4 + 2);
        __builtin_amdgcn_s_setprio(0);
        const u32 zx = WS_Z + LSH * layer * ZB + (9 * pn + 8) * 65536;
        u32x4 xr[4][2];
#pragma unroll
        for (int k = 0; k < 3; ++k) {
            xr[k][0] = __builtin_amdgcn_raw_buffer_load_b128(rs, vo, zx + k * 2048, 16);
            xr[k][1] = __builtin_amdgcn_raw_buffer_load_b128(rs, vo + 1024, zx + k * 2048, 16);
            SB();
        }
        u32 slot = 0;
        for (int s0 = 0; s0 < 32; s0 += 4) {
#pragma unroll
            for (int k = 0; k < 4; ++k) {
                const int s = s0 + k;
                lds_wait_sleep(FL + 16 + (lane & 3) * 4, 18 + s - NS + 1);
                *(LAS u32x4*)(size_t)(lds + slot * STAGE + 16384 + lane * 16) = xr[k][0];
                *(LAS u32x4*)(size_t)(lds + slot * STAGE + 17408 + lane * 16) = xr[k][1];
                FENCE();
                stsw(FL + 4, 18 + s + 1);
                FENCE();
                const u32 sn = s + 3 < 32 ? s + 3 : 31;
                K3_NEED(sn + 1);
                xr[(k + 3) & 3][0] = __builtin_amdgcn_raw_buffer_load_b128(rs, vo, zx + sn * 2048, 16);
                xr[(k + 3) & 3][1] = __builtin_amdgcn_raw_buffer_load_b128(rs, vo + 1024, zx + sn * 2048, 16);
                slot = slot + 1 == NS ? 0 : slot + 1;
            }
        }
        lds_wait_sleep(FL + 92, 4u);
        __builtin_amdgcn_s_sleep(HDELAY);
        GAS u32* hm = (GAS u32*)(ws + WS_CNT + (layer * 16 + pm) * 4096 + pn * 256 + 128);
#pragma nounroll
        for (u32 spins = 0; spins < SPIN_CAP; ++spins) {
            const u32 v = __builtin_amdgcn_readfirstlane(__hip_atomic_load(hm, RLX_AGENT));
            FENCE();
            stsw(FL + 88, v);
            if (v == 0xffffu) break;
        }
        return;
    }
    STAMP(layer * 4 + 2, 0);
    u32 vA = lane * 16, vB = lane * 16 + 18432;
    u32 so = WS_WIN + (144 + 9 * pn + 2 * wid) * 18432;
    bf16x8 bb[6][2];
#define BLD(voff, imm, aux) __builtin_bit_cast(bf16x8, __builtin_amdgcn_raw_buffer_load_b128(rs, (voff) + (imm), so, aux))
#define BPRO(aux) do { _Pragma("unroll") for (int r = 0; r < 5; ++r) { if (r == 3) so += 3072;                       \
        bb[r][0] = BLD(vA, (r % 3) * 1024, aux); SB(); bb[r][1] = BLD(vB, (r % 3) * 1024, aux); SB(); } } while (0)
    BPRO(0);
    u32 pa[2][4];
#pragma unroll
    for (int q = 0; q < 4; ++q) { pa[0][q] = lds + lane * 16 + ((q ^ wid) * 4096); pa[1][q] = pa[0][q] + 1024; }
    u32 px = lds + 16384 + lane * 16;
    u32 myfree = FL + 16 + wid * 4, fladdr = FL + (lane & 1) * 4, cnt = 0;
    PINV(cnt); PINV(myfree); PINV(fladdr);
    int dstep = 3 * STAGE;
    f32x4 acc[8][2], accx[2];
    bf16x8 a[8], xf;
#define ALOAD() do { a[0] = LDSR(pa[0][0], 0); SB(); xf = LDSR(px, 0); SB();                                         \
        _Pragma("unroll") for (int m = 1; m < 8; ++m) { a[m] = LDSR(pa[0][m >> 1], (m & 1) * 2048); SB(); } } while (0)
#define MFS(acc, x, y) do { MFMA(acc, x, y); SB(); } while (0)
#define HALF(r, RD, kn, sf, rl, imm, aux, E0, E1, E2, E3, E4, E5) do {                                               \
        WCNT(8, 5); MFS(acc[0][0], bb[r][0], a[0]); E0; SB();                                                       \
        MFS(acc[0][1], bb[r][1], a[0]); E1; SB();                                                                   \
        MFS(accx[0], xf, a[0]); if (RD) a[0] = LDSR(pa[kn][0], (sf)); SB();                                         \
        MFS(accx[1], xf, a[1]); if (RD) xf = LDSR(px, (sf) + (kn) * 1024); SB();                                    \
        MFS(acc[1][1], bb[r][1], a[1]); bb[rl][0] = BLD(vA, imm, aux); SB();                                        \
        MFS(acc[1][0], bb[r][0], a[1]); if (RD) a[1] = LDSR(pa[kn][0], (sf) + 2048); SB();                          \
        MFS(acc[2][0], bb[r][0], a[2]); E2; SB();                                                                   \
        MFS(acc[2][1], bb[r][1], a[2]); if (RD) a[2] = LDSR(pa[kn][1], (sf)); SB();                                 \
        WCNT(63, 5); MFS(acc[3][1], bb[r][1], a[3]); SB();                                                          \
        MFS(acc[3][0], bb[r][0], a[3]); if (RD) a[3] = LDSR(pa[kn][1], (sf) + 2048); SB();                          \
        MFS(acc[4][0], bb[r][0], a[4]); bb[rl][1] = BLD(vB, imm, aux); SB();                                        \
        MFS(acc[4][1], bb[r][1], a[4]); if (RD) a[4] = LDSR(pa[kn][2], (sf)); SB();                                 \
        MFS(acc[5][1], bb[r][1], a[5]); E3; SB();                                                                   \
        MFS(acc[5][0], bb[r][0], a[5]); if (RD) a[5] = LDSR(pa[kn][2], (sf) + 2048); SB();                          \
        MFS(acc[6][0], bb[r][0], a[6]); E4; SB();                                                                   \
        MFS(acc[6][1], bb[r][1], a[6]); if (RD) a[6] = LDSR(pa[kn][3], (sf)); SB();                                 \
        MFS(acc[7][1], bb[r][1], a[7]); E5; SB();                                                                   \
        MFS(acc[7][0], bb[r][0], a[7]); if (RD) a[7] = LDSR(pa[kn][3], (sf) + 2048); SB(); } while (0)
#define STEP(u, chk, nso, aux) do { u32 fl_; u64 msk_;                                                               \
        HALF(2 * (u), 1, 1, (u) * STAGE, (2 * (u) + 5) % 6, ((2 * (u) + 5) % 3) * 1024, aux,                         \
             { if ((u) == 2) so += 3072; }, { cnt += 1; PINV(cnt); }, { FENCE(); fl_ = ldsw(fladdr); FENCE(); },     \
             { if ((u) == 2) { pa[0][0] += dstep; pa[0][1] += dstep; PINV(pa[0][0]); PINV(pa[0][1]); } },            \
             { if ((u) == 2) { pa[0][2] += dstep; pa[0][3] += dstep; PINV(pa[0][2]); PINV(pa[0][3]); } },            \
             { asm volatile("v_cmp_le_u32 %0, %1, %2" : "=s"(msk_) : "v"(fl_), "v"(cnt)); if ((u) == 2) { px += dstep; PINV(px); } }); \
        HALF(2 * (u) + 1, 1, 0, (((u) + 1) % 3) * STAGE, (2 * (u) + 6) % 6, ((2 * (u) + 6) % 3) * 1024, aux,         \
             { if (__builtin_expect(msk_ != 0, 0)) { if (chk) lds_wait_gt(fladdr, cnt); } FENCE(); }, { if ((u) == 0) so = (nso); }, \
             { FENCE(); stsw(myfree, cnt); FENCE(); },                                                               \
             { if ((u) == 2) { pa[1][0] += dstep; pa[1][1] += dstep; PINV(pa[1][0]); PINV(pa[1][1]); } },            \
             { if ((u) == 2) { pa[1][2] += dstep; pa[1][3] += dstep; PINV(pa[1][2]); PINV(pa[1][3]); } },            \
             { if ((u) == 2) dstep = -dstep; }); } while (0)
#define PINACC() do { _Pragma("unroll") for (int m = 0; m < 8; ++m) { PINV(acc[m][0]); PINV(acc[m][1]); } PINV(accx[0]); PINV(accx[1]); asm volatile("s_nop 1"); } while (0)
    BAR();
    u32 yp[18][2], early = 0;
    const u32 s1a = lds + K3_S1 + c * 16;
#pragma unroll
    for (int ph = 0; ph < 2; ++ph) {
        if (ph == 0) {
#pragma unroll
            for (int m = 0; m < 8; ++m) { acc[m][0] = acc[m][1] = (f32x4){0.f, 0.f, 0.f, 0.f}; if (m < 2) accx[m] = acc[m][0]; }
        } else {
            const u32 ca = lds + K3_CS + ph * 640 + g * 16;
            const f32x4 c0 = *(LAS f32x4*)(size_t)(ca + wid * 128), c1 = *(LAS f32x4*)(size_t)(ca + wid * 128 + 64), c2 = *(LAS f32x4*)(size_t)(ca + 512);
#pragma unroll
            for (int m = 0; m < 8; ++m) {
                const float nm = *(LAS float*)(size_t)(s1a + ((((m >> 1) ^ wid) << 1) + (m & 1)) * 256);
                acc[m][0] = c0 * nm; acc[m][1] = c1 * nm;
                if (m < 2) accx[m] = c2 * nm;
            }
        }
        PINACC();
        if (ph == 0) { lds_wait(fladdr, 1u); ALOAD(); STAMP(layer * 4 + 2, 1); }
        const u32 nxt = WS_WIN + (9 * pn + 2 * wid) * 18432;
        for (int j = 0; j < 3; ++j) {
            STEP(0, true, j == 2 ? nxt : so + 3072, 0);
            STEP(1, true, 0, 0);
            STEP(2, ph == 0 || j < 2, 0, 0);
        }
        asm volatile("s_nop 15" ::: "memory");
        STAMP(layer * 4 + 2, 2 + 2 * ph);
        if (ph == 1) {
            vB = lane * 16 + 65536;
            so = WS_Z + LSH * layer * ZB + (9 * pn + 2 * wid) * 65536;
            FENCE();
            early = __builtin_amdgcn_readfirstlane(ldsw(FL + 84));
            FENCE();
            if (early) BPRO(16);
        }
        float t1[8], t2[8];
        f32x4 rcs[8], cz[3];
        if (ph == 0) {
            lds_wait(FL + 32 + (lane % 3) * 4, 1u);
            const u32 ca = lds + K3_CS + g * 16;
            cz[0] = *(LAS f32x4*)(size_t)(ca + wid * 128); cz[1] = *(LAS f32x4*)(size_t)(ca + wid * 128 + 64); cz[2] = *(LAS f32x4*)(size_t)(ca + 512);
        }
#pragma unroll
        for (int m = 0; m < 8; ++m) rcs[m] = *(LAS f32x4*)(size_t)(s1a + ((((m >> 1) ^ wid) << 1) + (m & 1)) * 256);
#pragma unroll
        for (int m = 0; m < 8; ++m) {
            if (ph == 1 && m == 4 && !early) { FENCE(); early = __builtin_amdgcn_readfirstlane(ldsw(FL + 84)); FENCE(); if (early) BPRO(16); }
            constexpr int NP = 6;
            const int np = m < 2 ? 6 : 4;
            const f32x2 k1 = {rcs[m][1], rcs[m][1]}, k3 = {rcs[m][2], rcs[m][2]}, sg = {rcs[m][3], rcs[m][3]};
            f32x2 v[NP], e[NP];
#pragma unroll
            for (int i = 0; i < NP; ++i) if (i < np) {
                const f32x4 av = i < 4 ? acc[m][i >> 1] : accx[m & 1];
                v[i] = (f32x2){av[2 * (i & 1)], av[2 * (i & 1) + 1]};
                if (ph == 0) v[i] += (f32x2){cz[i >> 1][2 * (i & 1)], cz[i >> 1][2 * (i & 1) + 1]} * (f32x2){rcs[m][0], rcs[m][0]};
            }
#pragma unroll
            for (int i = 0; i < NP; ++i) if (i < np) e[i] = v[i] * v[i];
#pragma unroll
            for (int i = 0; i < NP; ++i) if (i < np) e[i] = e[i] * k3 + k1;
#pragma unroll
            for (int i = 0; i < NP; ++i) if (i < np) e[i] = e[i] * v[i];
            SB();
#pragma unroll
            for (int i = 0; i < NP; ++i) if (i < np) { e[i][0] = __builtin_amdgcn_exp2f(e[i][0]); e[i][1] = __builtin_amdgcn_exp2f(e[i][1]); }
            SB();
#pragma unroll
            for (int i = 0; i < NP; ++i) if (i < np) e[i] = e[i] * sg + sg;
            SB();
#pragma unroll
            for (int i = 0; i < NP; ++i) if (i < np) { e[i][0] = __builtin_amdgcn_rcpf(e[i][0]); e[i][1] = __builtin_amdgcn_rcpf(e[i][1]); }
            SB();
#pragma unroll
            for (int i = 0; i < NP; ++i) if (i < np) v[i] = v[i] * e[i];
#pragma unroll
            for (int i = 0; i < NP; ++i) if (i < np) yp[i < 4 ? 2 * m + (i >> 1) : 16 + m][i & 1] = pk2(v[i][0], v[i][1]);
            if (ph == 0) {
                f32x2 u1 = (v[0] + v[1]) + (v[2] + v[3]), u2 = v[0] * v[0], u3 = v[1] * v[1];
                u2 = v[2] * v[2] + u2; u3 = v[3] * v[3] + u3;
                if (m < 2) { u1 += v[4] + v[5]; u2 = v[4] * v[4] + u2; u3 = v[5] * v[5] + u3; }
                u2 += u3;
                t1[m] = u1[0] + u1[1]; t2[m] = u2[0] + u2[1];
            }
            PINV(yp[2 * m][0]); PINV(yp[2 * m][1]); PINV(yp[2 * m + 1][0]); PINV(yp[2 * m + 1][1]);
            if (m < 2) { PINV(yp[16 + m][0]); PINV(yp[16 + m][1]); }
            if (ph == 0) {
                const u32 row = (((((m >> 1) ^ wid) << 1) + (m & 1)) << 4) + c;
                u32x4 o = {yp[2 * m][0], yp[2 * m][1], yp[2 * m + 1][0], yp[2 * m + 1][1]};
                *(LAS u32x4*)(size_t)(lds + K3_PK + row * K3_PP + 64 * wid + 16 * g) = o;
                if (m < 2) { u32x2 ox = {yp[16 + m][0], yp[16 + m][1]}; *(LAS u32x2*)(size_t)(lds + K3_PK + row * K3_PP + 256 + 8 * g) = ox; }
            }
        }
        if (ph == 0) {
#pragma unroll
            for (int m = 0; m < 4; ++m) {
                auto r1 = __builtin_amdgcn_permlane32_swap(F2U(t1[m]), F2U(t1[m + 4]), false, false);
                auto r2 = __builtin_amdgcn_permlane32_swap(F2U(t2[m]), F2U(t2[m + 4]), false, false);
                t1[m] = U2F(r1[0]) + U2F(r1[1]); t2[m] = U2F(r2[0]) + U2F(r2[1]);
            }
#pragma unroll
            for (int m = 0; m < 2; ++m) {
                auto r1 = __builtin_amdgcn_permlane16_swap(F2U(t1[m]), F2U(t1[m + 2]), false, false);
                auto r2 = __builtin_amdgcn_permlane16_swap(F2U(t2[m]), F2U(t2[m + 2]), false, false);
                f32x2 o = {U2F(r1[0]) + U2F(r1[1]), U2F(r2[0]) + U2F(r2[1])};
                *(LAS f32x2*)(size_t)(lds + K3_WS + (wid * 128 + 32 * (g ^ wid) + 16 * m + c) * 8) = o;
            }
            FENCE();
            stsw(FL + 48 + wid * 4, 1u);
            FENCE();
        }
        STAMP(layer * 4 + 2, 3 + 2 * ph);
    }
    {
        float bv[8];
#pragma unroll
        for (int m = 0; m < 8; ++m) bv[m] = bflo(bsp[128 * pm + (((((m >> 1) ^ wid) << 1) + (m & 1)) << 4) + c]);
#pragma unroll
        for (int m = 0; m < 8; ++m) {
            acc[m][0] = acc[m][1] = (f32x4){bv[m], bv[m], bv[m], bv[m]};
            if (m < 2) accx[m] = acc[m][0];
        }
    }
#pragma unroll
    for (int q = 0; q < 4; ++q) { pa[0][q] = lds + (c >> 3) * 1024 + (c & 7) * 128 + ((g ^ ((c >> 1) & 7)) * 16) + ((q ^ wid) * 4096); pa[1][q] = pa[0][q] ^ 64; }
    if (!early) { lds_wait(FL + 84, 1u); BPRO(16); }
    lds_wait(fladdr, 19u);
    STAMP(layer * 4 + 2, 6);
    ALOAD();
    PINACC();
    for (int j = 0; j < 10; ++j) {
        STEP(0, true, so + BADV, BAUX);
        STEP(1, true, 0, BAUX);
        STEP(2, true, 0, BAUX);
    }
    if (BADV > 0) so -= 8192;
    STEP(0, true, so, 16);
    STEP(1, false, 0, 16);
    asm volatile("s_nop 15" ::: "memory");
    STAMP(layer * 4 + 2, 7);
    {
        lds_wait(FL + 112 + (lane & 1) * 4, 1u);
        const u32 hb = WS_H + LSH * layer * ZB + (8 * pm * 8 + (pn >> 1)) * 9216;
#pragma unroll
        for (int m = 0; m < 8; ++m) {
            const int mt = (((m >> 1) ^ wid) << 1) + (m & 1);
            u32x4 o;
            o[0] = pk2(bflo(yp[2 * m][0]) * acc[m][0][0], bfhi(yp[2 * m][0]) * acc[m][0][1]);
            o[1] = pk2(bflo(yp[2 * m][1]) * acc[m][0][2], bfhi(yp[2 * m][1]) * acc[m][0][3]);
            o[2] = pk2(bflo(yp[2 * m + 1][0]) * acc[m][1][0], bfhi(yp[2 * m + 1][0]) * acc[m][1][1]);
            o[3] = pk2(bflo(yp[2 * m + 1][1]) * acc[m][1][2], bfhi(yp[2 * m + 1][1]) * acc[m][1][3]);
            BST128(o, rs, vA, hb + mt * 73728 + ((pn & 1) * 4 + wid) * 1024, 16);
            if (m < 2) {
                u32x2 ox = {pk2(bflo(yp[16 + m][0]) * accx[m][0], bfhi(yp[16 + m][0]) * accx[m][1]), pk2(bflo(yp[16 + m][1]) * accx[m][2], bfhi(yp[16 + m][1]) * accx[m][3])};
                __builtin_amdgcn_raw_buffer_store_b64(ox, rs, vA, hb + mt * 73728 + 8192 + (pn & 1) * 8, 16);
            }
        }
        STAMP(layer * 4 + 2, 12);
        WAIT_V(0);
        u32 old = 0;
        if (lane == 0) old = __hip_atomic_fetch_add((LAS u32*)(size_t)(FL + 92), 1u, __ATOMIC_RELAXED, __HIP_MEMORY_SCOPE_WORKGROUP);
        if (__builtin_amdgcn_readfirstlane(old) == 3 && lane < 16)
            __hip_atomic_fetch_add((GAS u32*)(ws + WS_CNT + (layer * 16 + pm) * 4096 + lane * 256 + 128), 1u << pn, RLX_AGENT);
    }
    STAMP_OUT(layer * 4 + 2);
    STAMP(layer * 4 + 3, 0);
    {
        const int mt = 2 * pm + (MAP5 ? pn >> 3 : pn & 1), nt = MAP5 ? pn & 7 : pn >> 1;
        const u32 wb = WS_WOUT + (nt * 72 + 18 * wid) * 4608;
        const u32 vo4 = 4096 + g * 128 + (lane & 7) * 16;
        bf16x8 fa[4][4], fw[3][5], fl[5];
        f32x4 ac[4][5];
#pragma unroll
        for (int m = 0; m < 4; ++m)
#pragma unroll
            for (int n = 0; n < 5; ++n) ac[m][n] = (f32x4){0.f, 0.f, 0.f, 0.f};
        u32 abm[4];
#pragma unroll
        for (int m = 0; m < 4; ++m) abm[m] = WS_H + LSH * layer * ZB + ((4 * mt + (m ^ wid)) * 8 + 2 * wid) * 9216;
#define K5_LD(v, sof, aux) __builtin_bit_cast(bf16x8, __builtin_amdgcn_raw_buffer_load_b128(rs, v, sof, aux))
#define K5_LW(r, s) do { _Pragma("unroll") for (int n = 0; n < 4; ++n) fw[r][n] = K5_LD(vA + n * 1024, wb + (s) * 4608, 0); fw[r][4] = K5_LD(vo4, wb + (s) * 4608, 0); SB(); } while (0)
#define K5_LL(s) do { _Pragma("unroll") for (int n = 0; n < 4; ++n) fl[n] = LDSR(wl, (s) * 4608 + n * 1024); fl[4] = LDSR(wl4, (s) * 4608); SB(); } while (0)
#define K5_LA(r, s) do { _Pragma("unroll") for (int m = 0; m < 4; ++m) fa[r][m] = K5_LD(vA + ((s) & 3) * 1024, abm[m] + ((s) & ~3) * 1024, 16); SB(); } while (0)
#define K5_MM(W, r) do { _Pragma("unroll") for (int m = 0; m < 4; ++m) _Pragma("unroll") for (int n = 0; n < 5; ++n) ac[m][n] = MFMAB(W[n], fa[r][m], ac[m][n]); SB(); } while (0)
        PINV(vA);
        const u32 wl = lds + wid * 2 * STAGE + vA, wl4 = lds + wid * 2 * STAGE + vo4;
        K5_LW(0, 8); K5_LW(1, 9); K5_LW(2, 10);
        u32x4 res[2]; u32x2 res2;
        const u32 xoff = WS_X + (2 * layer + 1) * XB + (4 * mt + wid) * 18432;
        {
            const u32 need = 15u << (4 * wid);
            FENCE();
#pragma nounroll
            for (u32 n = 0; n < SPIN_CAP; ++n) { if ((__builtin_amdgcn_readfirstlane(ldsw(FL + 88)) & need) == need) break; }
            FENCE();
        }
        STAMP(layer * 4 + 3, 1);
        constexpr int ord[18] = {8, 0, 9, 1, 10, 2, 11, 3, 12, 4, 13, 5, 14, 6, 15, 7, 16, 17};
#pragma unroll
        for (int i = 0; i < 4; ++i) K5_LA(i, ord[i]);
        lds_wait(FL, 100u);
#pragma unroll
        for (int m = 0; m < 4; ++m)
#pragma unroll
            for (int n = 0; n < 5; ++n) PINV(ac[m][n]);
        asm volatile("s_nop 1");
#pragma unroll
        for (int i = 0; i < 18; ++i) {
            const int sl = ord[i];
#pragma unroll
            for (int m = 0; m < 4; ++m)
#pragma unroll
                for (int n = 0; n < 5; ++n) {
                    if (sl >= 8) MFS(ac[m][n], fw[(sl - 8) % 3][n], fa[i & 3][m]); else MFS(ac[m][n], fl[n], fa[i & 3][m]);
                    const int e = (n == 1 ? m : (m == 0 && n == 3 ? 4 : -1));
                    if (e >= 0) {
                        if (sl >= 8 && sl < 16) fl[e] = e < 4 ? LDSR(wl, (sl - 8) * 4608 + e * 1024) : LDSR(wl4, (sl - 8) * 4608);
                        if (sl < 7) fw[sl % 3][e] = e < 4 ? K5_LD(vA + e * 1024, wb + (sl + 11) * 4608, 0) : K5_LD(vo4, wb + (sl + 11) * 4608, 0);
                        if (sl == 7 && e == 0) res[0] = __builtin_amdgcn_raw_buffer_load_b128(rs, vA, xoff + 2304 * nt, 0);
                        if (sl == 7 && e == 1) res[1] = __builtin_amdgcn_raw_buffer_load_b128(rs, vA + 1024, xoff + 2304 * nt, 0);
                        if (sl == 7 && e == 2) res2 = __builtin_amdgcn_raw_buffer_load_b64(rs, (vA & 255) + ((vA >> 5) & 8) + 2048, xoff + 2304 * nt, 0);
                    }
                    if (n == 4 && i + 4 < 18) fa[i & 3][m] = K5_LD(vA + (ord[i + 4 < 18 ? i + 4 : 0] & 3) * 1024, abm[m] + (ord[i + 4 < 18 ? i + 4 : 0] & ~3) * 1024, 16);
                    SB();
                }
        }
        asm volatile("s_nop 15" ::: "memory");
        STAMP(layer * 4 + 3, 3);
        proj_tail(ac, res, res2, lds, 2 * STAGE, FL + 96, ws, out, 2 * layer + 2, mt, nt, wid, lane);
    }
    STAMP(layer * 4 + 3, 2);
    STAMP_OUT(layer * 4 + 3);
}

extern "C" __global__ void k9_end() {}

extern "C" void kernel_launch(void* const* d_in, const int* in_sizes, int n_in, void* d_out, int out_size, void* d_ws, size_t ws_size,
                              hipStream_t stream) {
    const u16* x = (const u16*)d_in[0];
    const u16* wqkv = (const u16*)d_in[1];
    const u16* wo = (const u16*)d_in[2];
    const u16* win = (const u16*)d_in[4];
    const u16* wsp = (const u16*)d_in[5];
    const u16* bsp = (const u16*)d_in[6];
    const u16* wout = (const u16*)d_in[7];
    char* ws = (char*)d_ws + WS_BASE(ws_size);
    u16* out = (u16*)d_out;
    (void)hipFuncSetAttribute((const void*)k1_attn, hipFuncAttributeMaxDynamicSharedMemorySize, K1_LDS);
    (void)hipFuncSetAttribute((const void*)k34_mlp, hipFuncAttributeMaxDynamicSharedMemorySize, K3_LDS);
    k0_prep<<<K0_GRID, 576, 0, stream>>>(x, wqkv, wo, win, wout, wsp, ws, out);
    for (int layer = 0; layer < 2; ++layer) {
        k1_attn<<<256, 512, K1_LDS, stream>>>(ws, layer);
        k34_mlp<<<256, 512, K3_LDS, stream>>>(ws, wsp, bsp, out, layer);
    }
}
```

```cpp
#include <hip/hip_runtime.h>
#include <hip/hip_bf16.h>
#include <stdint.h>

#define LAS __attribute__((address_space(3)))
#define GAS __attribute__((address_space(1)))
typedef unsigned short u16;
typedef unsigned u32;
typedef unsigned long long u64;
typedef short bf16x8 __attribute__((ext_vector_type(8)));
typedef float f32x2 __attribute__((ext_vector_type(2)));
typedef float f32x4 __attribute__((ext_vector_type(4)));
typedef unsigned u32x2 __attribute__((ext_vector_type(2)));
typedef unsigned u32x4 __attribute__((ext_vector_type(4)));
typedef __bf16 bf2_t __attribute__((ext_vector_type(2)));

#define ANY(p) (__builtin_amdgcn_ballot_w64(p) != 0)
#define U2F(x) __builtin_bit_cast(float, (u32)(x))
#define F2U(x) __builtin_bit_cast(u32, (float)(x))
#define WAIT_V(n) asm volatile("s_waitcnt vmcnt(%0)" ::"i"(n) : "memory")
#define WAIT_L0() asm volatile("s_waitcnt lgkmcnt(0)" ::: "memory")
#define FENCE() asm volatile("" ::: "memory")
#define SB() __builtin_amdgcn_sched_barrier(0)
#define BAR() do { FENCE(); __builtin_amdgcn_s_barrier(); FENCE(); } while (0)
#define WCNT(vm, lg) __builtin_amdgcn_s_waitcnt(((vm) & 15) | (7 << 4) | ((lg) << 8) | (((vm) >> 4) << 14))
#define MFMA(acc, x, y) asm volatile("v_mfma_f32_16x16x32_bf16 %0, %1, %2, %0" : "+v"(acc) : "v"(x), "v"(y))
#define MFMAB(x, y, c) __builtin_amdgcn_mfma_f32_16x16x32_bf16(x, y, c, 0, 0, 0)
#define BST128(o, rs, vo, so, aux) do { __builtin_amdgcn_raw_buffer_store_b128(o, rs, vo, so, aux); SB(); asm volatile("s_nop 1"); SB(); } while (0)
#define PINV(x) asm volatile("" : "+v"(x))
#define RLX_AGENT __ATOMIC_RELAXED, __HIP_MEMORY_SCOPE_AGENT

constexpr int S_ = 2048, D_ = 576, FH_ = 2304;
#define XDELAY1 18
#define XDELAY3 20
#define LGATE 12
#define LGATE2 14
#define POLLD 40
#define STAG 3
#define HPRIO 3
#define FDELAY 45
#define STAG2 60
#define MAP1 1
#define MAP3 1
#define CODETOUCH 1
#define WSPTOUCH 0
#define LSH 0
#define L2PF 0
#define ZSPLIT 10
#define ZGATE 8
#define QLATE 0
#define BADV 3072
#define AFAKE 0
#define BAUX 16
#define HDELAY 10
#define MAP5 1
#define WGATE 0
#define WGI 7
#define WEARLY 0
#define ODELAY 12
#define K0NT 1
#define NTL(p) __builtin_nontemporal_load(p)
#define TS3 2
#define TG0 6
#define TG1 6
#define HPW 0
#define HPS 4
#define K0AUX 0
#define RTD 0
#define RTAUX 16
#define CUT 2
#define XRD 12
#define NST 4
#define WOG 0
constexpr u32 SENT = 0xFFFFFFFFu;
constexpr u32 SPIN_CAP = 1u << 16;

constexpr u32 WS_WQ = 0;
constexpr u32 WS_CSQ = WS_WQ + 16 * 129024;
constexpr u32 WS_WO = WS_CSQ + 18 * 8192;
constexpr u32 WS_WIN = WS_WO + 8 * 18 * 4608;
constexpr u32 WS_CSIN = WS_WIN + 288 * 18 * 1024;
constexpr u32 WS_WOUT = WS_CSIN + 3 * 4608 * 4;
constexpr u32 WS_X = WS_WOUT + 8 * 72 * 4608;
constexpr u32 XB = 128 * 18 * 1024, XSB = 2048 * 64, ZB = 144 * 64 * 1024;
constexpr u32 WS_XS = WS_X + 4 * XB;
constexpr u32 WS_O = WS_XS + 4 * XSB;
constexpr u32 WS_Z = WS_O + (1 + LSH) * XB;
constexpr u32 WS_H = WS_Z + (1 + LSH) * ZB;
constexpr u32 WS_KVP = WS_H + (1 + LSH) * ZB;
constexpr u32 WS_ZST = WS_KVP + 2 * 256 * 2816;
constexpr u32 WS_CNT = WS_ZST + 2 * 256 * 1024;
constexpr u32 WS_CNO = WS_CNT + 2 * 16 * 32 * 128;
constexpr u32 WS_CNX = WS_CNO + 2 * 16 * 16 * 128;
constexpr u32 WS_END = WS_CNX + 4 * 16 * 16 * 128;
constexpr u32 WS_DBG = WS_END;
#define WSTOP 0
#define WS_BASE(n) (WSTOP ? (((n) - WS_END - 0x100000) & ~(size_t)0xfffff) : 0)

#define STAMP_DECL
#define STAMP(k, i) do { } while (0)
#define HST(i) do { } while (0)
#define HST_DECL
#define HST_OUT(k) do { } while (0)
#define STAMPV(k, i, v) do { } while (0)
#define STAMP_OUT(k) do { } while (0)

__device__ __forceinline__ u32 pk2(float lo, float hi) {
    f32x2 v = {lo, hi};
    bf2_t b = __builtin_convertvector(v, bf2_t);
    return __builtin_bit_cast(u32, b);
}
__device__ __forceinline__ float bflo(u32 w) { return U2F(w << 16); }
__device__ __forceinline__ float bfhi(u32 w) { return U2F(w & 0xffff0000u); }
__device__ __forceinline__ bf16x8 mk8(u32 a, u32 b, u32 c, u32 d) { u32x4 v = {a, b, c, d}; return __builtin_bit_cast(bf16x8, v); }
__device__ __forceinline__ float sfma(float a, float b, float c) { float r; asm("v_fma_f32 %0, %1, %2, %3" : "=v"(r) : "v"(a), "v"(b), "v"(c)); return r; }
__device__ __forceinline__ f32x4 sadd4(f32x4 a, f32x4 b) {
    float r0 = a[0], r1 = a[1], r2 = a[2], r3 = a[3];
    asm("v_add_f32 %0, %0, %4\n\tv_add_f32 %1, %1, %5\n\tv_add_f32 %2, %2, %6\n\tv_add_f32 %3, %3, %7" : "+v"(r0), "+v"(r1), "+v"(r2), "+v"(r3) : "v"(b[0]), "v"(b[1]), "v"(b[2]), "v"(b[3]));
    f32x4 r = {r0, r1, r2, r3};
    return r;
}
__device__ __forceinline__ f32x4 phi4(f32x4 t) {
    const f32x4 u = t * 1.44269504089f;
    f32x4 e, m;
#pragma unroll
    for (int r = 0; r < 4; ++r) { e[r] = __builtin_amdgcn_exp2f(__builtin_amdgcn_fmed3f(u[r], 0.f, -__builtin_inff())); m[r] = __builtin_amdgcn_fmed3f(t[r], 0.f, __builtin_inff()); }
    return (m + e) + 1e-6f;
}
__device__ __forceinline__ int lane_id() { int l; asm volatile("v_mbcnt_lo_u32_b32 %0, -1, 0\n\tv_mbcnt_hi_u32_b32 %0, -1, %0" : "=v"(l)); __builtin_assume(l >= 0 && l < 64); return l; }
#define LANES() const int lane = lane_id(), c = lane & 15, g = lane >> 4; (void)c; (void)g
__device__ __forceinline__ u32 ldsw(u32 a) { return *(volatile LAS u32*)(size_t)a; }
__device__ __forceinline__ void stsw(u32 a, u32 v) { *(volatile LAS u32*)(size_t)a = v; }
#define LDSR(addr, off) (*(LAS bf16x8*)((LAS char*)(size_t)(addr) + (off)))
__device__ __forceinline__ void lds_wait(u32 a, u32 target) {
    FENCE();
#pragma nounroll
    for (u32 n = 0; n < SPIN_CAP; ++n) { const u32 v = ldsw(a); if (!ANY(v < target)) break; }
    FENCE();
}
__device__ __forceinline__ void lds_wait_gt(u32 a, u32 low) {
    FENCE();
#pragma nounroll
    for (u32 n = 0; n < SPIN_CAP; ++n) { const u32 v = ldsw(a); if (!ANY(v <= low)) break; }
    FENCE();
}
__device__ __forceinline__ void lds_wait_sleep(u32 a, u32 target) {
    FENCE();
#pragma nounroll
    for (u32 n = 0; n < SPIN_CAP; ++n) { const u32 v = ldsw(a); if (!ANY(v < target)) break; __builtin_amdgcn_s_sleep(1); }
    FENCE();
}
#define DMA4(v0, v1, v2, v3, sbase, m0v)                                                                            \
    asm volatile("s_mov_b32 m0, %5\n\ts_nop 0\n\tglobal_load_lds_dwordx4 %0, %4\n\t"                                \
                 "global_load_lds_dwordx4 %1, %4 offset:1024\n\tglobal_load_lds_dwordx4 %2, %4 offset:2048\n\t"     \
                 "global_load_lds_dwordx4 %3, %4 offset:3072" ::"v"(v0), "v"(v1), "v"(v2), "v"(v3), "s"(sbase), "s"(m0v) : "memory")
#define DMA2(v0, v1, sbase, m0v)                                                                                    \
    asm volatile("s_mov_b32 m0, %3\n\ts_nop 0\n\tglobal_load_lds_dwordx4 %0, %2\n\t"                                \
                 "global_load_lds_dwordx4 %1, %2 offset:1024" ::"v"(v0), "v"(v1), "s"(sbase), "s"(m0v) : "memory")

template <int SLP>
__device__ __forceinline__ void touch_paced(const char* p, int n, u32 vo) {
    u32x4 sink = {0u, 0u, 0u, 0u};
#pragma nounroll
    for (int i = 0; i < n; ++i) {
        asm volatile("global_load_dwordx4 %0, %1, %2" : "+v"(sink) : "v"(vo), "s"(p + i * 1024));
        __builtin_amdgcn_s_sleep(SLP);
    }
    WAIT_V(0);
    PINV(sink);
}

__device__ __forceinline__ u32x4 tcol(const u32x2 (&ld)[8], int i) {
    const u32 sel = (i & 1) ? 0x07060302u : 0x05040100u;
    u32x4 o;
    o.x = __builtin_amdgcn_perm(ld[1][i >> 1], ld[0][i >> 1], sel);
    o.y = __builtin_amdgcn_perm(ld[3][i >> 1], ld[2][i >> 1], sel);
    o.z = __builtin_amdgcn_perm(ld[5][i >> 1], ld[4][i >> 1], sel);
    o.w = __builtin_amdgcn_perm(ld[7][i >> 1], ld[6][i >> 1], sel);
    return o;
}
__device__ __forceinline__ float sum8(u32x4 o) {
    return ((bflo(o.x) + bfhi(o.x)) + (bflo(o.y) + bfhi(o.y))) + ((bflo(o.z) + bfhi(o.z)) + (bflo(o.w) + bfhi(o.w)));
}

constexpr int K0_QKV = 72, K0_X = 64, K0_WIN = 0, K0_WO = 90, K0_GRID = 256;
static_assert((WS_END - WS_KVP) / 16 <= K0_GRID * 576, "k0 grid too small for the hand-off words");
#define K0ST(off, val) __builtin_amdgcn_raw_buffer_store_b128(val, rs0, off, 0, K0AUX)
__device__ __forceinline__ void k0_body(const u16* __restrict__ x, const u16* __restrict__ wqkv, const u16* __restrict__ wo,
                                        const u16* __restrict__ wout, char* __restrict__ ws) {
    __shared__ __attribute__((aligned(16))) char img[36864 + 576 * 8];
    const auto rs0 = __builtin_amdgcn_make_buffer_rsrc(ws, 0, WS_END, 0x00020000);
    const int tid = threadIdx.x, w = __builtin_amdgcn_readfirstlane(tid >> 6), lane = tid & 63, c = lane & 15, g = lane >> 4;
    int b = blockIdx.x;
    if (b < K0_QKV) {
        const int t = (b >> 3) % 3, kt = (b & 7) + 8 * (b / 24);
        if (kt >= 18) return;
        u32x2 ld[8];
#pragma unroll
        for (int j = 0; j < 8; ++j) ld[j] = NTL((const u32x2*)(wqkv + (size_t)(32 * kt + 8 * g + j) * 1728 + 576 * t + 64 * w + 4 * c));
        float* cst = (float*)(img + 36864);
#pragma unroll
        for (int i = 0; i < 4; ++i) {
            const int col = 64 * w + 4 * c + i, h = col / 18, d = col - 18 * h;
            const u32x4 o = tcol(ld, i);
            *(u32x4*)(img + (d < 16 ? h * 1024 + (d + 16 * g) * 16 : 32768 + (h >> 1) * 256 + g * 64 + (2 * (h & 1) + d - 16) * 16)) = o;
            float cs = sum8(o);
            cs += __shfl_xor(cs, 16); cs += __shfl_xor(cs, 32);
            if (g == 0) cst[(h >> 1) * 36 + (d < 16 ? (h & 1) * 16 + d : 32 + 2 * (h & 1) + d - 16)] = cs;
        }
        __syncthreads();
        const int rc = t == 1 ? 0 : (t == 2 ? 4 : 8);
        if (w < 8) {
#pragma unroll
            for (int k = 0; k < 4; ++k) {
                const int h = 4 * w + k;
                const u32 tile = t == 0 ? 92160 + (kt * 2 + (h & 1)) * 1024 : (kt * 5 + 2 * (t - 1) + (h & 1)) * 1024;
                K0ST(WS_WQ + (h >> 1) * 129024 + tile + lane * 16, *(const u32x4*)(img + h * 1024 + lane * 16));
            }
        } else {
#pragma unroll
            for (int k = 0; k < 4; ++k) {
                char* dst = ws + WS_WQ + (4 * k + g) * 129024 + (kt * 5 + 4) * 1024 + (c >> 2) * 256 + (rc + (c & 3)) * 16;
                *(u32x4*)dst = *(const u32x4*)(img + 32768 + k * 1024 + lane * 16);
                if (t == 0) *(u32x4*)(dst + 64) = (u32x4){0u, 0u, 0u, 0u};
            }
        }
        {
            const int p = tid / 36, e = tid - 36 * p;
            float* cq = (float*)(ws + WS_CSQ) + (kt * 16 + p) * 128;
            cq[e < 32 ? (t == 0 ? 80 : 32 * (t - 1)) + e : 64 + rc + e - 32] = cst[tid];
            if (t == 0 && e >= 32) cq[76 + e - 32] = 0.f;
        }
        return;
    }
    b -= K0_QKV;
    if (b < K0_X) {
        const int rl = tid / 18, t18 = tid - 18 * rl;
        u32x4 v[4];
#pragma unroll
        for (int i = 0; i < 4; ++i) v[i] = NTL((const u32x4*)(x + (size_t)(32 * b + rl) * 576 + (18 * i + t18) * 8));
        float s1 = 0.f, s2 = 0.f;
#pragma unroll
        for (int i = 0; i < 4; ++i) {
            const int ch = 18 * i + t18;
            *(u32x4*)(img + ((rl >> 4) * 18 + (ch >> 2)) * 1024 + ((rl & 15) + 16 * (ch & 3)) * 16) = v[i];
#pragma unroll
            for (int k = 0; k < 4; ++k) { const float a = bflo(v[i][k]), bb = bfhi(v[i][k]); s1 += a + bb; s2 += a * a + bb * bb; }
        }
        f32x2 o = {s1, s2};
        ((f32x2*)(img + 36864))[tid] = o;
        __syncthreads();
#pragma unroll
        for (int t = 0; t < 4; ++t) K0ST(WS_X + b * 36864 + (w * 4 + t) * 1024 + lane * 16, *(const u32x4*)(img + (w * 4 + t) * 1024 + lane * 16));
        if (tid < 256) {
            const int r = tid & 31, q = tid >> 5;
            const f32x2* pp = (const f32x2*)(img + 36864) + r * 18;
            o = pp[2 * q] + pp[2 * q + 1];
            if (q < 2) o += pp[16 + q];
            *(f32x2*)(ws + WS_XS + (q * 2048 + 32 * b + r) * 8) = o;
        }
        return;
    }
    b -= K0_X;
    if (b < K0_WO) {
        const bool iso = b < 18;
        const int sl = iso ? b : b - 18, G = sl / 9, t = sl - 9 * G;
        const u16* src = iso ? wo : wout;
        const int col = 64 * w + 4 * c, nt = col / 72, cl = col - 72 * nt;
        const int r0 = iso ? (sl < 16 ? 36 * sl : 288 * (sl - 16) + 16) : (t < 8 ? 288 * G + 144 * (t >> 2) + 32 * (t & 3) : 288 * G + 128);
        const int gs = (iso && sl >= 16) ? 72 : 4, s4 = iso ? (sl < 16 ? 18 : 36) : (t < 8 ? 16 : 144), s2 = (iso && sl >= 16) ? 18 : 2;
        u32x2 ld[8];
#pragma unroll
        for (int j = 0; j < 8; ++j) ld[j] = NTL((const u32x2*)(src + (size_t)(r0 + gs * g + s4 * (j >> 2) + s2 * ((j >> 1) & 1) + (j & 1)) * 576 + col));
        const int n = cl < 64 ? 2 * (cl >> 5) + ((cl >> 2) & 1) : 4, ii = cl < 64 ? 4 * ((cl >> 3) & 3) : cl - 64;
#pragma unroll
        for (int i = 0; i < 4; ++i) {
            const u32 off = n < 4 ? n * 1024 + (ii + i + 16 * g) * 16 : 4096 + g * 128 + (ii + i) * 16;
            *(u32x4*)(img + nt * 4608 + off) = tcol(ld, i);
        }
        __syncthreads();
#pragma unroll
        for (int k = 0; k < 4; ++k) {
            const u32 i = (w * 4 + k) * 1024 + lane * 16, n2 = i / 4608, off = i - 4608 * n2;
            K0ST((iso ? WS_WO + (n2 * 18 + sl) * 4608 : WS_WOUT + (n2 * 72 + sl) * 4608) + off, *(const u32x4*)(img + i));
        }
        return;
    }
}

__global__ __launch_bounds__(576) void k0_prep(const u16* __restrict__ x, const u16* __restrict__ wqkv, const u16* __restrict__ wo,
                                               const u16* __restrict__ wout, char* __restrict__ ws) {
    STAMP_DECL;
    STAMP(8, 0);
    const u32 gid = blockIdx.x * 576 + threadIdx.x;
    u32 warm = 0;
    if (blockIdx.x >= 240) {
        u64 pc; int dist;
        asm volatile("s_getpc_b64 %0\n\ts_mov_b32 %1, k9_end@rel32@lo+4" : "=&s"(pc), "=&s"(dist));
        if ((int)threadIdx.x * 128 < dist) warm |= *(const GAS u32*)(pc + threadIdx.x * 128);
    }
    if (gid < (WS_END - WS_KVP) / 16) {
        const auto rs = __builtin_amdgcn_make_buffer_rsrc(ws, 0, WS_END, 0x00020000);
        const u32 v = gid < (WS_CNT - WS_KVP) / 16 ? SENT : 0u;
        const u32x4 o = {v, v, v, v};
        BST128(o, rs, gid * 16, WS_KVP, 16);
    }
    k0_body(x, wqkv, wo, wout, ws);
    asm volatile("" :: "v"(warm));
    STAMP(8, 1);
    STAMP_OUT(11);
}

constexpr u32 LDS_FL = 158080, LDS_TOTAL = LDS_FL + 640;
typedef __amdgpu_buffer_rsrc_t rsrc_t;

__device__ __forceinline__ void x_ready_stats(char* ws, rsrc_t rs, u32 FL, bool fresh, int ver, int pm, int j, int lane, f32x2& mean, f32x2& var) {
    if (fresh) {
        lds_wait_sleep(FL + 120, 4u);
        __builtin_amdgcn_s_sleep(XRD);
        GAS u32* xm = (GAS u32*)(ws + WS_CNX + ((ver * 16 + pm) * 16 + j) * 128);
#pragma nounroll
        for (u32 n = 0; n < SPIN_CAP; ++n) { if (__builtin_amdgcn_readfirstlane(__hip_atomic_load(xm, RLX_AGENT)) == 0xffffu) break; }
        FENCE();
        stsw(FL + 124, 1u);
        FENCE();
    }
    f32x4 v[8];
#pragma unroll
    for (int k = 0; k < 8; ++k) v[k] = __builtin_bit_cast(f32x4, __builtin_amdgcn_raw_buffer_load_b128(rs, lane * 16, WS_XS + ver * XSB + (k * 2048 + 128 * pm) * 8, 16));
    const f32x4 sm = sadd4(sadd4(sadd4(v[0], v[1]), sadd4(v[2], v[3])), sadd4(sadd4(v[4], v[5]), sadd4(v[6], v[7])));
#pragma unroll
    for (int k = 0; k < 2; ++k) {
        mean[k] = sfma(sm[2 * k], 1.f / 576.f, 0.f);
        var[k] = fmaxf(sfma(-mean[k], mean[k], sfma(sm[2 * k + 1], 1.f / 576.f, 0.f)), 0.f) + 1e-5f;
    }
}

__device__ __forceinline__ void proj_store(const f32x4 (&y)[5], char* ws, rsrc_t rs, u16* out, u32 FLN, bool hand, int ver, int mt, int nt, int wid) {
    const int lane = lane_id();
    const u32 vo = lane * 16;
    const int c = lane & 15, g = lane >> 4;
    u32x4 o[2]; u32x2 o2;
#pragma unroll
    for (int i = 0; i < 2; ++i) o[i] = (u32x4){pk2(y[2 * i][0], y[2 * i][1]), pk2(y[2 * i][2], y[2 * i][3]), pk2(y[2 * i + 1][0], y[2 * i + 1][1]), pk2(y[2 * i + 1][2], y[2 * i + 1][3])};
    o2 = (u32x2){pk2(y[4][0], y[4][1]), pk2(y[4][2], y[4][3])};
    if (ver == 4) {
        u16* op = out + (size_t)(64 * mt + 16 * wid + c) * 576 + 72 * nt;
        *(u32x4*)(op + 8 * g) = o[0];
        *(u32x4*)(op + 32 + 8 * g) = o[1];
        if (g < 2) *(u32x2*)(op + 64 + 4 * g) = o2;
    } else {
        const u32 xo = WS_X + ver * XB + (4 * mt + wid) * 18432 + 2304 * nt;
        BST128(o[0], rs, vo, xo, 16);
        BST128(o[1], rs, vo + 1024, xo, 16);
        if (g < 2) __builtin_amdgcn_raw_buffer_store_b64(o2, rs, 2048 + c * 16 + g * 8, xo, 16); else o2 = (u32x2){0u, 0u};
        float s1 = 0.f, s2 = 0.f;
#pragma unroll
        for (int k = 0; k < 10; ++k) { const u32 wv = k < 8 ? o[k >> 2][k & 3] : o2[k & 1]; const float u = bflo(wv), v = bfhi(wv); s1 += u + v; s2 += u * u + v * v; }
        auto r1 = __builtin_amdgcn_permlane32_swap(F2U(s1), F2U(s2), false, false);
        const float t = U2F(r1[0]) + U2F(r1[1]);
        auto r2 = __builtin_amdgcn_permlane16_swap(F2U(t), F2U(t), false, false);
        const float tt = U2F(r2[0]) + U2F(r2[1]);
        if ((g & 1) == 0) __builtin_amdgcn_raw_buffer_store_b32(F2U(tt), rs, c * 8 + (g >> 1) * 4, WS_XS + ver * XSB + (nt * 2048 + 64 * mt + 16 * wid) * 8, 16);
        if (hand) {
            WAIT_V(0);
            u32 old = 0;
            if (lane == 0) old = __hip_atomic_fetch_add((LAS u32*)(size_t)(FLN + 120), 1u, __ATOMIC_RELAXED, __HIP_MEMORY_SCOPE_WORKGROUP);
            if (__builtin_amdgcn_readfirstlane(old) == 3 && lane < 16)
                __hip_atomic_fetch_add((GAS u32*)(ws + WS_CNX + ((ver * 16 + (mt >> 1)) * 16 + lane) * 128), 1u << (8 * (mt & 1) + nt), RLX_AGENT);
        }
    }
}

__device__ __forceinline__ void proj_tail(f32x4 (&ac)[4][5], const u32x4 (&res)[2], u32x2 res2, u32 lds, u32 wstride, u32 flags, char* ws, rsrc_t rs, u16* out,
                                          u32 FLN, bool hand, int ver, int mt, int nt, int wid) {
    const int lane = lane_id();
    const u32 vo = lane * 16;
#pragma unroll
    for (int m = 1; m < 4; ++m)
#pragma unroll
        for (int n = 0; n < 5; ++n) *(LAS f32x4*)(size_t)(lds + wid * wstride + vo + ((m - 1) * 5 + n) * 1024) = ac[m][n];
    FENCE();
    stsw(flags + wid * 4, 1u);
#pragma unroll
    for (int i = 0; i < 4; ++i) {
        const u32 w0 = res[i >> 1][2 * (i & 1)], w1 = res[i >> 1][2 * (i & 1) + 1];
        ac[0][i] += (f32x4){bflo(w0), bfhi(w0), bflo(w1), bfhi(w1)};
        PINV(ac[0][i]);
    }
    ac[0][4] += (f32x4){bflo(res2[0]), bfhi(res2[0]), bflo(res2[1]), bfhi(res2[1])};
    PINV(ac[0][4]);
    lds_wait(flags + (lane & 3) * 4, 1u);
#pragma unroll
    for (int n = 0; n < 5; ++n)
        ac[0][n] = (ac[0][n] + *(LAS f32x4*)(size_t)(lds + (wid ^ 1) * wstride + vo + n * 1024)) +
                   (*(LAS f32x4*)(size_t)(lds + (wid ^ 2) * wstride + vo + (5 + n) * 1024) + *(LAS f32x4*)(size_t)(lds + (wid ^ 3) * wstride + vo + (10 + n) * 1024));
    proj_store(ac[0], ws, rs, out, FLN, hand, ver, mt, nt, wid);
}

constexpr u32 K1_PW = 129024, K1_OP = K1_PW + 4 * 176 * 16, K1_ST = K1_OP + 1536, K1_CS = K1_ST + 1024, K1_RT = K1_CS + 512;

__device__ __forceinline__ void phase_a(char* ws, const u16* win, u32 lds, int wid, int layer, bool fresh, bool hand) {
    int bid = blockIdx.x;
    GAS char* wsg = (GAS char*)ws;
    asm volatile("" : "+s"(bid), "+s"(wsg), "+s"(wid));
    ws = (char*)wsg;
    const int xcd = bid & 7, bi = bid >> 3;
    const int pm = MAP1 ? (xcd & 3) * 4 + (bi & 3) : (xcd & 1) * 8 + (bi & 7), p = MAP1 ? (xcd >> 2) * 8 + (bi >> 2) : (xcd >> 1) * 4 + (bi >> 3);
    const u32 FL = lds + LDS_FL + 256 * layer;
    const auto rs = __builtin_amdgcn_make_buffer_rsrc(ws, 0, WS_END, 0x00020000);
    STAMP_DECL;
    if (wid == 4) {
        LANES();
        const char* src = ws + WS_WQ + p * 129024;
        const u32 vo = lane * 16;
#define K1_U10(u) do { DMA4(vo, vo, vo, vo, src + (u) * 10240, lds + (u) * 10240); DMA4(vo, vo, vo, vo, src + (u) * 10240 + 4096, lds + (u) * 10240 + 4096); \
                       DMA2(vo, vo, src + (u) * 10240 + 8192, lds + (u) * 10240 + 8192); } while (0)
#define K1_U12(u) do { DMA4(vo, vo, vo, vo, src + 92160 + (u) * 12288, lds + 92160 + (u) * 12288); DMA4(vo, vo, vo, vo, src + 92160 + (u) * 12288 + 4096, lds + 92160 + (u) * 12288 + 4096); \
                       DMA4(vo, vo, vo, vo, src + 92160 + (u) * 12288 + 8192, lds + 92160 + (u) * 12288 + 8192); } while (0)
        if (fresh) lds_wait_sleep(FL + 120, 4u);
        K1_U10(0);
        if (!fresh) BAR();
        K1_U10(1); K1_U10(2);
#pragma unroll
        for (int u = 3; u < 9; ++u) { WAIT_V(20); stsw(FL, u - 2); FENCE(); K1_U10(u); }
        WAIT_V(20); stsw(FL, 7); FENCE(); K1_U12(0);
        WAIT_V(22); stsw(FL, 8); FENCE(); K1_U12(1);
        WAIT_V(24); stsw(FL, 9); FENCE(); K1_U12(2);
        WAIT_V(24); stsw(FL, 10); FENCE();
        WAIT_V(12); stsw(FL, 11); FENCE();
        WAIT_V(0); stsw(FL, 12);
        const bool rt = layer == 0 && bid < 216;
        const int cb = bid / 3, k0 = 6 * (bid - 3 * cb);
        u32x2 ld[6][8];
        if (rt) {
#pragma unroll
            for (int i = 0; i < 6; ++i)
#pragma unroll
                for (int j = 0; j < 8; ++j) ld[i][j] = NTL((const u32x2*)(win + (size_t)(32 * (k0 + i) + 8 * g + j) * 4608 + 64 * cb + 4 * c));
            SB();
        }
        lds_wait_sleep(FL + 16 + (lane & 3) * 4, 2u);
        if (WOG && fresh) lds_wait_sleep(FL + 32 + (lane % 3) * 4, 1u);
        src = ws + WS_WO + (p & 7) * 82944;
        WAIT_V(12);
#pragma unroll
        for (int u = 0; u < 8; ++u) { K1_U10(u); if (u >= 4) WAIT_V(40); }
        asm volatile("s_mov_b32 m0, %2\n\ts_nop 0\n\tglobal_load_lds_dwordx4 %0, %1" ::"v"(vo), "s"(src + 81920), "s"(lds + 81920) : "memory");
        WAIT_V(0); stsw(FL, 13);
        __builtin_amdgcn_s_sleep(RTD);
        float cs[4] = {0.f, 0.f, 0.f, 0.f};
        u32x4 rb[6][4];
        if (rt) {
            const bf2_t one2 = __builtin_bit_cast(bf2_t, 0x3f803f80u);
#pragma unroll
            for (int i = 0; i < 6; ++i) {
#pragma unroll
                for (int e = 0; e < 4; ++e) {
                    const u32x4 o = tcol(ld[i], e);
                    *(LAS u32x4*)(size_t)(lds + K1_RT + (c >> 2) * 1024 + (4 * (c & 3) + e + 16 * g) * 16) = o;
#pragma unroll
                    for (int k = 0; k < 4; ++k) { const u32 wv = o[k]; cs[e] = __builtin_amdgcn_fdot2_f32_bf16(__builtin_bit_cast(bf2_t, wv), one2, cs[e], false); }
                }
                FENCE();
#pragma unroll
                for (int t = 0; t < 4; ++t) rb[i][t] = *(LAS u32x4*)(size_t)(lds + K1_RT + t * 1024 + vo);
                FENCE();
            }
#pragma unroll
            for (int i = 0; i < 24; ++i) PINV(rb[i >> 2][i & 3]);
        }
        if (rt) {
#pragma unroll
            for (int i = 0; i < 6; ++i)
#pragma unroll
                for (int t = 0; t < 4; ++t) BST128(rb[i][t], rs, vo, WS_WIN + ((4 * cb + t) * 18 + k0 + i) * 1024, RTAUX);
#pragma unroll
            for (int e = 0; e < 4; ++e) {
                auto r1 = __builtin_amdgcn_permlane32_swap(F2U(cs[e]), F2U(cs[e]), false, false);
                const float t1 = sfma(U2F(r1[0]), 1.f, U2F(r1[1]));
                float t2 = t1;
                PINV(t2);
                asm volatile("s_nop 1");
                auto r2 = __builtin_amdgcn_permlane16_swap(F2U(t2), F2U(t2), false, false);
                cs[e] = sfma(U2F(r2[0]), 1.f, U2F(r2[1]));
            }
            if (g == 0) *(f32x4*)(ws + WS_CSIN + ((k0 / 6) * 4608 + 64 * cb + 4 * c) * 4) = (f32x4){cs[0], cs[1], cs[2], cs[3]};
        }
        return;
    }
    if (wid > 4) {
        LANES();
        const int ht = (wid - 5) * 64 + lane;
        if (!fresh) BAR();
        if (wid == 5) {
            f32x2 mean, var, r, mr;
            x_ready_stats(ws, rs, FL, fresh, 2 * layer, pm, p, lane, mean, var);
#pragma unroll
            for (int k = 0; k < 2; ++k) { r[k] = __builtin_amdgcn_rsqf(var[k]); float t2 = -mean[k] * r[k]; PINV(t2); mr[k] = t2; }
            *(LAS f32x2*)(size_t)(lds + K1_ST + lane * 8) = r;
            *(LAS f32x2*)(size_t)(lds + K1_ST + 512 + lane * 8) = mr;
        } else if (wid == 7) {
            const f32x2* cp = (const f32x2*)(ws + WS_CSQ) + p * 64 + min(lane, 55);
            f32x2 q[18];
#pragma unroll
            for (int k = 0; k < 18; ++k) q[k] = cp[k * 1024];
            float q0 = q[0][0], q1 = q[0][1];
#pragma unroll
            for (int k = 1; k < 18; ++k) { q0 = sfma(q[k][0], 1.f, q0); q1 = sfma(q[k][1], 1.f, q1); }
            if (fresh) lds_wait_sleep(FL + 120, 4u);
            *(LAS f32x2*)(size_t)(lds + K1_CS + lane * 8) = (f32x2){q0, q1};
        }
        FENCE();
        stsw(FL + 4 + (wid - 5) * 4, 1u);
        __builtin_amdgcn_s_setprio(3);
        lds_wait_sleep(FL + 16 + (lane & 3) * 4, 1u);
        STAMP(layer * 4 + 0, 7);
        const int it = ht < 173 ? ht : 172;
        f32x4 v = *(LAS f32x4*)(size_t)(lds + K1_PW + it * 16);
#pragma unroll
        for (int k = 1; k < 4; ++k) v = sadd4(v, *(LAS f32x4*)(size_t)(lds + K1_PW + k * 2816 + it * 16));
        const u32 gbase = WS_KVP + (layer * 16 + p) * 16 * 2816;
        {
            u32x4 o = __builtin_bit_cast(u32x4, v);
#pragma unroll
            for (int k = 0; k < 4; ++k) o[k] = o[k] == SENT ? 0x7fc00000u : o[k];
            if (ht < 173) BST128(o, rs, it * 16, gbase + pm * 2816, 16);
        }
        u32x4 t[16];
        STAMP(layer * 4 + 0, 8); STAMP(layer * 4 + 0, 9);
        __builtin_amdgcn_s_sleep(XDELAY1);
#pragma unroll
        for (int k = 0; k < 16; ++k) t[k] = __builtin_amdgcn_raw_buffer_load_b128(rs, it * 16, gbase + k * 2816, 16);
        u32 mx = 0, spins = 0;
#pragma unroll
        for (int k = 0; k < 16; ++k) mx = max(max(mx, max(t[k][0], t[k][1])), max(t[k][2], t[k][3]));
        if (__builtin_expect(ANY(mx == SENT), 0)) {
#pragma nounroll
            for (spins = 1; spins < SPIN_CAP; ++spins) {
                FENCE();
                u32 miss = 0;
#pragma unroll
                for (int k = 0; k < 16; ++k) miss |= ANY(max(max(t[k][0], t[k][1]), max(t[k][2], t[k][3])) == SENT) ? 1u << k : 0u;
                if (!miss) break;
#pragma unroll
                for (int k = 0; k < 16; ++k) if (miss >> k & 1) t[k] = __builtin_amdgcn_raw_buffer_load_b128(rs, it * 16, gbase + k * 2816, 16);
            }
        }
        STAMP(layer * 4 + 0, 10); STAMPV(layer * 4 + 0, 15, spins);
        v = __builtin_bit_cast(f32x4, t[0]);
#pragma unroll
        for (int k = 1; k < 16; ++k) v = sadd4(v, __builtin_bit_cast(f32x4, t[k]));
        u32x2 lh = {pk2(v[0], v[1]), pk2(v[2], v[3])};
        if (ht >= 173) lh = (u32x2){0u, 0u};
        *(LAS u32x2*)(size_t)(lds + K1_OP + ht * 8) = lh;
        FENCE();
        stsw(FL + 32 + (wid - 5) * 4, 1u);
        __builtin_amdgcn_s_setprio(0);
        if (wid != 5) return;
        STAMP_OUT(layer * 4 + 0);
        lds_wait_sleep(FL + 48, 4u);
        __builtin_amdgcn_s_sleep(ODELAY);
        GAS u32* om = (GAS u32*)(ws + WS_CNO + ((layer * 16 + pm) * 16 + p) * 128);
#pragma nounroll
        for (u32 n = 0; n < SPIN_CAP; ++n) { if (__builtin_amdgcn_readfirstlane(__hip_atomic_load(om, RLX_AGENT)) == 0xffffu) break; }
        FENCE();
        stsw(FL + 52, 1u);
        return;
    }
    LANES();
    STAMP(layer * 4 + 0, 0);

    const u32 xb = WS_X + 2 * layer * XB + (8 * pm + 2 * wid) * 18432, vo = lane * 16;
    bf16x8 xa[2][18];
#define K1_LX(m, kt) xa[m][kt] = __builtin_bit_cast(bf16x8, __builtin_amdgcn_raw_buffer_load_b128(rs, vo, xb + ((m) * 18 + (kt)) * 1024, 16))
    if (fresh) lds_wait(FL + 124, 1u);
#pragma unroll
    for (int kt = 0; kt < 6; ++kt) { K1_LX(0, kt); SB(); K1_LX(1, kt); SB(); }
    f32x4 acc[2][5];
#pragma unroll
    for (int f = 0; f < 5; ++f) { acc[0][f] = acc[1][f] = (f32x4){0.f, 0.f, 0.f, 0.f}; PINV(acc[0][f]); PINV(acc[1][f]); }
    u32 wb0 = lds + lane * 16, wb1 = wb0 + 61440, wb2 = wb0 + 92160;
    PINV(wb0); PINV(wb1); PINV(wb2);
#define K1_W1(kt, f) ((kt) < 12 ? LDSR(wb0, ((kt) * 5 + (f)) * 1024) : LDSR(wb1, (((kt) - 12) * 5 + (f)) * 1024))
    if (!fresh) BAR();
    lds_wait(FL, 1u);
    STAMP(layer * 4 + 0, 1);
    bf16x8 wf[2][5];
#pragma unroll
    for (int f = 0; f < 5; ++f) { wf[0][f] = K1_W1(0, f); SB(); }
#pragma unroll
    for (int f = 0; f < 5; ++f) { wf[1][f] = K1_W1(1, f); SB(); }
    u32 fl = 0;
#pragma unroll
    for (int kt = 0; kt < 18; ++kt) {
        if ((kt & 1) == 0 && kt + 2 < 18) { if (__builtin_expect(ANY(fl < (u32)(kt / 2 + 2)), 0)) lds_wait(FL, kt / 2 + 2); FENCE(); }
#pragma unroll
        for (int f = 0; f < 5; ++f) {
            MFMA(acc[0][f], xa[0][kt], wf[kt & 1][f]);
            if (f == 1 && kt + 6 < 18) K1_LX(0, kt + 6);
            if (f == 3 && kt + 6 < 18) K1_LX(1, kt + 6);
            if (f == 0 && (kt & 1) && kt < 16) { FENCE(); fl = ldsw(FL); FENCE(); }
            SB();
            MFMA(acc[1][f], xa[1][kt], wf[kt & 1][f]);
            if (kt + 2 < 18) wf[kt & 1][f] = K1_W1(kt + 2, f);
            SB();
        }
    }
    asm volatile("s_nop 15" ::: "memory");
    STAMP(layer * 4 + 0, 2);
    lds_wait(FL + 4 + (lane % 3) * 4, 1u);
    {
        u32 op[5][4], rk[4];
        float cs1[5];
#pragma unroll
        for (int f = 0; f < 5; ++f) cs1[f] = *(LAS float*)(size_t)(lds + K1_CS + f * 64 + c * 4);
#pragma unroll
        for (int m = 0; m < 2; ++m) {
            const f32x4 rr = *(LAS f32x4*)(size_t)(lds + K1_ST + (32 * wid + 16 * m + 4 * g) * 4);
            const f32x4 mm = *(LAS f32x4*)(size_t)(lds + K1_ST + 512 + (32 * wid + 16 * m + 4 * g) * 4);
#pragma unroll
            for (int f = 0; f < 5; ++f) {
                f32x4 v = rr * acc[m][f] + mm * cs1[f];
                if (f < 2) v = phi4(v);
                if (f == 4) { const f32x4 u = phi4(v); rk[2 * m] = pk2(u[0], u[1]); rk[2 * m + 1] = pk2(u[2], u[3]); }
                op[f][2 * m] = pk2(v[0], v[1]); op[f][2 * m + 1] = pk2(v[2], v[3]);
                if (f == 4 && c >= 12) { op[f][2 * m] = 0x3f803f80u; op[f][2 * m + 1] = 0x3f803f80u; }
            }
        }
        const bf16x8 KAa = mk8(op[0][0], op[0][1], op[0][2], op[0][3]), KAb = mk8(op[1][0], op[1][1], op[1][2], op[1][3]);
        const bf16x8 VBa = mk8(op[2][0], op[2][1], op[2][2], op[2][3]), VBb = mk8(op[3][0], op[3][1], op[3][2], op[3][3]);
        const bf16x8 RV = mk8(op[4][0], op[4][1], op[4][2], op[4][3]), RK = mk8(rk[0], rk[1], rk[2], rk[3]);
        const f32x4 z4 = {0.f, 0.f, 0.f, 0.f};
        const f32x4 T1a = MFMAB(KAa, VBa, z4), T1b = MFMAB(KAb, VBb, z4), T2a = MFMAB(KAa, RV, z4), T2b = MFMAB(KAb, RV, z4);
        const f32x4 T3a = MFMAB(RK, VBa, z4), T3b = MFMAB(RK, VBb, z4), T4 = MFMAB(RK, RV, z4);
        const u32 pw = lds + K1_PW + wid * 2816;
        *(LAS f32x4*)(size_t)(pw + lane * 16) = T1a;
        *(LAS f32x4*)(size_t)(pw + 1024 + lane * 16) = T1b;
        const f32x4 u3 = {T3a[0], T3a[1], T3b[2], T3b[3]};
        *(LAS f32x4*)(size_t)(pw + (g == 0 ? 128 + c : 175) * 16) = u3;
        *(LAS f32x4*)(size_t)(pw + (c == 12 ? 152 + g : ((c & 14) == 4 ? 144 + (c - 4) * 4 + g : 175)) * 16) = T2a;
        *(LAS f32x4*)(size_t)(pw + (c == 12 ? 164 + g : ((c & 14) == 6 ? 156 + (c - 6) * 4 + g : 175)) * 16) = T2b;
        *(LAS f32x4*)(size_t)(pw + (lane == 12 ? 172 : ((lane & 60) == 4 ? 164 + lane : 175)) * 16) = T4;
        FENCE();
        stsw(FL + 16 + wid * 4, 1u);
    }
    STAMP(layer * 4 + 0, 3);
    f32x4 qa[2][3];
#pragma unroll
    for (int f = 0; f < 3; ++f) { qa[0][f] = qa[1][f] = (f32x4){0.f, 0.f, 0.f, 0.f}; PINV(qa[0][f]); PINV(qa[1][f]); }
#define K1_W2(kt, f) ((f) < 2 ? LDSR(wb2, ((kt) * 2 + (f)) * 1024) : K1_W1(kt, 4))
    lds_wait(FL, 10u);
    bf16x8 wq[3][3];
#pragma unroll
    for (int k = 0; k < 3; ++k)
#pragma unroll
        for (int f = 0; f < 3; ++f) { wq[k][f] = K1_W2(k, f); SB(); }
#pragma unroll
    for (int kt = 0; kt < 18; ++kt) {
        if (kt == 3) lds_wait(FL, 11u);
        if (kt == 9) lds_wait(FL, 12u);
#pragma unroll
        for (int f = 0; f < 3; ++f) {
            MFMA(qa[0][f], wq[kt % 3][f], xa[0][kt]);
            SB();
            MFMA(qa[1][f], wq[kt % 3][f], xa[1][kt]);
            if (kt + 3 < 18) wq[kt % 3][f] = K1_W2(kt + 3, f);
            SB();
        }
    }
    asm volatile("s_nop 15" ::: "memory");
    STAMP(layer * 4 + 0, 4);
    stsw(FL + 16 + wid * 4, 2u);
    f32x4 cs2[3];
#pragma unroll
    for (int f = 0; f < 3; ++f) cs2[f] = *(LAS f32x4*)(size_t)(lds + K1_CS + (f < 2 ? 5 + f : 4) * 64 + g * 16);
    bf16x8 PQ[2][2];
#pragma unroll
    for (int m = 0; m < 2; ++m) {
        const float rr = *(LAS float*)(size_t)(lds + K1_ST + (32 * wid + 16 * m + c) * 4), mm = *(LAS float*)(size_t)(lds + K1_ST + 512 + (32 * wid + 16 * m + c) * 4);
        u32 w2[3][2];
#pragma unroll
        for (int f = 0; f < 3; ++f) {
            const f32x4 v = phi4(qa[m][f] * rr + cs2[f] * mm);
            w2[f][0] = pk2(v[0], v[1]); w2[f][1] = pk2(v[2], v[3]);
        }
        if (g != 2) { w2[2][0] = 0u; w2[2][1] = 0u; }
        PQ[m][0] = mk8(w2[0][0], w2[0][1], w2[2][0], w2[2][1]);
        PQ[m][1] = mk8(w2[1][0], w2[1][1], w2[2][0], w2[2][1]);
        PINV(PQ[m][0]); PINV(PQ[m][1]);
    }
    u32 ga[6];
    ga[0] = lane; ga[1] = g == 2 ? 128 + c : 175;
    ga[2] = c < 2 ? 144 + 4 * c + g : ((c & 3) == 2 ? 152 + g : 175);
    ga[3] = (c & 14) == 4 ? 156 + 4 * (c - 4) + g : ((c & 3) == 3 ? 164 + g : 175);
    ga[4] = g != 2 ? 175 : (c < 2 ? 168 + c : ((c & 3) == 2 ? 172 : 175));
    ga[5] = g != 2 ? 175 : ((c & 14) == 4 ? 166 + c : ((c & 3) == 3 ? 172 : 175));
#pragma unroll
    for (int i = 0; i < 6; ++i) { ga[i] = lds + K1_OP + ga[i] * 8; PINV(ga[i]); }
    const u32 ob = WS_O + LSH * layer * XB + (8 * pm + 2 * wid) * 18432;
    u32 orem = (16 + (p >> 3)) * 1024 + (c + 16 * ((p & 7) >> 1)) * 16 + (p & 1) * 8 + g * 4;
    PINV(orem);
    lds_wait(FL + 32 + (lane % 3) * 4, 1u);
    STAMP(layer * 4 + 0, 5);
    {
        const u32x2 l0 = *(LAS u32x2*)(size_t)ga[0], l1 = *(LAS u32x2*)(size_t)(ga[0] + 512), h01 = *(LAS u32x2*)(size_t)ga[1];
        const u32x2 l2 = *(LAS u32x2*)(size_t)ga[2], l3 = *(LAS u32x2*)(size_t)ga[3];
        const u32 h2 = *(LAS u32*)(size_t)ga[4], h3 = *(LAS u32*)(size_t)(ga[5] + 4);
        const bf16x8 A1a = mk8(l0[0], l0[1], h01[0], 0u), A1b = mk8(l1[0], l1[1], 0u, h01[1]), A2a = mk8(l2[0], l2[1], h2, 0u), A2b = mk8(l3[0], l3[1], 0u, h3);
        const f32x4 z4 = {0.f, 0.f, 0.f, 0.f};
        f32x4 O1a[2], O1b[2], O2[2];
#pragma unroll
        for (int m = 0; m < 2; ++m) O2[m] = MFMAB(A2a, PQ[m][0], z4);
#pragma unroll
        for (int m = 0; m < 2; ++m) O2[m] = MFMAB(A2b, PQ[m][1], O2[m]);
#pragma unroll
        for (int m = 0; m < 2; ++m) { O1a[m] = MFMAB(A1a, PQ[m][0], z4); O1b[m] = MFMAB(A1b, PQ[m][1], z4); }
#pragma unroll
        for (int m = 0; m < 2; ++m) {
            const float ia = __builtin_amdgcn_rcpf(O2[m][2] + 1e-6f), ib = __builtin_amdgcn_rcpf(O2[m][3] + 1e-6f);
            const f32x4 ya = O1a[m] * ia, yb = O1b[m] * ib;
            const u32x4 o = {pk2(ya[0], ya[1]), pk2(ya[2], ya[3]), pk2(yb[0], yb[1]), pk2(yb[2], yb[3])};
            BST128(o, rs, vo, ob + m * 18432 + p * 1024, 16);
            const float ir = g == 0 ? ia : ib;
            if (g < 2) __builtin_amdgcn_raw_buffer_store_b32(pk2(O2[m][0] * ir, O2[m][1] * ir), rs, orem, ob + m * 18432, 16);
        }
    }
    STAMP(layer * 4 + 0, 6);
    STAMP_OUT(layer * 4 + 0);
    WAIT_V(0);
    {
        u32 old = 0;
        if (lane == 0) old = __hip_atomic_fetch_add((LAS u32*)(size_t)(FL + 48), 1u, __ATOMIC_RELAXED, __HIP_MEMORY_SCOPE_WORKGROUP);
        if (__builtin_amdgcn_readfirstlane(old) == 3 && lane < 16)
            __hip_atomic_fetch_add((GAS u32*)(ws + WS_CNO + ((layer * 16 + pm) * 16 + lane) * 128), 1u << p, RLX_AGENT);
    }
    STAMP(layer * 4 + 1, 0);
    const int mt = 2 * pm + (p >> 3), nt = p & 7;
    const u32 vo4 = 4096 + g * 128 + (lane & 7) * 16;
#define K2_LD(v, sof, aux) __builtin_bit_cast(bf16x8, __builtin_amdgcn_raw_buffer_load_b128(rs, v, sof, aux))
    bf16x8 fw[6][5], fa[18];
    f32x4 ac[5];
    {
        const u32 xo = WS_X + 2 * layer * XB + (4 * mt + wid) * 18432 + 2304 * nt;
        const u32x4 r0 = __builtin_amdgcn_raw_buffer_load_b128(rs, vo, xo, 16), r1 = __builtin_amdgcn_raw_buffer_load_b128(rs, vo + 1024, xo, 16);
        const u32x2 r2 = __builtin_amdgcn_raw_buffer_load_b64(rs, 2048 + c * 16 + (g & 1) * 8, xo, 16);
        ac[0] = (f32x4){bflo(r0[0]), bfhi(r0[0]), bflo(r0[1]), bfhi(r0[1])}; ac[1] = (f32x4){bflo(r0[2]), bfhi(r0[2]), bflo(r0[3]), bfhi(r0[3])};
        ac[2] = (f32x4){bflo(r1[0]), bfhi(r1[0]), bflo(r1[1]), bfhi(r1[1])}; ac[3] = (f32x4){bflo(r1[2]), bfhi(r1[2]), bflo(r1[3]), bfhi(r1[3])};
        ac[4] = (f32x4){bflo(r2[0]), bfhi(r2[0]), bflo(r2[1]), bfhi(r2[1])};
    }
    u32 wl[2] = {lds + vo, lds + vo + 41472}, wl4[2] = {lds + vo4, lds + vo4 + 41472};
    PINV(wl[0]); PINV(wl[1]); PINV(wl4[0]); PINV(wl4[1]);
#define K2_W(s, n) ((n) < 4 ? LDSR(wl[(s) / 9], ((s) % 9) * 4608 + (n) * 1024) : LDSR(wl4[(s) / 9], ((s) % 9) * 4608))
    lds_wait(FL, 13u);
#pragma unroll
    for (int sl = 0; sl < 6; ++sl)
#pragma unroll
        for (int n = 0; n < 5; ++n) { fw[sl][n] = K2_W(sl, n); SB(); }
#pragma unroll
    for (int n = 0; n < 5; ++n) PINV(ac[n]);
    lds_wait(FL + 52, 1u);
    STAMP(layer * 4 + 1, 1);
#pragma unroll
    for (int sl = 0; sl < 18; ++sl) { fa[sl] = K2_LD(vo + (sl & 3) * 1024, WS_O + LSH * layer * XB + ((4 * mt + wid) * 18 + (sl & ~3)) * 1024, 16); SB(); }
#pragma unroll
    for (int sl = 0; sl < 18; ++sl)
#pragma unroll
        for (int n = 0; n < 5; ++n) {
            MFMA(ac[n], fw[sl % 6][n], fa[sl]);
            SB();
            if (sl + 6 < 18) fw[sl % 6][n] = K2_W(sl + 6, n);
            SB();
        }
    asm volatile("s_nop 15" ::: "memory");
    STAMP(layer * 4 + 1, 3);
    proj_store(ac, ws, rs, nullptr, FL + 128, hand, 2 * layer + 1, mt, nt, wid);
    STAMP(layer * 4 + 1, 2);
    STAMP_OUT(layer * 4 + 1);
}

constexpr u32 STAGE = 18432, NS = 6;
constexpr u32 K3_PK = NS * STAGE, K3_PP = 304, K3_WS = K3_PK + 128 * K3_PP, K3_S1 = K3_WS + 4096, K3_S2 = K3_S1 + 2048, K3_CS = K3_S2 + 1024;
static_assert(K3_CS + 1280 <= LDS_FL, "flag blocks overlap");

__device__ __forceinline__ void phase_b(char* ws, const u16* wsp, const u16* bsp, u16* out, u32 lds, int wid, int layer, bool fresh, bool hand) {
    int bid = blockIdx.x;
    GAS char* wsg = (GAS char*)ws;
    asm volatile("" : "+s"(bid), "+s"(wsg), "+s"(wid));
    ws = (char*)wsg;
    const int xcd = bid & 7, bi = bid >> 3;
    const int pm = MAP3 ? (xcd & 3) * 4 + (bi & 3) : (xcd & 1) * 8 + (bi & 7), pn = MAP3 ? (xcd >> 2) * 8 + (bi >> 2) : (xcd >> 1) * 4 + (bi >> 3);
    const u32 FL = lds + LDS_FL + 256 * layer + 128;
    STAMP_DECL;
    const auto rs = __builtin_amdgcn_make_buffer_rsrc(ws, 0, WS_END, 0x00020000);
    if (wid == 4) {
        LANES();
        u32 ao[16];
        const u32 vo = lane * 16;
#pragma unroll
        for (int q = 0; q < 16; ++q) { ao[q] = vo + (q >> 1) * 18432 - (q & 2) * 1024 + 4096; PINV(ao[q]); }
        const char* ap = ws + WS_X + (2 * layer + 1) * XB + 8 * pm * 18432 - 4096;
        const char* xp = ws + WS_WIN + (144 + 9 * pn + 8) * 18432;
        u32 slot = 0;
#define K3_ISSUE() do { const u32 dst = lds + slot * STAGE;                                                          \
            DMA4(ao[0], ao[1], ao[2], ao[3], ap, dst); DMA4(ao[4], ao[5], ao[6], ao[7], ap, dst + 4096);             \
            DMA4(ao[8], ao[9], ao[10], ao[11], ap, dst + 8192); DMA4(ao[12], ao[13], ao[14], ao[15], ap, dst + 12288); } while (0)
#define K3_SEG(s0, s1, GATE) for (int s = (s0); s < (s1) + 3; ++s) {                                                 \
            if (s >= (s0) + 3) {                                                                                     \
                if (s <= (s1)) WAIT_V(36); else if (s == (s1) + 1) WAIT_V(18); else WAIT_V(0);                       \
                stsw(FL, s - 2); FENCE(); }                                                                          \
            if (s == 1) BAR();                                                                                       \
            if (s == (s0) + 2 && (s0) < ZSPLIT) { WAIT_V(18); stsw(FL, (s0) + 1); FENCE(); }                         \
            if (s < (s1)) {                                                                                          \
                if (s >= (int)NS) lds_wait_sleep(FL + 16 + (lane & 3) * 4, GATE);                                    \
                if (s == 9) { ap -= 9 * 2048; xp = ws + WS_WIN + (9 * pn + 8) * 18432; }                             \
                K3_ISSUE();                                                                                          \
                DMA2(vo, vo, xp, lds + slot * STAGE + 16384);                                                        \
                xp += 2048; ap += 2048;                                                                              \
                slot = slot + 1 == NS ? 0 : slot + 1; } }
        int sfirst = 0;
        if (fresh) {
            lds_wait_sleep(FL + 120, 4u);
#pragma unroll
            for (int i = 0; i < NST; ++i) DMA2(vo, vo, xp + i * 2048, lds + i * STAGE + 16384);
            WAIT_V(0);
            stsw(FL, NST);
            lds_wait(FL + 124, 1u);
            __builtin_amdgcn_fence(__ATOMIC_ACQUIRE, "agent");
            xp += NST * 2048; ap += NST * 2048; slot = NST; sfirst = NST;
        }
        K3_SEG(sfirst, ZSPLIT, s - NS + 1);
        K3_SEG(ZSPLIT, 18, s >= 15 ? LGATE : max(ZGATE, s - (int)NS + 1));
#pragma unroll
        for (int q = 0; q < 16; ++q) { ao[q] = (q * 8 + (lane >> 3)) * 4096 + (((lane & 7) ^ ((q & 1) * 4 + g)) * 16) - (q & 3) * 1024 + 4096; PINV(ao[q]); }
        ap = (const char*)wsp + (size_t)pm * 128 * 4096 - 4096;
        lds_wait_sleep(FL + 16 + (lane & 3) * 4, LGATE2);
        for (int s = 18; s < 53; ++s) {
            if (s >= 21) {
                if (s <= 50) WAIT_V(32); else if (s == 51) WAIT_V(16); else WAIT_V(0);
                stsw(FL, s - 2);
                FENCE();
            }
            if (s < 50) {
                lds_wait_sleep(FL + 16 + (lane & 3) * 4, s - NS + 1);
                K3_ISSUE();
                ap += AFAKE ? 0 : (s == 33 ? 128 - 15 * 256 : 256);
                slot = slot + 1 == NS ? 0 : slot + 1;
            }
        }
        lds_wait_sleep(FL + 80, 2u);
        for (int i = 0; i < 8; ++i) {
            const int r = i < 2 ? 6 + i : (i < 6 ? i : i - 6);
            if (i >= 2) lds_wait_sleep(FL + 16 + (lane & 3) * 4, 43 + i);
            if (WGATE && i == WGI) lds_wait_sleep(FL + 92, WGATE);
            const char* wp = ws + WS_WOUT + ((MAP5 ? pn & 7 : pn >> 1) * 72 + 18 * (r >> 1) + 4 * (r & 1)) * 4608;
            const u32 dst = lds + r * STAGE;
            DMA4(vo, vo, vo, vo, wp, dst); DMA4(vo, vo, vo, vo, wp + 4096, dst + 4096); DMA4(vo, vo, vo, vo, wp + 8192, dst + 8192);
            DMA4(vo, vo, vo, vo, wp + 12288, dst + 12288); DMA2(vo, vo, wp + 16384, dst + 16384);
            if (i >= 2) WAIT_V(36);
        }
        WAIT_V(0);
        stsw(FL, 100u);
        return;
    }
    if (wid > 4) {
        LANES();
        if (!fresh) BAR();
        if (wid == 5) {
            f32x2 mean, var;
            x_ready_stats(ws, rs, FL, fresh, 2 * layer + 1, pm, pn, lane, mean, var);
#pragma unroll
            for (int k = 0; k < 2; ++k) {
                float r = __builtin_amdgcn_rsqf(var[k]);
                r = fmaxf(r, 0.f);
                const f32x4 o = {-mean[k], sfma(r, -2.302208198f, 0.f), sfma(sfma(sfma(r, r, 0.f), r, 0.f), -0.1029432397f, 0.f), sfma(var[k], r, 0.f)};
                *(LAS f32x4*)(size_t)(lds + K3_S1 + (2 * lane + k) * 16) = o;
            }
        } else if (wid == 7) {
            const int i = min(lane, 35);
            const f32x4* cp = (const f32x4*)(ws + WS_CSIN) + 36 * pn + i;
            const f32x4 a0 = cp[576], a1 = cp[576 + 1152], a2 = cp[576 + 2304], b0 = cp[0], b1 = cp[1152], b2 = cp[2304];
            *(LAS f32x4*)(size_t)(lds + K3_CS + i * 16) = sadd4(sadd4(a0, a1), a2);
            *(LAS f32x4*)(size_t)(lds + K3_CS + 640 + i * 16) = sadd4(sadd4(b0, b1), b2);
        }
        FENCE();
        stsw(FL + 32 + (wid - 5) * 4, 1u);
        u32 vo = lane * 16;
        PINV(vo);
        GAS u32* flg = (GAS u32*)(ws + WS_CNT + (layer * 16 + pn) * 4096);
        HST_DECL;
        if (wid != 5) {
            const int h = wid - 6;
            u32 ta = lds + K3_PK + (32 * h + 8 * g + (c >> 2)) * K3_PP + 16 * (c & 3), tx = ta + 256 - 8 * (c & 3), s2r = lds + K3_S2 + 128 * h + 32 * g;
            PINV(ta); PINV(tx); PINV(s2r);
            if (fresh) {
                lds_wait(FL + 124, 1u);
                u32x4 xt[NST][8];
#pragma unroll
                for (int i = 0; i < NST; ++i)
#pragma unroll
                    for (int q = 0; q < 8; ++q) { xt[i][q] = __builtin_amdgcn_raw_buffer_load_b128(rs, vo + (q & 1) * 1024, WS_X + (2 * layer + 1) * XB + (8 * pm + 4 * h + (q >> 1)) * 18432 + i * 2048, 16); SB(); }
#pragma unroll
                for (int i = 0; i < NST; ++i) {
#pragma unroll
                    for (int q = 0; q < 8; ++q) *(LAS u32x4*)(size_t)(lds + i * STAGE + h * 8192 + q * 1024 + vo) = xt[i][q];
                    FENCE();
                    stsw(FL + 8 + 4 * h, i + 1 < NST ? i + 1 : 1000u);
                    FENCE();
                }
            }
            const u32 zob = WS_Z + LSH * layer * ZB + (9 * pn * 64 + 2 * pm + h) * 1024;
            if (layer == 0) {
                if (h == 1) lds_wait_sleep(FL + 16 + (lane & 3) * 4, TG1); else lds_wait_sleep(FL + 16 + (lane & 3) * 4, TG0);
                touch_paced<TS3>((const char*)wsp + bid * 32768 + h * 16384, 16, vo);
            }
            lds_wait_sleep(FL + 48 + (lane & 3) * 4, 1u);
            HST(0);
            if (pm < HPRIO) __builtin_amdgcn_s_setprio(3);
            float zf[18][8];
#define K3_TR(fi, A, O) do { u32x2 t0_, t1_, t2_, t3_;                                                               \
                asm volatile("ds_read_b64_tr_b16 %0, %4 offset:%5\n\tds_read_b64_tr_b16 %1, %4 offset:%6\n\tds_read_b64_tr_b16 %2, %4 offset:%7\n\t" \
                             "ds_read_b64_tr_b16 %3, %4 offset:%8\n\ts_waitcnt lgkmcnt(0)" : "=&v"(t0_), "=&v"(t1_), "=&v"(t2_), "=&v"(t3_) \
                             : "v"(A), "i"(O), "i"((O) + 4 * K3_PP), "i"((O) + 64 * K3_PP), "i"((O) + 68 * K3_PP) : "memory"); \
                const u32 w_[8] = {t0_[0], t0_[1], t1_[0], t1_[1], t2_[0], t2_[1], t3_[0], t3_[1]};                  \
                _Pragma("unroll") for (int i_ = 0; i_ < 8; ++i_) { zf[2 * (fi) + (i_ >> 2)][2 * (i_ & 3)] = bflo(w_[i_]); zf[2 * (fi) + (i_ >> 2)][2 * (i_ & 3) + 1] = bfhi(w_[i_]); \
                    PINV(zf[2 * (fi) + (i_ >> 2)][2 * (i_ & 3)]); PINV(zf[2 * (fi) + (i_ >> 2)][2 * (i_ & 3) + 1]); } } while (0)
            K3_TR(0, ta, 0); K3_TR(1, ta, 8); K3_TR(2, ta, 64); K3_TR(3, ta, 72); K3_TR(4, ta, 128); K3_TR(5, ta, 136); K3_TR(6, ta, 192); K3_TR(7, ta, 200);
            K3_TR(8, tx, 0);
            HST(1);
            lds_wait_sleep(FL + 64, 1u);
            for (int i = 2; i < pm; ++i) __builtin_amdgcn_s_sleep(STAG);
            HST(2);
            f32x4 sv[2][4];
#pragma unroll
            for (int q = 0; q < 2; ++q)
#pragma unroll
                for (int k = 0; k < 4; ++k) sv[q][k] = *(LAS f32x4*)(size_t)(s2r + (k >> 1) * 512 + 256 * q + (k & 1) * 16);
#pragma unroll
            for (int ii = 0; ii < 18; ++ii) {
                const int q = ii / 9, i = 2 * (ii % 9) + q;
                u32x4 o;
                asm("v_fma_f32 %4, %4, %12, %20\n\tv_fma_f32 %5, %5, %13, %21\n\tv_fma_f32 %6, %6, %14, %22\n\tv_fma_f32 %7, %7, %15, %23\n\t"
                    "v_fma_f32 %8, %8, %16, %24\n\tv_fma_f32 %9, %9, %17, %25\n\tv_fma_f32 %10, %10, %18, %26\n\tv_fma_f32 %11, %11, %19, %27\n\t"
                    "v_cvt_pk_bf16_f32 %0, %4, %5\n\tv_cvt_pk_bf16_f32 %1, %6, %7\n\tv_cvt_pk_bf16_f32 %2, %8, %9\n\tv_cvt_pk_bf16_f32 %3, %10, %11"
                    : "=&v"(o[0]), "=&v"(o[1]), "=&v"(o[2]), "=&v"(o[3]), "+v"(zf[i][0]), "+v"(zf[i][1]), "+v"(zf[i][2]), "+v"(zf[i][3]), "+v"(zf[i][4]), "+v"(zf[i][5]), "+v"(zf[i][6]), "+v"(zf[i][7])
                    : "v"(sv[q][0][0]), "v"(sv[q][0][1]), "v"(sv[q][0][2]), "v"(sv[q][0][3]), "v"(sv[q][1][0]), "v"(sv[q][1][1]), "v"(sv[q][1][2]), "v"(sv[q][1][3]),
                      "v"(sv[q][2][0]), "v"(sv[q][2][1]), "v"(sv[q][2][2]), "v"(sv[q][2][3]), "v"(sv[q][3][0]), "v"(sv[q][3][1]), "v"(sv[q][3][2]), "v"(sv[q][3][3]));
                __builtin_amdgcn_raw_buffer_store_b128(o, rs, vo, zob + q * 32768 + (i >> 1) * 65536, 16);
                SB();
                if (ii % 9 == 8) {
                    if (q == 0) HST(3);
                    WAIT_V(0);
                    if (q == 0) HST(4);
                    u32 old = 0;
                    if (lane == 0) old = __hip_atomic_fetch_add((LAS u32*)(size_t)(FL + 76 + 4 * q), 1u, __ATOMIC_RELAXED, __HIP_MEMORY_SCOPE_WORKGROUP);
                    if (__builtin_amdgcn_readfirstlane(old) == 1 && lane < 16)
                        __hip_atomic_fetch_add(flg + lane * 64, 1u << (16 * q + pm), RLX_AGENT);
                    if (q == 0) { __builtin_amdgcn_s_setprio(0); __builtin_amdgcn_s_sleep(STAG2); }
                    SB();
                }
            }
            if (HPW && layer == 0) {
                lds_wait_sleep(FL + 16 + (lane & 3) * 4, HPW);
                const u32x4 z4 = {0u, 0u, 0u, 0u};
#pragma nounroll
                for (int i = 0; i < 16; ++i) {
                    BST128(z4, rs, vo, WS_H + ((8 * pm + 4 * h + (i >> 2)) * 8 + (pn >> 1)) * 9216 + ((pn & 1) * 4 + (i & 3)) * 1024, 16);
                    __builtin_amdgcn_s_sleep(HPS);
                }
                WAIT_V(0);
            }
            stsw(FL + 112 + 4 * h, 1u);
            HST(5);
            if (wid == 6) HST_OUT(9 + layer);
            return;
        }
        u32 wsa = lds + K3_WS + lane * 16, s2w = lds + K3_S2 + lane * 8;
        PINV(wsa); PINV(s2w);
        const u32 zsb = WS_ZST + (layer * 16 + pm) * 16384;
        lds_wait_sleep(FL + 16 + (lane & 3) * 4, 9u);
        __builtin_amdgcn_s_sleep(POLLD);
        __builtin_amdgcn_s_setprio(3);
        lds_wait(FL + 48 + (lane & 3) * 4, 1u);
        STAMP(layer * 4 + 2, 8);
        {
            const f32x4 st = sadd4(sadd4(*(LAS f32x4*)(size_t)wsa, *(LAS f32x4*)(size_t)(wsa + 1024)), sadd4(*(LAS f32x4*)(size_t)(wsa + 2048), *(LAS f32x4*)(size_t)(wsa + 3072)));
            u32x4 o = __builtin_bit_cast(u32x4, st);
#pragma unroll
            for (int k = 0; k < 4; ++k) o[k] = o[k] == SENT ? 0x7fc00000u : o[k];
            BST128(o, rs, vo, zsb + pn * 1024, 16);
            __builtin_amdgcn_s_sleep(XDELAY3);
            u32x4 t[16];
#pragma unroll
            for (int k = 0; k < 16; ++k) t[k] = __builtin_amdgcn_raw_buffer_load_b128(rs, vo + (k & 3) * 1024, zsb + (k >> 2) * 4096, 16);
            u32 mx = 0;
#pragma unroll
            for (int k = 0; k < 16; ++k) mx = max(max(mx, max(t[k][0], t[k][1])), max(t[k][2], t[k][3]));
            u32 spins = 0;
            if (__builtin_expect(ANY(mx == SENT), 0)) {
#pragma nounroll
                for (spins = 1; spins < SPIN_CAP; ++spins) {
                    FENCE();
                    u32 miss = 0;
#pragma unroll
                    for (int k = 0; k < 16; ++k) miss |= ANY(max(max(t[k][0], t[k][1]), max(t[k][2], t[k][3])) == SENT) ? 1u << k : 0u;
                    if (!miss) break;
#pragma unroll
                    for (int k = 0; k < 16; ++k) if (miss >> k & 1) t[k] = __builtin_amdgcn_raw_buffer_load_b128(rs, vo + (k & 3) * 1024, zsb + (k >> 2) * 4096, 16);
                }
            }
            f32x4 sa[4];
#pragma unroll
            for (int k = 0; k < 4; ++k)
                sa[k] = sadd4(sadd4(__builtin_bit_cast(f32x4, t[4 * k]), __builtin_bit_cast(f32x4, t[4 * k + 1])), sadd4(__builtin_bit_cast(f32x4, t[4 * k + 2]), __builtin_bit_cast(f32x4, t[4 * k + 3])));
            const f32x4 ss = sadd4(sadd4(sa[0], sa[1]), sadd4(sa[2], sa[3]));
            f32x2 rr, mr;
#pragma unroll
            for (int k = 0; k < 2; ++k) {
                const float m2 = sfma(ss[2 * k], 1.f / 2304.f, 0.f);
                float v2 = fmaxf(sfma(-m2, m2, sfma(ss[2 * k + 1], 1.f / 2304.f, 0.f)), 0.f) + 1e-5f;
                PINV(v2);
                rr[k] = __builtin_amdgcn_rsqf(v2);
                float t2 = -m2 * rr[k];
                PINV(t2);
                mr[k] = t2;
            }
            *(LAS f32x2*)(size_t)s2w = rr;
            *(LAS f32x2*)(size_t)(s2w + 512) = mr;
            FENCE();
            stsw(FL + 64, 1u);
            FENCE();
        }
        STAMP(layer * 4 + 2, 9);
        STAMP(layer * 4 + 2, 10);
        u32 ready = 0;
#define K3_NEED(n) do { _Pragma("nounroll") for (u32 spins_ = 0; ready < (u32)(n) && spins_ < SPIN_CAP; ++spins_) {   \
            const u32 v_ = __builtin_amdgcn_readfirstlane(__hip_atomic_load(flg + pm * 64, RLX_AGENT));                                               \
            ready = __builtin_ctzll(~(u64)v_); } FENCE(); } while (0)
        __builtin_amdgcn_s_sleep(FDELAY);
        K3_NEED(3);
        stsw(FL + 84, 1u);
        FENCE();
        STAMP(layer * 4 + 2, 11);
        STAMP_OUT(layer * 4 + 2);
        __builtin_amdgcn_s_setprio(0);
        const u32 zx = WS_Z + LSH * layer * ZB + (9 * pn + 8) * 65536;
        u32x4 xr[4][2];
#pragma unroll
        for (int k = 0; k < 3; ++k) {
            xr[k][0] = __builtin_amdgcn_raw_buffer_load_b128(rs, vo, zx + k * 2048, 16);
            xr[k][1] = __builtin_amdgcn_raw_buffer_load_b128(rs, vo + 1024, zx + k * 2048, 16);
            SB();
        }
        u32 slot = 0;
        for (int s0 = 0; s0 < 32; s0 += 4) {
#pragma unroll
            for (int k = 0; k < 4; ++k) {
                const int s = s0 + k;
                lds_wait_sleep(FL + 16 + (lane & 3) * 4, 18 + s - NS + 1);
                *(LAS u32x4*)(size_t)(lds + slot * STAGE + 16384 + lane * 16) = xr[k][0];
                *(LAS u32x4*)(size_t)(lds + slot * STAGE + 17408 + lane * 16) = xr[k][1];
                FENCE();
                stsw(FL + 4, 18 + s + 1);
                FENCE();
                const u32 sn = s + 3 < 32 ? s + 3 : 31;
                K3_NEED(sn + 1);
                xr[(k + 3) & 3][0] = __builtin_amdgcn_raw_buffer_load_b128(rs, vo, zx + sn * 2048, 16);
                xr[(k + 3) & 3][1] = __builtin_amdgcn_raw_buffer_load_b128(rs, vo + 1024, zx + sn * 2048, 16);
                slot = slot + 1 == NS ? 0 : slot + 1;
            }
        }
        lds_wait_sleep(FL + 92, 4u);
        __builtin_amdgcn_s_sleep(HDELAY);
        GAS u32* hm = (GAS u32*)(ws + WS_CNT + (layer * 16 + pm) * 4096 + pn * 256 + 128);
#pragma nounroll
        for (u32 spins = 0; spins < SPIN_CAP; ++spins) {
            const u32 v = __builtin_amdgcn_readfirstlane(__hip_atomic_load(hm, RLX_AGENT));
            FENCE();
            stsw(FL + 88, v);
            if (v == 0xffffu) break;
        }
        return;
    }
    LANES();
    STAMP(layer * 4 + 2, 0);
    u32 vA = lane * 16, vB = lane * 16 + 18432;
    u32 so = WS_WIN + (144 + 9 * pn + 2 * wid) * 18432;
    bf16x8 bb[6][2];
#define BLD(voff, imm, aux) __builtin_bit_cast(bf16x8, __builtin_amdgcn_raw_buffer_load_b128(rs, (voff) + (imm), so, aux))
#define BPRO(aux) do { _Pragma("unroll") for (int r = 0; r < 5; ++r) { if (r == 3) so += 3072;                       \
        bb[r][0] = BLD(vA, (r % 3) * 1024, aux); SB(); bb[r][1] = BLD(vB, (r % 3) * 1024, aux); SB(); } } while (0)
    BPRO(0);
    u32 pa[2][4];
#pragma unroll
    for (int q = 0; q < 4; ++q) { pa[0][q] = lds + lane * 16 + ((q ^ wid) * 4096); pa[1][q] = pa[0][q] + 1024; }
    u32 px = lds + 16384 + lane * 16;
    u32 myfree = FL + 16 + wid * 4, fladdr = FL + (lane & 3) * 4, cnt = 0;
    PINV(cnt); PINV(myfree); PINV(fladdr);
    int dstep = 3 * STAGE;
    f32x4 acc[8][2], accx[2];
    bf16x8 a[8], xf;
#define ALOAD() do { a[0] = LDSR(pa[0][0], 0); SB(); xf = LDSR(px, 0); SB();                                         \
        _Pragma("unroll") for (int m = 1; m < 8; ++m) { a[m] = LDSR(pa[0][m >> 1], (m & 1) * 2048); SB(); } } while (0)
#define MFS(acc, x, y) do { MFMA(acc, x, y); SB(); } while (0)
#define HALF(r, RD, kn, sf, rl, imm, aux, E0, E1, E2, E3, E4, E5) do {                                               \
        WCNT(8, 5); MFS(acc[0][0], bb[r][0], a[0]); E0; SB();                                                       \
        MFS(acc[0][1], bb[r][1], a[0]); E1; SB();                                                                   \
        MFS(accx[0], xf, a[0]); if (RD) a[0] = LDSR(pa[kn][0], (sf)); SB();                                         \
        MFS(accx[1], xf, a[1]); if (RD) xf = LDSR(px, (sf) + (kn) * 1024); SB();                                    \
        MFS(acc[1][1], bb[r][1], a[1]); bb[rl][0] = BLD(vA, imm, aux); SB();                                        \
        MFS(acc[1][0], bb[r][0], a[1]); if (RD) a[1] = LDSR(pa[kn][0], (sf) + 2048); SB();                          \
        MFS(acc[2][0], bb[r][0], a[2]); E2; SB();                                                                   \
        MFS(acc[2][1], bb[r][1], a[2]); if (RD) a[2] = LDSR(pa[kn][1], (sf)); SB();                                 \
        WCNT(63, 5); MFS(acc[3][1], bb[r][1], a[3]); SB();                                                          \
        MFS(acc[3][0], bb[r][0], a[3]); if (RD) a[3] = LDSR(pa[kn][1], (sf) + 2048); SB();                          \
        MFS(acc[4][0], bb[r][0], a[4]); bb[rl][1] = BLD(vB, imm, aux); SB();                                        \
        MFS(acc[4][1], bb[r][1], a[4]); if (RD) a[4] = LDSR(pa[kn][2], (sf)); SB();                                 \
        MFS(acc[5][1], bb[r][1], a[5]); E3; SB();                                                                   \
        MFS(acc[5][0], bb[r][0], a[5]); if (RD) a[5] = LDSR(pa[kn][2], (sf) + 2048); SB();                          \
        MFS(acc[6][0], bb[r][0], a[6]); E4; SB();                                                                   \
        MFS(acc[6][1], bb[r][1], a[6]); if (RD) a[6] = LDSR(pa[kn][3], (sf)); SB();                                 \
        MFS(acc[7][1], bb[r][1], a[7]); E5; SB();                                                                   \
        MFS(acc[7][0], bb[r][0], a[7]); if (RD) a[7] = LDSR(pa[kn][3], (sf) + 2048); SB(); } while (0)
#define STEP(u, chk, nso, aux) do { u32 fl_; u64 msk_;                                                               \
        HALF(2 * (u), 1, 1, (u) * STAGE, (2 * (u) + 5) % 6, ((2 * (u) + 5) % 3) * 1024, aux,                         \
             { if ((u) == 2) so += 3072; }, { cnt += 1; PINV(cnt); }, { FENCE(); fl_ = ldsw(fladdr); FENCE(); },     \
             { if ((u) == 2) { pa[0][0] += dstep; pa[0][1] += dstep; PINV(pa[0][0]); PINV(pa[0][1]); } },            \
             { if ((u) == 2) { pa[0][2] += dstep; pa[0][3] += dstep; PINV(pa[0][2]); PINV(pa[0][3]); } },            \
             { asm volatile("v_cmp_le_u32 %0, %1, %2" : "=s"(msk_) : "v"(fl_), "v"(cnt)); if ((u) == 2) { px += dstep; PINV(px); } }); \
        HALF(2 * (u) + 1, 1, 0, (((u) + 1) % 3) * STAGE, (2 * (u) + 6) % 6, ((2 * (u) + 6) % 3) * 1024, aux,         \
             { if (__builtin_expect(msk_ != 0, 0)) { if (chk) lds_wait_gt(fladdr, cnt); } FENCE(); }, { if ((u) == 0) so = (nso); }, \
             { FENCE(); stsw(myfree, cnt); FENCE(); },                                                               \
             { if ((u) == 2) { pa[1][0] += dstep; pa[1][1] += dstep; PINV(pa[1][0]); PINV(pa[1][1]); } },            \
             { if ((u) == 2) { pa[1][2] += dstep; pa[1][3] += dstep; PINV(pa[1][2]); PINV(pa[1][3]); } },            \
             { if ((u) == 2) dstep = -dstep; }); } while (0)
#define PINACC() do { _Pragma("unroll") for (int m = 0; m < 8; ++m) { PINV(acc[m][0]); PINV(acc[m][1]); } PINV(accx[0]); PINV(accx[1]); asm volatile("s_nop 1"); } while (0)
    if (!fresh) BAR();
    u32 yp[18][2], early = 0;
    const u32 s1a = lds + K3_S1 + c * 16;
#pragma unroll
    for (int ph = 0; ph < 2; ++ph) {
        if (ph == 0) {
#pragma unroll
            for (int m = 0; m < 8; ++m) { acc[m][0] = acc[m][1] = (f32x4){0.f, 0.f, 0.f, 0.f}; if (m < 2) accx[m] = acc[m][0]; }
        } else {
            const u32 ca = lds + K3_CS + ph * 640 + g * 16;
            const f32x4 c0 = *(LAS f32x4*)(size_t)(ca + wid * 128), c1 = *(LAS f32x4*)(size_t)(ca + wid * 128 + 64), c2 = *(LAS f32x4*)(size_t)(ca + 512);
#pragma unroll
            for (int m = 0; m < 8; ++m) {
                const float nm = *(LAS float*)(size_t)(s1a + ((((m >> 1) ^ wid) << 1) + (m & 1)) * 256);
                acc[m][0] = c0 * nm; acc[m][1] = c1 * nm;
                if (m < 2) accx[m] = c2 * nm;
            }
        }
        PINACC();
        if (ph == 0) { lds_wait(fladdr, 1u); ALOAD(); STAMP(layer * 4 + 2, 1); }
        const u32 nxt = WS_WIN + (9 * pn + 2 * wid) * 18432;
        for (int j = 0; j < 3; ++j) {
            STEP(0, true, j == 2 ? nxt : so + 3072, 0);
            STEP(1, true, 0, 0);
            STEP(2, ph == 0 || j < 2, 0, 0);
        }
        asm volatile("s_nop 15" ::: "memory");
        STAMP(layer * 4 + 2, 2 + 2 * ph);
        if (ph == 1) {
            vB = lane * 16 + 65536;
            so = WS_Z + LSH * layer * ZB + (9 * pn + 2 * wid) * 65536;
            FENCE();
            early = __builtin_amdgcn_readfirstlane(ldsw(FL + 84));
            FENCE();
            if (early) BPRO(16);
        }
        float t1[8], t2[8];
        f32x4 rcs[8], cz[3];
        if (ph == 0) {
            lds_wait(FL + 32 + (lane % 3) * 4, 1u);
            const u32 ca = lds + K3_CS + g * 16;
            cz[0] = *(LAS f32x4*)(size_t)(ca + wid * 128); cz[1] = *(LAS f32x4*)(size_t)(ca + wid * 128 + 64); cz[2] = *(LAS f32x4*)(size_t)(ca + 512);
        }
#pragma unroll
        for (int m = 0; m < 8; ++m) rcs[m] = *(LAS f32x4*)(size_t)(s1a + ((((m >> 1) ^ wid) << 1) + (m & 1)) * 256);
#pragma unroll
        for (int m = 0; m < 8; ++m) {
            if (ph == 1 && m == 4 && !early) { FENCE(); early = __builtin_amdgcn_readfirstlane(ldsw(FL + 84)); FENCE(); if (early) BPRO(16); }
            constexpr int NP = 6;
            const int np = m < 2 ? 6 : 4;
            const f32x2 k1 = {rcs[m][1], rcs[m][1]}, k3 = {rcs[m][2], rcs[m][2]}, sg = {rcs[m][3], rcs[m][3]};
            f32x2 v[NP], e[NP];
#pragma unroll
            for (int i = 0; i < NP; ++i) if (i < np) {
                const f32x4 av = i < 4 ? acc[m][i >> 1] : accx[m & 1];
                v[i] = (f32x2){av[2 * (i & 1)], av[2 * (i & 1) + 1]};
                if (ph == 0) v[i] += (f32x2){cz[i >> 1][2 * (i & 1)], cz[i >> 1][2 * (i & 1) + 1]} * (f32x2){rcs[m][0], rcs[m][0]};
            }
#pragma unroll
            for (int i = 0; i < NP; ++i) if (i < np) e[i] = v[i] * v[i];
#pragma unroll
            for (int i = 0; i < NP; ++i) if (i < np) e[i] = e[i] * k3 + k1;
#pragma unroll
            for (int i = 0; i < NP; ++i) if (i < np) e[i] = e[i] * v[i];
            SB();
#pragma unroll
            for (int i = 0; i < NP; ++i) if (i < np) { e[i][0] = __builtin_amdgcn_exp2f(e[i][0]); e[i][1] = __builtin_amdgcn_exp2f(e[i][1]); }
            SB();
#pragma unroll
            for (int i = 0; i < NP; ++i) if (i < np) e[i] = e[i] * sg + sg;
            SB();
#pragma unroll
            for (int i = 0; i < NP; ++i) if (i < np) { e[i][0] = __builtin_amdgcn_rcpf(e[i][0]); e[i][1] = __builtin_amdgcn_rcpf(e[i][1]); }
            SB();
#pragma unroll
            for (int i = 0; i < NP; ++i) if (i < np) v[i] = v[i] * e[i];
#pragma unroll
            for (int i = 0; i < NP; ++i) if (i < np) yp[i < 4 ? 2 * m + (i >> 1) : 16 + m][i & 1] = pk2(v[i][0], v[i][1]);
            if (ph == 0) {
                f32x2 u1 = (v[0] + v[1]) + (v[2] + v[3]), u2 = v[0] * v[0], u3 = v[1] * v[1];
                u2 = v[2] * v[2] + u2; u3 = v[3] * v[3] + u3;
                if (m < 2) { u1 += v[4] + v[5]; u2 = v[4] * v[4] + u2; u3 = v[5] * v[5] + u3; }
                u2 += u3;
                t1[m] = u1[0] + u1[1]; t2[m] = u2[0] + u2[1];
            }
            PINV(yp[2 * m][0]); PINV(yp[2 * m][1]); PINV(yp[2 * m + 1][0]); PINV(yp[2 * m + 1][1]);
            if (m < 2) { PINV(yp[16 + m][0]); PINV(yp[16 + m][1]); }
            if (ph == 0) {
                const u32 row = (((((m >> 1) ^ wid) << 1) + (m & 1)) << 4) + c;
                u32x4 o = {yp[2 * m][0], yp[2 * m][1], yp[2 * m + 1][0], yp[2 * m + 1][1]};
                *(LAS u32x4*)(size_t)(lds + K3_PK + row * K3_PP + 64 * wid + 16 * g) = o;
                if (m < 2) { u32x2 ox = {yp[16 + m][0], yp[16 + m][1]}; *(LAS u32x2*)(size_t)(lds + K3_PK + row * K3_PP + 256 + 8 * g) = ox; }
            }
        }
        if (ph == 0) {
#pragma unroll
            for (int m = 0; m < 4; ++m) {
                auto r1 = __builtin_amdgcn_permlane32_swap(F2U(t1[m]), F2U(t1[m + 4]), false, false);
                auto r2 = __builtin_amdgcn_permlane32_swap(F2U(t2[m]), F2U(t2[m + 4]), false, false);
                t1[m] = U2F(r1[0]) + U2F(r1[1]); t2[m] = U2F(r2[0]) + U2F(r2[1]);
            }
#pragma unroll
            for (int m = 0; m < 2; ++m) {
                auto r1 = __builtin_amdgcn_permlane16_swap(F2U(t1[m]), F2U(t1[m + 2]), false, false);
                auto r2 = __builtin_amdgcn_permlane16_swap(F2U(t2[m]), F2U(t2[m + 2]), false, false);
                f32x2 o = {U2F(r1[0]) + U2F(r1[1]), U2F(r2[0]) + U2F(r2[1])};
                *(LAS f32x2*)(size_t)(lds + K3_WS + (wid * 128 + 32 * (g ^ wid) + 16 * m + c) * 8) = o;
            }
            FENCE();
            stsw(FL + 48 + wid * 4, 1u);
            FENCE();
        }
        STAMP(layer * 4 + 2, 3 + 2 * ph);
    }
    {
        float bv[8];
#pragma unroll
        for (int m = 0; m < 8; ++m) bv[m] = bflo(bsp[128 * pm + (((((m >> 1) ^ wid) << 1) + (m & 1)) << 4) + c]);
#pragma unroll
        for (int m = 0; m < 8; ++m) {
            acc[m][0] = acc[m][1] = (f32x4){bv[m], bv[m], bv[m], bv[m]};
            if (m < 2) accx[m] = acc[m][0];
        }
    }
#pragma unroll
    for (int q = 0; q < 4; ++q) { pa[0][q] = lds + (c >> 3) * 1024 + (c & 7) * 128 + ((g ^ ((c >> 1) & 7)) * 16) + ((q ^ wid) * 4096); pa[1][q] = pa[0][q] ^ 64; }
    if (!early) { lds_wait(FL + 84, 1u); BPRO(16); }
    lds_wait(fladdr, 19u);
    STAMP(layer * 4 + 2, 6);
    ALOAD();
    PINACC();
    for (int j = 0; j < 10; ++j) {
        STEP(0, true, so + BADV, BAUX);
        STEP(1, true, 0, BAUX);
        STEP(2, true, 0, BAUX);
    }
    if (BADV > 0) so -= 8192;
    STEP(0, true, so, 16);
    STEP(1, false, 0, 16);
    asm volatile("s_nop 15" ::: "memory");
    STAMP(layer * 4 + 2, 7);
    {
        lds_wait(FL + 112 + (lane & 1) * 4, 1u);
        const u32 hb = WS_H + LSH * layer * ZB + (8 * pm * 8 + (pn >> 1)) * 9216;
#pragma unroll
        for (int m = 0; m < 8; ++m) {
            const int mt = (((m >> 1) ^ wid) << 1) + (m & 1);
            u32x4 o;
            o[0] = pk2(bflo(yp[2 * m][0]) * acc[m][0][0], bfhi(yp[2 * m][0]) * acc[m][0][1]);
            o[1] = pk2(bflo(yp[2 * m][1]) * acc[m][0][2], bfhi(yp[2 * m][1]) * acc[m][0][3]);
            o[2] = pk2(bflo(yp[2 * m + 1][0]) * acc[m][1][0], bfhi(yp[2 * m + 1][0]) * acc[m][1][1]);
            o[3] = pk2(bflo(yp[2 * m + 1][1]) * acc[m][1][2], bfhi(yp[2 * m + 1][1]) * acc[m][1][3]);
            BST128(o, rs, vA, hb + mt * 73728 + ((pn & 1) * 4 + wid) * 1024, 16);
            if (m < 2) {
                u32x2 ox = {pk2(bflo(yp[16 + m][0]) * accx[m][0], bfhi(yp[16 + m][0]) * accx[m][1]), pk2(bflo(yp[16 + m][1]) * accx[m][2], bfhi(yp[16 + m][1]) * accx[m][3])};
                __builtin_amdgcn_raw_buffer_store_b64(ox, rs, vA, hb + mt * 73728 + 8192 + (pn & 1) * 8, 16);
            }
        }
        STAMP(layer * 4 + 2, 12);
        WAIT_V(0);
        u32 old = 0;
        if (lane == 0) old = __hip_atomic_fetch_add((LAS u32*)(size_t)(FL + 92), 1u, __ATOMIC_RELAXED, __HIP_MEMORY_SCOPE_WORKGROUP);
        if (__builtin_amdgcn_readfirstlane(old) == 3 && lane < 16)
            __hip_atomic_fetch_add((GAS u32*)(ws + WS_CNT + (layer * 16 + pm) * 4096 + lane * 256 + 128), 1u << pn, RLX_AGENT);
    }
    STAMP_OUT(layer * 4 + 2);
    STAMP(layer * 4 + 3, 0);
    {
        const int mt = 2 * pm + (MAP5 ? pn >> 3 : pn & 1), nt = MAP5 ? pn & 7 : pn >> 1;
        const u32 wb = WS_WOUT + (nt * 72 + 18 * wid) * 4608;
        const u32 vo4 = 4096 + g * 128 + (lane & 7) * 16;
        bf16x8 fa[4][4], fw[3][5], fl[5];
        f32x4 ac[4][5];
#pragma unroll
        for (int m = 0; m < 4; ++m)
#pragma unroll
            for (int n = 0; n < 5; ++n) ac[m][n] = (f32x4){0.f, 0.f, 0.f, 0.f};
        u32 abm[4];
#pragma unroll
        for (int m = 0; m < 4; ++m) abm[m] = WS_H + LSH * layer * ZB + ((4 * mt + (m ^ wid)) * 8 + 2 * wid) * 9216;
#define K5_LD(v, sof, aux) __builtin_bit_cast(bf16x8, __builtin_amdgcn_raw_buffer_load_b128(rs, v, sof, aux))
#define K5_LW(r, s) do { _Pragma("unroll") for (int n = 0; n < 4; ++n) fw[r][n] = K5_LD(vA + n * 1024, wb + (s) * 4608, 0); fw[r][4] = K5_LD(vo4, wb + (s) * 4608, 0); SB(); } while (0)
#define K5_LL(s) do { _Pragma("unroll") for (int n = 0; n < 4; ++n) fl[n] = LDSR(wl, (s) * 4608 + n * 1024); fl[4] = LDSR(wl4, (s) * 4608); SB(); } while (0)
#define K5_LA(r, s) do { _Pragma("unroll") for (int m = 0; m < 4; ++m) fa[r][m] = K5_LD(vA + ((s) & 3) * 1024, abm[m] + ((s) & ~3) * 1024, 16); SB(); } while (0)
#define K5_MM(W, r) do { _Pragma("unroll") for (int m = 0; m < 4; ++m) _Pragma("unroll") for (int n = 0; n < 5; ++n) ac[m][n] = MFMAB(W[n], fa[r][m], ac[m][n]); SB(); } while (0)
        PINV(vA);
        const u32 wl = lds + wid * 2 * STAGE + vA, wl4 = lds + wid * 2 * STAGE + vo4;
        K5_LW(0, 8); K5_LW(1, 9); K5_LW(2, 10);
        u32x4 res[2]; u32x2 res2;
        const u32 xoff = WS_X + (2 * layer + 1) * XB + (4 * mt + wid) * 18432;
        {
            const u32 need = 15u << (4 * wid);
            FENCE();
#pragma nounroll
            for (u32 n = 0; n < SPIN_CAP; ++n) { if ((__builtin_amdgcn_readfirstlane(ldsw(FL + 88)) & need) == need) break; }
            FENCE();
        }
        STAMP(layer * 4 + 3, 1);
        constexpr int ord[18] = {8, 0, 9, 1, 10, 2, 11, 3, 12, 4, 13, 5, 14, 6, 15, 7, 16, 17};
#pragma unroll
        for (int i = 0; i < 4; ++i) K5_LA(i, ord[i]);
        lds_wait(FL, 100u);
#pragma unroll
        for (int m = 0; m < 4; ++m)
#pragma unroll
            for (int n = 0; n < 5; ++n) PINV(ac[m][n]);
        asm volatile("s_nop 1");
#pragma unroll
        for (int i = 0; i < 18; ++i) {
            const int sl = ord[i];
#pragma unroll
            for (int m = 0; m < 4; ++m)
#pragma unroll
                for (int n = 0; n < 5; ++n) {
                    if (sl >= 8) MFS(ac[m][n], fw[(sl - 8) % 3][n], fa[i & 3][m]); else MFS(ac[m][n], fl[n], fa[i & 3][m]);
                    const int e = (n == 1 ? m : (m == 0 && n == 3 ? 4 : -1));
                    if (e >= 0) {
                        if (sl >= 8 && sl < 16) fl[e] = e < 4 ? LDSR(wl, (sl - 8) * 4608 + e * 1024) : LDSR(wl4, (sl - 8) * 4608);
                        if (sl < 7) fw[sl % 3][e] = e < 4 ? K5_LD(vA + e * 1024, wb + (sl + 11) * 4608, 0) : K5_LD(vo4, wb + (sl + 11) * 4608, 0);
                        if (sl == 7 && e == 0) res[0] = __builtin_amdgcn_raw_buffer_load_b128(rs, vA, xoff + 2304 * nt, 16);
                        if (sl == 7 && e == 1) res[1] = __builtin_amdgcn_raw_buffer_load_b128(rs, vA + 1024, xoff + 2304 * nt, 16);
                        if (sl == 7 && e == 2) { const int l2 = lane_id(); res2 = __builtin_amdgcn_raw_buffer_load_b64(rs, (l2 & 15) * 16 + ((l2 >> 1) & 8) + 2048, xoff + 2304 * nt, 16); }
                    }
                    if (n == 4 && i + 4 < 18) fa[i & 3][m] = K5_LD(vA + (ord[i + 4 < 18 ? i + 4 : 0] & 3) * 1024, abm[m] + (ord[i + 4 < 18 ? i + 4 : 0] & ~3) * 1024, 16);
                    SB();
                }
        }
        asm volatile("s_nop 15" ::: "memory");
        STAMP(layer * 4 + 3, 3);
        proj_tail(ac, res, res2, lds, 2 * STAGE, FL + 96, ws, rs, out, FL + 128, hand, 2 * layer + 2, mt, nt, wid);
    }
    STAMP(layer * 4 + 3, 2);
    STAMP_OUT(layer * 4 + 3);
}

__global__ __launch_bounds__(512, 2) void k1_main(char* __restrict__ ws, const u16* __restrict__ win, const u16* __restrict__ wsp, const u16* __restrict__ bsp,
                                                  u16* __restrict__ out, int first, int last) {
    const u32 lds = 0;
    const int wid = __builtin_amdgcn_readfirstlane(threadIdx.x >> 6);
    if (threadIdx.x >= 352) {
        const int i = threadIdx.x - 352, blk = i >> 5, w = i & 31;
        stsw(lds + LDS_FL + i * 4, !(blk & 1) ? 0u : (w == 1 ? 18u : ((w & 30) == 2 && blk == first ? 1000u : 0u)));
        WAIT_L0();
    }
#pragma nounroll
    for (int layer = first >> 1; 2 * layer < last; ++layer) {
        if (2 * layer >= first) phase_a(ws, win, lds, wid, layer, 2 * layer > first, 2 * layer + 1 < last);
        if (2 * layer + 1 < last) phase_b(ws, wsp, bsp, out, lds, wid, layer, 2 * layer + 1 > first, 2 * layer + 2 < last);
    }
}

extern "C" __global__ void k9_end() {}

extern "C" void kernel_launch(void* const* d_in, const int* in_sizes, int n_in, void* d_out, int out_size, void* d_ws, size_t ws_size,
                              hipStream_t stream) {
    const u16* x = (const u16*)d_in[0];
    const u16* wqkv = (const u16*)d_in[1];
    const u16* wo = (const u16*)d_in[2];
    const u16* win = (const u16*)d_in[4];
    const u16* wsp = (const u16*)d_in[5];
    const u16* bsp = (const u16*)d_in[6];
    const u16* wout = (const u16*)d_in[7];
    char* ws = (char*)d_ws + WS_BASE(ws_size);
    u16* out = (u16*)d_out;
    (void)hipFuncSetAttribute((const void*)k1_main, hipFuncAttributeMaxDynamicSharedMemorySize, LDS_TOTAL);
    k0_prep<<<K0_GRID, 576, 0, stream>>>(x, wqkv, wo, wout, ws);
    k1_main<<<256, 512, LDS_TOTAL, stream>>>(ws, win, wsp, bsp, out, 0, 1);
    k1_main<<<256, 512, LDS_TOTAL, stream>>>(ws, win, wsp, bsp, out, 1, 4);
}
```

```cpp
#include <hip/hip_runtime.h>
#include <hip/hip_bf16.h>
#include <stdint.h>

#define LAS __attribute__((address_space(3)))
#define GAS __attribute__((address_space(1)))
typedef unsigned short u16;
typedef unsigned u32;
typedef unsigned long long u64;
typedef short bf16x8 __attribute__((ext_vector_type(8)));
typedef float f32x2 __attribute__((ext_vector_type(2)));
typedef float f32x4 __attribute__((ext_vector_type(4)));
typedef unsigned u32x2 __attribute__((ext_vector_type(2)));
typedef unsigned u32x4 __attribute__((ext_vector_type(4)));
typedef __bf16 bf2_t __attribute__((ext_vector_type(2)));

#define ANY(p) (__builtin_amdgcn_ballot_w64(p) != 0)
#define U2F(x) __builtin_bit_cast(float, (u32)(x))
#define F2U(x) __builtin_bit_cast(u32, (float)(x))
#define WAIT_V(n) asm volatile("s_waitcnt vmcnt(%0)" ::"i"(n) : "memory")
#define WAIT_L0() asm volatile("s_waitcnt lgkmcnt(0)" ::: "memory")
#define FENCE() asm volatile("" ::: "memory")
#define SB() __builtin_amdgcn_sched_barrier(0)
#define BAR() do { FENCE(); __builtin_amdgcn_s_barrier(); FENCE(); } while (0)
#define WCNT(vm, lg) __builtin_amdgcn_s_waitcnt(((vm) & 15) | (7 << 4) | ((lg) << 8) | (((vm) >> 4) << 14))
#define MFMA(acc, x, y) asm volatile("v_mfma_f32_16x16x32_bf16 %0, %1, %2, %0" : "+v"(acc) : "v"(x), "v"(y))
#define MFMAB(x, y, c) __builtin_amdgcn_mfma_f32_16x16x32_bf16(x, y, c, 0, 0, 0)
#define BST128(o, rs, vo, so, aux) do { __builtin_amdgcn_raw_buffer_store_b128(o, rs, vo, so, aux); SB(); asm volatile("s_nop 1"); SB(); } while (0)
#define PINV(x) asm volatile("" : "+v"(x))
#define RLX_AGENT __ATOMIC_RELAXED, __HIP_MEMORY_SCOPE_AGENT

constexpr int S_ = 2048, D_ = 576, FH_ = 2304;
#define XDELAY1 18
#define XDELAY3 20
#define LGATE 12
#define LGATE2 14
#define POLLD 40
#define STAG 1
#define HPRIO 3
#define FDELAY 45
#define STAG2 60
#define MAP1 1
#define MAP3 1
#define CODETOUCH 1
#define WSPTOUCH 0
#define LSH 0
#define L2PF 0
#define ZSPLIT 10
#define ZGATE 8
#define QLATE 0
#define BADV 3072
#define AFAKE 0
#define BAUX 16
#define HDELAY 10
#define MAP5 1
#define WGATE 0
#define WGI 7
#define WEARLY 0
#define ODELAY 12
#define K0NT 1
#define NTL(p) __builtin_nontemporal_load(p)
#define TS3 2
#define TG0 6
#define TG1 6
#define HPW 0
#define HPS 4
#define K0AUX 0
#define RTD 0
#define RTAUX 16
#define CUT 1
#define XRD 12
#define NST 4
#define WOG 0
#define LAZY 2
#define PSL 0
#define WRD 90
constexpr u32 SENT = 0xFFFFFFFFu;
constexpr u32 SPIN_CAP = 1u << 16;

constexpr u32 WS_WQ = 0;
constexpr u32 WS_CSQ = WS_WQ + 16 * 129024;
constexpr u32 WS_WO = WS_CSQ + 18 * 8192;
constexpr u32 WS_WIN = WS_WO + 8 * 18 * 4608;
constexpr u32 WS_CSIN = WS_WIN + 288 * 18 * 1024;
constexpr u32 WS_WOUT = WS_CSIN + 3 * 4608 * 4;
constexpr u32 WS_X = WS_WOUT + 8 * 72 * 4608;
constexpr u32 XB = 128 * 18 * 1024, XSB = 2048 * 64, ZB = 144 * 64 * 1024;
constexpr u32 WS_XS = WS_X + 4 * XB;
constexpr u32 WS_O = WS_XS + 4 * XSB;
constexpr u32 WS_Z = WS_O + (1 + LSH) * XB;
constexpr u32 WS_H = WS_Z + (1 + LSH) * ZB;
constexpr u32 WS_KVP = WS_H + (1 + LSH) * ZB;
constexpr u32 WS_ZST = WS_KVP + 2 * 256 * 2816;
constexpr u32 WS_CNT = WS_ZST + 2 * 256 * 1024;
constexpr u32 WS_CNO = WS_CNT + 2 * 16 * 32 * 128;
constexpr u32 WS_CNX = WS_CNO + 2 * 16 * 16 * 128;
constexpr u32 WS_CNW = WS_CNX + 4 * 16 * 16 * 128;
constexpr u32 WS_END = WS_CNW + 72 * 128;
constexpr u32 WS_DBG = WS_END;
#define WSTOP 0
#define WS_BASE(n) (WSTOP ? (((n) - WS_END - 0x100000) & ~(size_t)0xfffff) : 0)

#define STAMP_DECL
#define STAMP(k, i) do { } while (0)
#define HST(i) do { } while (0)
#define HST_DECL
#define HST_OUT(k) do { } while (0)
#define STAMPV(k, i, v) do { } while (0)
#define STAMP_OUT(k) do { } while (0)

__device__ __forceinline__ u32 pk2(float lo, float hi) {
    f32x2 v = {lo, hi};
    bf2_t b = __builtin_convertvector(v, bf2_t);
    return __builtin_bit_cast(u32, b);
}
__device__ __forceinline__ float bflo(u32 w) { return U2F(w << 16); }
__device__ __forceinline__ float bfhi(u32 w) { return U2F(w & 0xffff0000u); }
__device__ __forceinline__ bf16x8 mk8(u32 a, u32 b, u32 c, u32 d) { u32x4 v = {a, b, c, d}; return __builtin_bit_cast(bf16x8, v); }
__device__ __forceinline__ float sfma(float a, float b, float c) { float r; asm("v_fma_f32 %0, %1, %2, %3" : "=v"(r) : "v"(a), "v"(b), "v"(c)); return r; }
__device__ __forceinline__ f32x4 sadd4(f32x4 a, f32x4 b) {
    float r0 = a[0], r1 = a[1], r2 = a[2], r3 = a[3];
    asm("v_add_f32 %0, %0, %4\n\tv_add_f32 %1, %1, %5\n\tv_add_f32 %2, %2, %6\n\tv_add_f32 %3, %3, %7" : "+v"(r0), "+v"(r1), "+v"(r2), "+v"(r3) : "v"(b[0]), "v"(b[1]), "v"(b[2]), "v"(b[3]));
    f32x4 r = {r0, r1, r2, r3};
    return r;
}
__device__ __forceinline__ f32x4 phi4(f32x4 t) {
    const f32x4 u = t * 1.44269504089f;
    f32x4 e, m;
#pragma unroll
    for (int r = 0; r < 4; ++r) { e[r] = __builtin_amdgcn_exp2f(__builtin_amdgcn_fmed3f(u[r], 0.f, -__builtin_inff())); m[r] = __builtin_amdgcn_fmed3f(t[r], 0.f, __builtin_inff()); }
    return (m + e) + 1e-6f;
}
__device__ __forceinline__ int lane_id() { int l; asm volatile("v_mbcnt_lo_u32_b32 %0, -1, 0\n\tv_mbcnt_hi_u32_b32 %0, -1, %0" : "=v"(l)); __builtin_assume(l >= 0 && l < 64); return l; }
#define LANES() const int lane = lane_id(), c = lane & 15, g = lane >> 4; (void)c; (void)g
__device__ __forceinline__ u32 ldsw(u32 a) { return *(volatile LAS u32*)(size_t)a; }
__device__ __forceinline__ void stsw(u32 a, u32 v) { *(volatile LAS u32*)(size_t)a = v; }
#define LDSR(addr, off) (*(LAS bf16x8*)((LAS char*)(size_t)(addr) + (off)))
__device__ __forceinline__ void lds_wait(u32 a, u32 target) {
    FENCE();
#pragma nounroll
    for (u32 n = 0; n < SPIN_CAP; ++n) { const u32 v = ldsw(a); if (!ANY(v < target)) break; }
    FENCE();
}
__device__ __forceinline__ void lds_wait_gt(u32 a, u32 low) {
    FENCE();
#pragma nounroll
    for (u32 n = 0; n < SPIN_CAP; ++n) { const u32 v = ldsw(a); if (!ANY(v <= low)) break; }
    FENCE();
}
__device__ __forceinline__ void lds_wait_sleep(u32 a, u32 target) {
    FENCE();
#pragma nounroll
    for (u32 n = 0; n < SPIN_CAP; ++n) { const u32 v = ldsw(a); if (!ANY(v < target)) break; __builtin_amdgcn_s_sleep(1); }
    FENCE();
}
#define DMA4(v0, v1, v2, v3, sbase, m0v)                                                                            \
    asm volatile("s_mov_b32 m0, %5\n\ts_nop 0\n\tglobal_load_lds_dwordx4 %0, %4\n\t"                                \
                 "global_load_lds_dwordx4 %1, %4 offset:1024\n\tglobal_load_lds_dwordx4 %2, %4 offset:2048\n\t"     \
                 "global_load_lds_dwordx4 %3, %4 offset:3072" ::"v"(v0), "v"(v1), "v"(v2), "v"(v3), "s"(sbase), "s"(m0v) : "memory")
#define DMA2(v0, v1, sbase, m0v)                                                                                    \
    asm volatile("s_mov_b32 m0, %3\n\ts_nop 0\n\tglobal_load_lds_dwordx4 %0, %2\n\t"                                \
                 "global_load_lds_dwordx4 %1, %2 offset:1024" ::"v"(v0), "v"(v1), "s"(sbase), "s"(m0v) : "memory")

template <int SLP>
__device__ __forceinline__ void touch_paced(const char* p, int n, u32 vo) {
    u32x4 sink = {0u, 0u, 0u, 0u};
#pragma nounroll
    for (int i = 0; i < n; ++i) {
        asm volatile("global_load_dwordx4 %0, %1, %2" : "+v"(sink) : "v"(vo), "s"(p + i * 1024));
        __builtin_amdgcn_s_sleep(SLP);
    }
    WAIT_V(0);
    PINV(sink);
}

__device__ __forceinline__ u32x4 tcol(const u32x2 (&ld)[8], int i) {
    const u32 sel = (i & 1) ? 0x07060302u : 0x05040100u;
    u32x4 o;
    o.x = __builtin_amdgcn_perm(ld[1][i >> 1], ld[0][i >> 1], sel);
    o.y = __builtin_amdgcn_perm(ld[3][i >> 1], ld[2][i >> 1], sel);
    o.z = __builtin_amdgcn_perm(ld[5][i >> 1], ld[4][i >> 1], sel);
    o.w = __builtin_amdgcn_perm(ld[7][i >> 1], ld[6][i >> 1], sel);
    return o;
}
__device__ __forceinline__ float sum8(u32x4 o) {
    return ((bflo(o.x) + bfhi(o.x)) + (bflo(o.y) + bfhi(o.y))) + ((bflo(o.z) + bfhi(o.z)) + (bflo(o.w) + bfhi(o.w)));
}

constexpr int K0_QKV = 72, K0_X = 64, K0_WIN = 0, K0_WO = 90, K0_GRID = 256;
static_assert((WS_END - WS_KVP) / 16 <= K0_GRID * 576, "k0 grid too small for the hand-off words");
#define K0ST(off, val) __builtin_amdgcn_raw_buffer_store_b128(val, rs0, off, 0, K0AUX)
__device__ __forceinline__ void k0_body(const u16* __restrict__ x, const u16* __restrict__ wqkv, const u16* __restrict__ wo,
                                        const u16* __restrict__ wout, char* __restrict__ ws) {
    __shared__ __attribute__((aligned(16))) char img[36864 + 576 * 8];
    const auto rs0 = __builtin_amdgcn_make_buffer_rsrc(ws, 0, WS_END, 0x00020000);
    const int tid = threadIdx.x, w = __builtin_amdgcn_readfirstlane(tid >> 6), lane = tid & 63, c = lane & 15, g = lane >> 4;
    int b = blockIdx.x;
    if (b < K0_QKV) {
        const int t = (b >> 3) % 3, kt = (b & 7) + 8 * (b / 24);
        if (kt >= 18) return;
        u32x2 ld[8];
#pragma unroll
        for (int j = 0; j < 8; ++j) ld[j] = NTL((const u32x2*)(wqkv + (size_t)(32 * kt + 8 * g + j) * 1728 + 576 * t + 64 * w + 4 * c));
        float* cst = (float*)(img + 36864);
#pragma unroll
        for (int i = 0; i < 4; ++i) {
            const int col = 64 * w + 4 * c + i, h = col / 18, d = col - 18 * h;
            const u32x4 o = tcol(ld, i);
            *(u32x4*)(img + (d < 16 ? h * 1024 + (d + 16 * g) * 16 : 32768 + (h >> 1) * 256 + g * 64 + (2 * (h & 1) + d - 16) * 16)) = o;
            float cs = sum8(o);
            cs += __shfl_xor(cs, 16); cs += __shfl_xor(cs, 32);
            if (g == 0) cst[(h >> 1) * 36 + (d < 16 ? (h & 1) * 16 + d : 32 + 2 * (h & 1) + d - 16)] = cs;
        }
        __syncthreads();
        const int rc = t == 1 ? 0 : (t == 2 ? 4 : 8);
        if (w < 8) {
#pragma unroll
            for (int k = 0; k < 4; ++k) {
                const int h = 4 * w + k;
                const u32 tile = t == 0 ? 92160 + (kt * 2 + (h & 1)) * 1024 : (kt * 5 + 2 * (t - 1) + (h & 1)) * 1024;
                K0ST(WS_WQ + (h >> 1) * 129024 + tile + lane * 16, *(const u32x4*)(img + h * 1024 + lane * 16));
            }
        } else {
#pragma unroll
            for (int k = 0; k < 4; ++k) {
                char* dst = ws + WS_WQ + (4 * k + g) * 129024 + (kt * 5 + 4) * 1024 + (c >> 2) * 256 + (rc + (c & 3)) * 16;
                *(u32x4*)dst = *(const u32x4*)(img + 32768 + k * 1024 + lane * 16);
                if (t == 0) *(u32x4*)(dst + 64) = (u32x4){0u, 0u, 0u, 0u};
            }
        }
        {
            const int p = tid / 36, e = tid - 36 * p;
            float* cq = (float*)(ws + WS_CSQ) + (kt * 16 + p) * 128;
            cq[e < 32 ? (t == 0 ? 80 : 32 * (t - 1)) + e : 64 + rc + e - 32] = cst[tid];
            if (t == 0 && e >= 32) cq[76 + e - 32] = 0.f;
        }
        return;
    }
    b -= K0_QKV;
    if (b < K0_X) {
        const int rl = tid / 18, t18 = tid - 18 * rl;
        u32x4 v[4];
#pragma unroll
        for (int i = 0; i < 4; ++i) v[i] = NTL((const u32x4*)(x + (size_t)(32 * b + rl) * 576 + (18 * i + t18) * 8));
        float s1 = 0.f, s2 = 0.f;
#pragma unroll
        for (int i = 0; i < 4; ++i) {
            const int ch = 18 * i + t18;
            *(u32x4*)(img + ((rl >> 4) * 18 + (ch >> 2)) * 1024 + ((rl & 15) + 16 * (ch & 3)) * 16) = v[i];
#pragma unroll
            for (int k = 0; k < 4; ++k) { const float a = bflo(v[i][k]), bb = bfhi(v[i][k]); s1 += a + bb; s2 += a * a + bb * bb; }
        }
        f32x2 o = {s1, s2};
        ((f32x2*)(img + 36864))[tid] = o;
        __syncthreads();
#pragma unroll
        for (int t = 0; t < 4; ++t) K0ST(WS_X + b * 36864 + (w * 4 + t) * 1024 + lane * 16, *(const u32x4*)(img + (w * 4 + t) * 1024 + lane * 16));
        if (tid < 256) {
            const int r = tid & 31, q = tid >> 5;
            const f32x2* pp = (const f32x2*)(img + 36864) + r * 18;
            o = pp[2 * q] + pp[2 * q + 1];
            if (q < 2) o += pp[16 + q];
            *(f32x2*)(ws + WS_XS + (q * 2048 + 32 * b + r) * 8) = o;
        }
        return;
    }
    b -= K0_X;
    if (b < K0_WO) {
        const bool iso = b < 18;
        const int sl = iso ? b : b - 18, G = sl / 9, t = sl - 9 * G;
        const u16* src = iso ? wo : wout;
        const int col = 64 * w + 4 * c, nt = col / 72, cl = col - 72 * nt;
        const int r0 = iso ? (sl < 16 ? 36 * sl : 288 * (sl - 16) + 16) : (t < 8 ? 288 * G + 144 * (t >> 2) + 32 * (t & 3) : 288 * G + 128);
        const int gs = (iso && sl >= 16) ? 72 : 4, s4 = iso ? (sl < 16 ? 18 : 36) : (t < 8 ? 16 : 144), s2 = (iso && sl >= 16) ? 18 : 2;
        u32x2 ld[8];
#pragma unroll
        for (int j = 0; j < 8; ++j) ld[j] = NTL((const u32x2*)(src + (size_t)(r0 + gs * g + s4 * (j >> 2) + s2 * ((j >> 1) & 1) + (j & 1)) * 576 + col));
        const int n = cl < 64 ? 2 * (cl >> 5) + ((cl >> 2) & 1) : 4, ii = cl < 64 ? 4 * ((cl >> 3) & 3) : cl - 64;
#pragma unroll
        for (int i = 0; i < 4; ++i) {
            const u32 off = n < 4 ? n * 1024 + (ii + i + 16 * g) * 16 : 4096 + g * 128 + (ii + i) * 16;
            *(u32x4*)(img + nt * 4608 + off) = tcol(ld, i);
        }
        __syncthreads();
#pragma unroll
        for (int k = 0; k < 4; ++k) {
            const u32 i = (w * 4 + k) * 1024 + lane * 16, n2 = i / 4608, off = i - 4608 * n2;
            K0ST((iso ? WS_WO + (n2 * 18 + sl) * 4608 : WS_WOUT + (n2 * 72 + sl) * 4608) + off, *(const u32x4*)(img + i));
        }
        return;
    }
}

__global__ __launch_bounds__(576) void k0_prep(const u16* __restrict__ x, const u16* __restrict__ wqkv, const u16* __restrict__ wo,
                                               const u16* __restrict__ wout, char* __restrict__ ws) {
    STAMP_DECL;
    STAMP(8, 0);
    const u32 gid = blockIdx.x * 576 + threadIdx.x;
    u32 warm = 0;
    if (blockIdx.x >= 240) {
        u64 pc; int dist;
        asm volatile("s_getpc_b64 %0\n\ts_mov_b32 %1, k9_end@rel32@lo+4" : "=&s"(pc), "=&s"(dist));
        if ((int)threadIdx.x * 128 < dist) warm |= *(const GAS u32*)(pc + threadIdx.x * 128);
    }
    if (gid < (WS_END - WS_KVP) / 16) {
        const auto rs = __builtin_amdgcn_make_buffer_rsrc(ws, 0, WS_END, 0x00020000);
        const u32 v = gid < (WS_CNT - WS_KVP) / 16 ? SENT : 0u;
        const u32x4 o = {v, v, v, v};
        BST128(o, rs, gid * 16, WS_KVP, 16);
    }
    k0_body(x, wqkv, wo, wout, ws);
    asm volatile("" :: "v"(warm));
    STAMP(8, 1);
    STAMP_OUT(11);
}

constexpr u32 LDS_FL = 158080, LDS_TOTAL = LDS_FL + 640;
typedef __amdgpu_buffer_rsrc_t rsrc_t;

__device__ __forceinline__ void x_ready_stats(char* ws, rsrc_t rs, u32 FL, bool fresh, int ver, int pm, int j, int lane, f32x2& mean, f32x2& var) {
    if (fresh) {
        lds_wait_sleep(FL + 120, 4u);
        __builtin_amdgcn_s_sleep(XRD);
        GAS u32* xm = (GAS u32*)(ws + WS_CNX + ((ver * 16 + pm) * 16 + j) * 128);
#pragma nounroll
        for (u32 n = 0; n < SPIN_CAP; ++n) { if (__builtin_amdgcn_readfirstlane(__hip_atomic_load(xm, RLX_AGENT)) == 0xffffu) break; }
        FENCE();
        stsw(FL + 124, 1u);
        FENCE();
    }
    f32x4 v[8];
#pragma unroll
    for (int k = 0; k < 8; ++k) v[k] = __builtin_bit_cast(f32x4, __builtin_amdgcn_raw_buffer_load_b128(rs, lane * 16, WS_XS + ver * XSB + (k * 2048 + 128 * pm) * 8, 16));
    const f32x4 sm = sadd4(sadd4(sadd4(v[0], v[1]), sadd4(v[2], v[3])), sadd4(sadd4(v[4], v[5]), sadd4(v[6], v[7])));
#pragma unroll
    for (int k = 0; k < 2; ++k) {
        mean[k] = sfma(sm[2 * k], 1.f / 576.f, 0.f);
        var[k] = fmaxf(sfma(-mean[k], mean[k], sfma(sm[2 * k + 1], 1.f / 576.f, 0.f)), 0.f) + 1e-5f;
    }
}

__device__ __forceinline__ void proj_store(const f32x4 (&y)[5], char* ws, rsrc_t rs, u16* out, u32 FLN, bool hand, int ver, int mt, int nt, int wid) {
    const int lane = lane_id();
    const u32 vo = lane * 16;
    const int c = lane & 15, g = lane >> 4;
    u32x4 o[2]; u32x2 o2;
#pragma unroll
    for (int i = 0; i < 2; ++i) o[i] = (u32x4){pk2(y[2 * i][0], y[2 * i][1]), pk2(y[2 * i][2], y[2 * i][3]), pk2(y[2 * i + 1][0], y[2 * i + 1][1]), pk2(y[2 * i + 1][2], y[2 * i + 1][3])};
    o2 = (u32x2){pk2(y[4][0], y[4][1]), pk2(y[4][2], y[4][3])};
    if (ver == 4) {
        u16* op = out + (size_t)(64 * mt + 16 * wid + c) * 576 + 72 * nt;
        *(u32x4*)(op + 8 * g) = o[0];
        *(u32x4*)(op + 32 + 8 * g) = o[1];
        if (g < 2) *(u32x2*)(op + 64 + 4 * g) = o2;
    } else {
        const u32 xo = WS_X + ver * XB + (4 * mt + wid) * 18432 + 2304 * nt;
        BST128(o[0], rs, vo, xo, 16);
        BST128(o[1], rs, vo + 1024, xo, 16);
        if (g < 2) __builtin_amdgcn_raw_buffer_store_b64(o2, rs, 2048 + c * 16 + g * 8, xo, 16); else o2 = (u32x2){0u, 0u};
        float s1 = 0.f, s2 = 0.f;
#pragma unroll
        for (int k = 0; k < 10; ++k) { const u32 wv = k < 8 ? o[k >> 2][k & 3] : o2[k & 1]; const float u = bflo(wv), v = bfhi(wv); s1 += u + v; s2 += u * u + v * v; }
        auto r1 = __builtin_amdgcn_permlane32_swap(F2U(s1), F2U(s2), false, false);
        const float t = U2F(r1[0]) + U2F(r1[1]);
        auto r2 = __builtin_amdgcn_permlane16_swap(F2U(t), F2U(t), false, false);
        const float tt = U2F(r2[0]) + U2F(r2[1]);
        if ((g & 1) == 0) __builtin_amdgcn_raw_buffer_store_b32(F2U(tt), rs, c * 8 + (g >> 1) * 4, WS_XS + ver * XSB + (nt * 2048 + 64 * mt + 16 * wid) * 8, 16);
        if (hand) {
            WAIT_V(0);
            u32 old = 0;
            if (lane == 0) old = __hip_atomic_fetch_add((LAS u32*)(size_t)(FLN + 120), 1u, __ATOMIC_RELAXED, __HIP_MEMORY_SCOPE_WORKGROUP);
            if (__builtin_amdgcn_readfirstlane(old) == 3 && lane < 16)
                __hip_atomic_fetch_add((GAS u32*)(ws + WS_CNX + ((ver * 16 + (mt >> 1)) * 16 + lane) * 128), 1u << (8 * (mt & 1) + nt), RLX_AGENT);
        }
    }
}

__device__ __forceinline__ void proj_tail(f32x4 (&ac)[4][5], const u32x4 (&res)[2], u32x2 res2, u32 lds, u32 wstride, u32 flags, char* ws, rsrc_t rs, u16* out,
                                          u32 FLN, bool hand, int ver, int mt, int nt, int wid) {
    const int lane = lane_id();
    const u32 vo = lane * 16;
#pragma unroll
    for (int m = 1; m < 4; ++m)
#pragma unroll
        for (int n = 0; n < 5; ++n) *(LAS f32x4*)(size_t)(lds + wid * wstride + vo + ((m - 1) * 5 + n) * 1024) = ac[m][n];
    FENCE();
    stsw(flags + wid * 4, 1u);
#pragma unroll
    for (int i = 0; i < 4; ++i) {
        const u32 w0 = res[i >> 1][2 * (i & 1)], w1 = res[i >> 1][2 * (i & 1) + 1];
        ac[0][i] += (f32x4){bflo(w0), bfhi(w0), bflo(w1), bfhi(w1)};
        PINV(ac[0][i]);
    }
    ac[0][4] += (f32x4){bflo(res2[0]), bfhi(res2[0]), bflo(res2[1]), bfhi(res2[1])};
    PINV(ac[0][4]);
    lds_wait(flags + (lane & 3) * 4, 1u);
#pragma unroll
    for (int n = 0; n < 5; ++n)
        ac[0][n] = (ac[0][n] + *(LAS f32x4*)(size_t)(lds + (wid ^ 1) * wstride + vo + n * 1024)) +
                   (*(LAS f32x4*)(size_t)(lds + (wid ^ 2) * wstride + vo + (5 + n) * 1024) + *(LAS f32x4*)(size_t)(lds + (wid ^ 3) * wstride + vo + (10 + n) * 1024));
    proj_store(ac[0], ws, rs, out, FLN, hand, ver, mt, nt, wid);
}

constexpr u32 K1_PW = 129024, K1_OP = K1_PW + 4 * 176 * 16, K1_ST = K1_OP + 1536, K1_CS = K1_ST + 1024, K1_RT = K1_CS + 512;

__device__ __forceinline__ void phase_a(char* ws, const u16* win, u32 lds, int wid, int layer, bool fresh, bool hand) {
    int bid = blockIdx.x;
    GAS char* wsg = (GAS char*)ws;
    asm volatile("" : "+s"(bid), "+s"(wsg), "+s"(wid));
    ws = (char*)wsg;
    const int xcd = bid & 7, bi = bid >> 3;
    const int pm = MAP1 ? (xcd & 3) * 4 + (bi & 3) : (xcd & 1) * 8 + (bi & 7), p = MAP1 ? (xcd >> 2) * 8 + (bi >> 2) : (xcd >> 1) * 4 + (bi >> 3);
    const u32 FL = lds + LDS_FL + 256 * layer;
    const auto rs = __builtin_amdgcn_make_buffer_rsrc(ws, 0, WS_END, 0x00020000);
    STAMP_DECL;
    if (wid == 4) {
        LANES();
        const char* src = ws + WS_WQ + p * 129024;
        const u32 vo = lane * 16;
#define K1_U10(u) do { DMA4(vo, vo, vo, vo, src + (u) * 10240, lds + (u) * 10240); DMA4(vo, vo, vo, vo, src + (u) * 10240 + 4096, lds + (u) * 10240 + 4096); \
                       DMA2(vo, vo, src + (u) * 10240 + 8192, lds + (u) * 10240 + 8192); } while (0)
#define K1_U12(u) do { DMA4(vo, vo, vo, vo, src + 92160 + (u) * 12288, lds + 92160 + (u) * 12288); DMA4(vo, vo, vo, vo, src + 92160 + (u) * 12288 + 4096, lds + 92160 + (u) * 12288 + 4096); \
                       DMA4(vo, vo, vo, vo, src + 92160 + (u) * 12288 + 8192, lds + 92160 + (u) * 12288 + 8192); } while (0)
        if (fresh) lds_wait_sleep(FL + 120, 4u);
        K1_U10(0);
        if (!fresh) BAR();
        K1_U10(1); K1_U10(2);
#pragma unroll
        for (int u = 3; u < 9; ++u) { WAIT_V(20); stsw(FL, u - 2); FENCE(); K1_U10(u); }
        WAIT_V(20); stsw(FL, 7); FENCE(); K1_U12(0);
        WAIT_V(22); stsw(FL, 8); FENCE(); K1_U12(1);
        WAIT_V(24); stsw(FL, 9); FENCE(); K1_U12(2);
        WAIT_V(24); stsw(FL, 10); FENCE();
        WAIT_V(12); stsw(FL, 11); FENCE();
        WAIT_V(0); stsw(FL, 12);
        const bool rt = layer == 0 && bid < 216;
        const int cb = bid / 3, k0 = 6 * (bid - 3 * cb);
        u32x2 ld[6][8];
        if (rt) {
#pragma unroll
            for (int i = 0; i < 6; ++i)
#pragma unroll
                for (int j = 0; j < 8; ++j) ld[i][j] = NTL((const u32x2*)(win + (size_t)(32 * (k0 + i) + 8 * g + j) * 4608 + 64 * cb + 4 * c));
            SB();
        }
        lds_wait_sleep(FL + 16 + (lane & 3) * 4, 2u);
        if (WOG && fresh) lds_wait_sleep(FL + 32 + (lane % 3) * 4, 1u);
        src = ws + WS_WO + (p & 7) * 82944;
        WAIT_V(12);
#pragma unroll
        for (int u = 0; u < 8; ++u) { K1_U10(u); if (u >= 4) WAIT_V(40); }
        asm volatile("s_mov_b32 m0, %2\n\ts_nop 0\n\tglobal_load_lds_dwordx4 %0, %1" ::"v"(vo), "s"(src + 81920), "s"(lds + 81920) : "memory");
        WAIT_V(0); stsw(FL, 13);
        __builtin_amdgcn_s_sleep(RTD);
        float cs[4] = {0.f, 0.f, 0.f, 0.f};
        u32x4 rb[6][4];
        if (rt) {
            const bf2_t one2 = __builtin_bit_cast(bf2_t, 0x3f803f80u);
#pragma unroll
            for (int i = 0; i < 6; ++i) {
#pragma unroll
                for (int e = 0; e < 4; ++e) {
                    const u32x4 o = tcol(ld[i], e);
                    *(LAS u32x4*)(size_t)(lds + K1_RT + (c >> 2) * 1024 + (4 * (c & 3) + e + 16 * g) * 16) = o;
#pragma unroll
                    for (int k = 0; k < 4; ++k) { const u32 wv = o[k]; cs[e] = __builtin_amdgcn_fdot2_f32_bf16(__builtin_bit_cast(bf2_t, wv), one2, cs[e], false); }
                }
                FENCE();
#pragma unroll
                for (int t = 0; t < 4; ++t) rb[i][t] = *(LAS u32x4*)(size_t)(lds + K1_RT + t * 1024 + vo);
                FENCE();
            }
#pragma unroll
            for (int i = 0; i < 24; ++i) PINV(rb[i >> 2][i & 3]);
        }
        if (rt) {
#pragma unroll
            for (int i = 0; i < 6; ++i)
#pragma unroll
                for (int t = 0; t < 4; ++t) BST128(rb[i][t], rs, vo, WS_WIN + ((4 * cb + t) * 18 + k0 + i) * 1024, RTAUX);
#pragma unroll
            for (int e = 0; e < 4; ++e) {
                auto r1 = __builtin_amdgcn_permlane32_swap(F2U(cs[e]), F2U(cs[e]), false, false);
                const float t1 = sfma(U2F(r1[0]), 1.f, U2F(r1[1]));
                float t2 = t1;
                PINV(t2);
                asm volatile("s_nop 1");
                auto r2 = __builtin_amdgcn_permlane16_swap(F2U(t2), F2U(t2), false, false);
                cs[e] = sfma(U2F(r2[0]), 1.f, U2F(r2[1]));
            }
            const u32x4 co = {F2U(cs[0]), F2U(cs[1]), F2U(cs[2]), F2U(cs[3])};
            if (g == 0) BST128(co, rs, c * 16, WS_CSIN + ((k0 / 6) * 4608 + 64 * cb) * 4, 16);
            WAIT_V(0);
            if (lane == 0) __hip_atomic_fetch_add((GAS u32*)(ws + WS_CNW + cb * 128), 1u, RLX_AGENT);
        }
        return;
    }
    if (wid > 4) {
        LANES();
        const int ht = (wid - 5) * 64 + lane;
        if (!fresh) BAR();
        if (wid == 5) {
            f32x2 mean, var, r, mr;
            x_ready_stats(ws, rs, FL, fresh, 2 * layer, pm, p, lane, mean, var);
#pragma unroll
            for (int k = 0; k < 2; ++k) { r[k] = __builtin_amdgcn_rsqf(var[k]); float t2 = -mean[k] * r[k]; PINV(t2); mr[k] = t2; }
            *(LAS f32x2*)(size_t)(lds + K1_ST + lane * 8) = r;
            *(LAS f32x2*)(size_t)(lds + K1_ST + 512 + lane * 8) = mr;
        } else if (wid == 7) {
            const f32x2* cp = (const f32x2*)(ws + WS_CSQ) + p * 64 + min(lane, 55);
            f32x2 q[18];
#pragma unroll
            for (int k = 0; k < 18; ++k) q[k] = cp[k * 1024];
            float q0 = q[0][0], q1 = q[0][1];
#pragma unroll
            for (int k = 1; k < 18; ++k) { q0 = sfma(q[k][0], 1.f, q0); q1 = sfma(q[k][1], 1.f, q1); }
            if (fresh) lds_wait_sleep(FL + 120, 4u);
            *(LAS f32x2*)(size_t)(lds + K1_CS + lane * 8) = (f32x2){q0, q1};
        }
        FENCE();
        stsw(FL + 4 + (wid - 5) * 4, 1u);
        __builtin_amdgcn_s_setprio(3);
        lds_wait_sleep(FL + 16 + (lane & 3) * 4, 1u);
        STAMP(layer * 4 + 0, 7);
        const int it = ht < 173 ? ht : 172;
        f32x4 v = *(LAS f32x4*)(size_t)(lds + K1_PW + it * 16);
#pragma unroll
        for (int k = 1; k < 4; ++k) v = sadd4(v, *(LAS f32x4*)(size_t)(lds + K1_PW + k * 2816 + it * 16));
        const u32 gbase = WS_KVP + (layer * 16 + p) * 16 * 2816;
        {
            u32x4 o = __builtin_bit_cast(u32x4, v);
#pragma unroll
            for (int k = 0; k < 4; ++k) o[k] = o[k] == SENT ? 0x7fc00000u : o[k];
            if (ht < 173) BST128(o, rs, it * 16, gbase + pm * 2816, 16);
        }
        u32x4 t[16];
        STAMP(layer * 4 + 0, 8); STAMP(layer * 4 + 0, 9);
        __builtin_amdgcn_s_sleep(XDELAY1);
#pragma unroll
        for (int k = 0; k < 16; ++k) t[k] = __builtin_amdgcn_raw_buffer_load_b128(rs, it * 16, gbase + k * 2816, 16);
        u32 mx = 0, spins = 0;
#pragma unroll
        for (int k = 0; k < 16; ++k) mx = max(max(mx, max(t[k][0], t[k][1])), max(t[k][2], t[k][3]));
        if (__builtin_expect(ANY(mx == SENT), 0)) {
#pragma nounroll
            for (spins = 1; spins < SPIN_CAP; ++spins) {
                FENCE();
                u32 miss = 0;
#pragma unroll
                for (int k = 0; k < 16; ++k) miss |= ANY(max(max(t[k][0], t[k][1]), max(t[k][2], t[k][3])) == SENT) ? 1u << k : 0u;
                if (!miss) break;
#pragma unroll
                for (int k = 0; k < 16; ++k) if (miss >> k & 1) t[k] = __builtin_amdgcn_raw_buffer_load_b128(rs, it * 16, gbase + k * 2816, 16);
            }
        }
        STAMP(layer * 4 + 0, 10); STAMPV(layer * 4 + 0, 15, spins);
        v = __builtin_bit_cast(f32x4, t[0]);
#pragma unroll
        for (int k = 1; k < 16; ++k) v = sadd4(v, __builtin_bit_cast(f32x4, t[k]));
        u32x2 lh = {pk2(v[0], v[1]), pk2(v[2], v[3])};
        if (ht >= 173) lh = (u32x2){0u, 0u};
        *(LAS u32x2*)(size_t)(lds + K1_OP + ht * 8) = lh;
        FENCE();
        stsw(FL + 32 + (wid - 5) * 4, 1u);
        __builtin_amdgcn_s_setprio(0);
        if (wid != 5) return;
        STAMP_OUT(layer * 4 + 0);
        lds_wait_sleep(FL + 48, 4u);
        __builtin_amdgcn_s_sleep(ODELAY);
        GAS u32* om = (GAS u32*)(ws + WS_CNO + ((layer * 16 + pm) * 16 + p) * 128);
#pragma nounroll
        for (u32 n = 0; n < SPIN_CAP; ++n) { if (__builtin_amdgcn_readfirstlane(__hip_atomic_load(om, RLX_AGENT)) == 0xffffu) break; }
        FENCE();
        stsw(FL + 52, 1u);
        return;
    }
    LANES();
    STAMP(layer * 4 + 0, 0);

    const u32 xb = WS_X + 2 * layer * XB + (8 * pm + 2 * wid) * 18432, vo = lane * 16;
    bf16x8 xa[2][18];
#define K1_LX(m, kt) xa[m][kt] = __builtin_bit_cast(bf16x8, __builtin_amdgcn_raw_buffer_load_b128(rs, vo, xb + ((m) * 18 + (kt)) * 1024, 16))
    if (fresh) lds_wait(FL + 124, 1u);
#pragma unroll
    for (int kt = 0; kt < 6; ++kt) { K1_LX(0, kt); SB(); K1_LX(1, kt); SB(); }
    f32x4 acc[2][5];
#pragma unroll
    for (int f = 0; f < 5; ++f) { acc[0][f] = acc[1][f] = (f32x4){0.f, 0.f, 0.f, 0.f}; PINV(acc[0][f]); PINV(acc[1][f]); }
    u32 wb0 = lds + lane * 16, wb1 = wb0 + 61440, wb2 = wb0 + 92160;
    PINV(wb0); PINV(wb1); PINV(wb2);
#define K1_W1(kt, f) ((kt) < 12 ? LDSR(wb0, ((kt) * 5 + (f)) * 1024) : LDSR(wb1, (((kt) - 12) * 5 + (f)) * 1024))
    if (!fresh) BAR();
    lds_wait(FL, 1u);
    STAMP(layer * 4 + 0, 1);
    bf16x8 wf[2][5];
#pragma unroll
    for (int f = 0; f < 5; ++f) { wf[0][f] = K1_W1(0, f); SB(); }
#pragma unroll
    for (int f = 0; f < 5; ++f) { wf[1][f] = K1_W1(1, f); SB(); }
    u32 fl = 0;
#pragma unroll
    for (int kt = 0; kt < 18; ++kt) {
        if ((kt & 1) == 0 && kt + 2 < 18) { if (__builtin_expect(ANY(fl < (u32)(kt / 2 + 2)), 0)) lds_wait(FL, kt / 2 + 2); FENCE(); }
#pragma unroll
        for (int f = 0; f < 5; ++f) {
            MFMA(acc[0][f], xa[0][kt], wf[kt & 1][f]);
            if (f == 1 && kt + 6 < 18) K1_LX(0, kt + 6);
            if (f == 3 && kt + 6 < 18) K1_LX(1, kt + 6);
            if (f == 0 && (kt & 1) && kt < 16) { FENCE(); fl = ldsw(FL); FENCE(); }
            SB();
            MFMA(acc[1][f], xa[1][kt], wf[kt & 1][f]);
            if (kt + 2 < 18) wf[kt & 1][f] = K1_W1(kt + 2, f);
            SB();
        }
    }
    asm volatile("s_nop 15" ::: "memory");
    STAMP(layer * 4 + 0, 2);
    lds_wait(FL + 4 + (lane % 3) * 4, 1u);
    {
        u32 op[5][4], rk[4];
        float cs1[5];
#pragma unroll
        for (int f = 0; f < 5; ++f) cs1[f] = *(LAS float*)(size_t)(lds + K1_CS + f * 64 + c * 4);
#pragma unroll
        for (int m = 0; m < 2; ++m) {
            const f32x4 rr = *(LAS f32x4*)(size_t)(lds + K1_ST + (32 * wid + 16 * m + 4 * g) * 4);
            const f32x4 mm = *(LAS f32x4*)(size_t)(lds + K1_ST + 512 + (32 * wid + 16 * m + 4 * g) * 4);
#pragma unroll
            for (int f = 0; f < 5; ++f) {
                f32x4 v = rr * acc[m][f] + mm * cs1[f];
                if (f < 2) v = phi4(v);
                if (f == 4) { const f32x4 u = phi4(v); rk[2 * m] = pk2(u[0], u[1]); rk[2 * m + 1] = pk2(u[2], u[3]); }
                op[f][2 * m] = pk2(v[0], v[1]); op[f][2 * m + 1] = pk2(v[2], v[3]);
                if (f == 4 && c >= 12) { op[f][2 * m] = 0x3f803f80u; op[f][2 * m + 1] = 0x3f803f80u; }
            }
        }
        const bf16x8 KAa = mk8(op[0][0], op[0][1], op[0][2], op[0][3]), KAb = mk8(op[1][0], op[1][1], op[1][2], op[1][3]);
        const bf16x8 VBa = mk8(op[2][0], op[2][1], op[2][2], op[2][3]), VBb = mk8(op[3][0], op[3][1], op[3][2], op[3][3]);
        const bf16x8 RV = mk8(op[4][0], op[4][1], op[4][2], op[4][3]), RK = mk8(rk[0], rk[1], rk[2], rk[3]);
        const f32x4 z4 = {0.f, 0.f, 0.f, 0.f};
        const f32x4 T1a = MFMAB(KAa, VBa, z4), T1b = MFMAB(KAb, VBb, z4), T2a = MFMAB(KAa, RV, z4), T2b = MFMAB(KAb, RV, z4);
        const f32x4 T3a = MFMAB(RK, VBa, z4), T3b = MFMAB(RK, VBb, z4), T4 = MFMAB(RK, RV, z4);
        const u32 pw = lds + K1_PW + wid * 2816;
        *(LAS f32x4*)(size_t)(pw + lane * 16) = T1a;
        *(LAS f32x4*)(size_t)(pw + 1024 + lane * 16) = T1b;
        const f32x4 u3 = {T3a[0], T3a[1], T3b[2], T3b[3]};
        *(LAS f32x4*)(size_t)(pw + (g == 0 ? 128 + c : 175) * 16) = u3;
        *(LAS f32x4*)(size_t)(pw + (c == 12 ? 152 + g : ((c & 14) == 4 ? 144 + (c - 4) * 4 + g : 175)) * 16) = T2a;
        *(LAS f32x4*)(size_t)(pw + (c == 12 ? 164 + g : ((c & 14) == 6 ? 156 + (c - 6) * 4 + g : 175)) * 16) = T2b;
        *(LAS f32x4*)(size_t)(pw + (lane == 12 ? 172 : ((lane & 60) == 4 ? 164 + lane : 175)) * 16) = T4;
        FENCE();
        stsw(FL + 16 + wid * 4, 1u);
    }
    STAMP(layer * 4 + 0, 3);
    f32x4 qa[2][3];
#pragma unroll
    for (int f = 0; f < 3; ++f) { qa[0][f] = qa[1][f] = (f32x4){0.f, 0.f, 0.f, 0.f}; PINV(qa[0][f]); PINV(qa[1][f]); }
#define K1_W2(kt, f) ((f) < 2 ? LDSR(wb2, ((kt) * 2 + (f)) * 1024) : K1_W1(kt, 4))
    lds_wait(FL, 10u);
    bf16x8 wq[3][3];
#pragma unroll
    for (int k = 0; k < 3; ++k)
#pragma unroll
        for (int f = 0; f < 3; ++f) { wq[k][f] = K1_W2(k, f); SB(); }
#pragma unroll
    for (int kt = 0; kt < 18; ++kt) {
        if (kt == 3) lds_wait(FL, 11u);
        if (kt == 9) lds_wait(FL, 12u);
#pragma unroll
        for (int f = 0; f < 3; ++f) {
            MFMA(qa[0][f], wq[kt % 3][f], xa[0][kt]);
            SB();
            MFMA(qa[1][f], wq[kt % 3][f], xa[1][kt]);
            if (kt + 3 < 18) wq[kt % 3][f] = K1_W2(kt + 3, f);
            SB();
        }
    }
    asm volatile("s_nop 15" ::: "memory");
    STAMP(layer * 4 + 0, 4);
    stsw(FL + 16 + wid * 4, 2u);
    f32x4 cs2[3];
#pragma unroll
    for (int f = 0; f < 3; ++f) cs2[f] = *(LAS f32x4*)(size_t)(lds + K1_CS + (f < 2 ? 5 + f : 4) * 64 + g * 16);
    bf16x8 PQ[2][2];
#pragma unroll
    for (int m = 0; m < 2; ++m) {
        const float rr = *(LAS float*)(size_t)(lds + K1_ST + (32 * wid + 16 * m + c) * 4), mm = *(LAS float*)(size_t)(lds + K1_ST + 512 + (32 * wid + 16 * m + c) * 4);
        u32 w2[3][2];
#pragma unroll
        for (int f = 0; f < 3; ++f) {
            const f32x4 v = phi4(qa[m][f] * rr + cs2[f] * mm);
            w2[f][0] = pk2(v[0], v[1]); w2[f][1] = pk2(v[2], v[3]);
        }
        if (g != 2) { w2[2][0] = 0u; w2[2][1] = 0u; }
        PQ[m][0] = mk8(w2[0][0], w2[0][1], w2[2][0], w2[2][1]);
        PQ[m][1] = mk8(w2[1][0], w2[1][1], w2[2][0], w2[2][1]);
        PINV(PQ[m][0]); PINV(PQ[m][1]);
    }
    u32 ga[6];
    ga[0] = lane; ga[1] = g == 2 ? 128 + c : 175;
    ga[2] = c < 2 ? 144 + 4 * c + g : ((c & 3) == 2 ? 152 + g : 175);
    ga[3] = (c & 14) == 4 ? 156 + 4 * (c - 4) + g : ((c & 3) == 3 ? 164 + g : 175);
    ga[4] = g != 2 ? 175 : (c < 2 ? 168 + c : ((c & 3) == 2 ? 172 : 175));
    ga[5] = g != 2 ? 175 : ((c & 14) == 4 ? 166 + c : ((c & 3) == 3 ? 172 : 175));
#pragma unroll
    for (int i = 0; i < 6; ++i) { ga[i] = lds + K1_OP + ga[i] * 8; PINV(ga[i]); }
    const u32 ob = WS_O + LSH * layer * XB + (8 * pm + 2 * wid) * 18432;
    u32 orem = (16 + (p >> 3)) * 1024 + (c + 16 * ((p & 7) >> 1)) * 16 + (p & 1) * 8 + g * 4;
    PINV(orem);
    lds_wait(FL + 32 + (lane % 3) * 4, 1u);
    STAMP(layer * 4 + 0, 5);
    {
        const u32x2 l0 = *(LAS u32x2*)(size_t)ga[0], l1 = *(LAS u32x2*)(size_t)(ga[0] + 512), h01 = *(LAS u32x2*)(size_t)ga[1];
        const u32x2 l2 = *(LAS u32x2*)(size_t)ga[2], l3 = *(LAS u32x2*)(size_t)ga[3];
        const u32 h2 = *(LAS u32*)(size_t)ga[4], h3 = *(LAS u32*)(size_t)(ga[5] + 4);
        const bf16x8 A1a = mk8(l0[0], l0[1], h01[0], 0u), A1b = mk8(l1[0], l1[1], 0u, h01[1]), A2a = mk8(l2[0], l2[1], h2, 0u), A2b = mk8(l3[0], l3[1], 0u, h3);
        const f32x4 z4 = {0.f, 0.f, 0.f, 0.f};
        f32x4 O1a[2], O1b[2], O2[2];
#pragma unroll
        for (int m = 0; m < 2; ++m) O2[m] = MFMAB(A2a, PQ[m][0], z4);
#pragma unroll
        for (int m = 0; m < 2; ++m) O2[m] = MFMAB(A2b, PQ[m][1], O2[m]);
#pragma unroll
        for (int m = 0; m < 2; ++m) { O1a[m] = MFMAB(A1a, PQ[m][0], z4); O1b[m] = MFMAB(A1b, PQ[m][1], z4); }
#pragma unroll
        for (int m = 0; m < 2; ++m) {
            const float ia = __builtin_amdgcn_rcpf(O2[m][2] + 1e-6f), ib = __builtin_amdgcn_rcpf(O2[m][3] + 1e-6f);
            const f32x4 ya = O1a[m] * ia, yb = O1b[m] * ib;
            const u32x4 o = {pk2(ya[0], ya[1]), pk2(ya[2], ya[3]), pk2(yb[0], yb[1]), pk2(yb[2], yb[3])};
            BST128(o, rs, vo, ob + m * 18432 + p * 1024, 16);
            const float ir = g == 0 ? ia : ib;
            if (g < 2) __builtin_amdgcn_raw_buffer_store_b32(pk2(O2[m][0] * ir, O2[m][1] * ir), rs, orem, ob + m * 18432, 16);
        }
    }
    STAMP(layer * 4 + 0, 6);
    STAMP_OUT(layer * 4 + 0);
    WAIT_V(0);
    {
        u32 old = 0;
        if (lane == 0) old = __hip_atomic_fetch_add((LAS u32*)(size_t)(FL + 48), 1u, __ATOMIC_RELAXED, __HIP_MEMORY_SCOPE_WORKGROUP);
        if (__builtin_amdgcn_readfirstlane(old) == 3 && lane < 16)
            __hip_atomic_fetch_add((GAS u32*)(ws + WS_CNO + ((layer * 16 + pm) * 16 + lane) * 128), 1u << p, RLX_AGENT);
    }
    STAMP(layer * 4 + 1, 0);
    const int mt = 2 * pm + (p >> 3), nt = p & 7;
    const u32 vo4 = 4096 + g * 128 + (lane & 7) * 16;
#define K2_LD(v, sof, aux) __builtin_bit_cast(bf16x8, __builtin_amdgcn_raw_buffer_load_b128(rs, v, sof, aux))
    bf16x8 fw[6][5], fa[18];
    f32x4 ac[5];
    {
        const u32 xo = WS_X + 2 * layer * XB + (4 * mt + wid) * 18432 + 2304 * nt;
        const u32x4 r0 = __builtin_amdgcn_raw_buffer_load_b128(rs, vo, xo, 16), r1 = __builtin_amdgcn_raw_buffer_load_b128(rs, vo + 1024, xo, 16);
        const u32x2 r2 = __builtin_amdgcn_raw_buffer_load_b64(rs, 2048 + c * 16 + (g & 1) * 8, xo, 16);
        ac[0] = (f32x4){bflo(r0[0]), bfhi(r0[0]), bflo(r0[1]), bfhi(r0[1])}; ac[1] = (f32x4){bflo(r0[2]), bfhi(r0[2]), bflo(r0[3]), bfhi(r0[3])};
        ac[2] = (f32x4){bflo(r1[0]), bfhi(r1[0]), bflo(r1[1]), bfhi(r1[1])}; ac[3] = (f32x4){bflo(r1[2]), bfhi(r1[2]), bflo(r1[3]), bfhi(r1[3])};
        ac[4] = (f32x4){bflo(r2[0]), bfhi(r2[0]), bflo(r2[1]), bfhi(r2[1])};
    }
    u32 wl[2] = {lds + vo, lds + vo + 41472}, wl4[2] = {lds + vo4, lds + vo4 + 41472};
    PINV(wl[0]); PINV(wl[1]); PINV(wl4[0]); PINV(wl4[1]);
#define K2_W(s, n) ((n) < 4 ? LDSR(wl[(s) / 9], ((s) % 9) * 4608 + (n) * 1024) : LDSR(wl4[(s) / 9], ((s) % 9) * 4608))
    lds_wait(FL, 13u);
#pragma unroll
    for (int sl = 0; sl < 6; ++sl)
#pragma unroll
        for (int n = 0; n < 5; ++n) { fw[sl][n] = K2_W(sl, n); SB(); }
#pragma unroll
    for (int n = 0; n < 5; ++n) PINV(ac[n]);
    lds_wait(FL + 52, 1u);
    STAMP(layer * 4 + 1, 1);
#pragma unroll
    for (int sl = 0; sl < 18; ++sl) { fa[sl] = K2_LD(vo + (sl & 3) * 1024, WS_O + LSH * layer * XB + ((4 * mt + wid) * 18 + (sl & ~3)) * 1024, 16); SB(); }
#pragma unroll
    for (int sl = 0; sl < 18; ++sl)
#pragma unroll
        for (int n = 0; n < 5; ++n) {
            MFMA(ac[n], fw[sl % 6][n], fa[sl]);
            SB();
            if (sl + 6 < 18) fw[sl % 6][n] = K2_W(sl + 6, n);
            SB();
        }
    asm volatile("s_nop 15" ::: "memory");
    STAMP(layer * 4 + 1, 3);
    proj_store(ac, ws, rs, nullptr, FL + 128, hand, 2 * layer + 1, mt, nt, wid);
    STAMP(layer * 4 + 1, 2);
    STAMP_OUT(layer * 4 + 1);
}

constexpr u32 STAGE = 18432, NS = 6;
constexpr u32 K3_PK = NS * STAGE, K3_PP = 304, K3_WS = K3_PK + 128 * K3_PP, K3_S1 = K3_WS + 4096, K3_S2 = K3_S1 + 2048, K3_CS = K3_S2 + 1024;
static_assert(K3_CS + 1280 <= LDS_FL, "flag blocks overlap");

__device__ __forceinline__ void phase_b(char* ws, const u16* wsp, const u16* bsp, u16* out, u32 lds, int wid, int layer, bool hand) {
    int bid = blockIdx.x;
    GAS char* wsg = (GAS char*)ws;
    asm volatile("" : "+s"(bid), "+s"(wsg), "+s"(wid));
    ws = (char*)wsg;
    const int xcd = bid & 7, bi = bid >> 3;
    const int pm = MAP3 ? (xcd & 3) * 4 + (bi & 3) : (xcd & 1) * 8 + (bi & 7), pn = MAP3 ? (xcd >> 2) * 8 + (bi >> 2) : (xcd >> 1) * 4 + (bi >> 3);
    const u32 FL = lds + LDS_FL + 256 * layer + 128;
    STAMP_DECL;
    const auto rs = __builtin_amdgcn_make_buffer_rsrc(ws, 0, WS_END, 0x00020000);
    if (wid == 4) {
        LANES();
        u32 ao[16];
        const u32 vo = lane * 16;
        {
            const u32 xw = WS_WIN + (9 * pn + 8) * 18432, xa = WS_X + (2 * layer + 1) * XB + 8 * pm * 18432;
            lds_wait_sleep(FL + 16 + (lane & 3) * 4, 2u);
            u32x4 xc[6][2];
#pragma unroll
            for (int i = 0; i < 12; ++i) { xc[i >> 1][i & 1] = __builtin_amdgcn_raw_buffer_load_b128(rs, vo, xw + 6144 + i * 1024, 16); SB(); }
#pragma unroll
            for (int i = 0; i < 6; ++i) {
                lds_wait_sleep(FL + 16 + (lane & 3) * 4, 4 + i);
                *(LAS u32x4*)(size_t)(lds + ((i + 3) % 6) * STAGE + 16384 + vo) = xc[i][0];
                *(LAS u32x4*)(size_t)(lds + ((i + 3) % 6) * STAGE + 17408 + vo) = xc[i][1];
                FENCE();
                stsw(FL, 10 + i);
                FENCE();
            }
            u32x4 xt[3][18];
#pragma unroll
            for (int i = 0; i < 3; ++i)
#pragma unroll
                for (int q = 0; q < 18; ++q) { xt[i][q] = __builtin_amdgcn_raw_buffer_load_b128(rs, vo, (q < 16 ? xa + (q >> 1) * 18432 : xw) + i * 2048 + (q & 1) * 1024, 16); SB(); }
#pragma unroll
            for (int i = 0; i < 3; ++i) {
                lds_wait_sleep(FL + 16 + (lane & 3) * 4, 10 + i);
#pragma unroll
                for (int q = 0; q < 18; ++q) *(LAS u32x4*)(size_t)(lds + (i + 3) * STAGE + q * 1024 + vo) = xt[i][q];
                FENCE();
                stsw(FL, 16 + i);
                FENCE();
            }
        }
        u32 slot = 0;
#define K3_ISSUE() do { const u32 dst = lds + slot * STAGE;                                                          \
            DMA4(ao[0], ao[1], ao[2], ao[3], ap, dst); DMA4(ao[4], ao[5], ao[6], ao[7], ap, dst + 4096);             \
            DMA4(ao[8], ao[9], ao[10], ao[11], ap, dst + 8192); DMA4(ao[12], ao[13], ao[14], ao[15], ap, dst + 12288); } while (0)
#pragma unroll
        for (int q = 0; q < 16; ++q) { ao[q] = (q * 8 + (lane >> 3)) * 4096 + (((lane & 7) ^ ((q & 1) * 4 + g)) * 16) - (q & 3) * 1024 + 4096; PINV(ao[q]); }
        const char* ap = (const char*)wsp + (size_t)pm * 128 * 4096 - 4096;
        lds_wait_sleep(FL + 16 + (lane & 3) * 4, LGATE2);
        for (int s = 18; s < 53; ++s) {
            if (s >= 21) {
                if (s <= 50) WAIT_V(32); else if (s == 51) WAIT_V(16); else WAIT_V(0);
                stsw(FL, s - 2);
                FENCE();
            }
            if (s < 50) {
                lds_wait_sleep(FL + 16 + (lane & 3) * 4, s - NS + 1);
                K3_ISSUE();
                ap += AFAKE ? 0 : (s == 33 ? 128 - 15 * 256 : 256);
                slot = slot + 1 == NS ? 0 : slot + 1;
            }
        }
        lds_wait_sleep(FL + 80, 2u);
        for (int i = 0; i < 8; ++i) {
            const int r = i < 2 ? 6 + i : (i < 6 ? i : i - 6);
            if (i >= 2) lds_wait_sleep(FL + 16 + (lane & 3) * 4, 43 + i);
            if (WGATE && i == WGI) lds_wait_sleep(FL + 92, WGATE);
            const char* wp = ws + WS_WOUT + ((MAP5 ? pn & 7 : pn >> 1) * 72 + 18 * (r >> 1) + 4 * (r & 1)) * 4608;
            const u32 dst = lds + r * STAGE;
            DMA4(vo, vo, vo, vo, wp, dst); DMA4(vo, vo, vo, vo, wp + 4096, dst + 4096); DMA4(vo, vo, vo, vo, wp + 8192, dst + 8192);
            DMA4(vo, vo, vo, vo, wp + 12288, dst + 12288); DMA2(vo, vo, wp + 16384, dst + 16384);
            if (i >= 2) WAIT_V(36);
        }
        WAIT_V(0);
        stsw(FL, 100u);
        return;
    }
    if (wid > 4) {
        LANES();
        if (wid == 5) {
            f32x2 mean, var;
            x_ready_stats(ws, rs, FL, true, 2 * layer + 1, pm, pn, lane, mean, var);
#pragma unroll
            for (int k = 0; k < 2; ++k) {
                float r = __builtin_amdgcn_rsqf(var[k]);
                r = fmaxf(r, 0.f);
                const f32x4 o = {-mean[k], sfma(r, -2.302208198f, 0.f), sfma(sfma(sfma(r, r, 0.f), r, 0.f), -0.1029432397f, 0.f), sfma(var[k], r, 0.f)};
                *(LAS f32x4*)(size_t)(lds + K3_S1 + (2 * lane + k) * 16) = o;
            }
        } else if (wid == 7) {
            if (layer == 0) {
                GAS u32* wm = (GAS u32*)(ws + WS_CNW + (((144 * pn + min(64 * (lane & 3), 143)) >> 6) + 36 * ((lane >> 2) & 1)) * 128);
                __builtin_amdgcn_s_sleep(WRD);
#pragma nounroll
                for (u32 n = 0; n < SPIN_CAP; ++n) { if (!ANY(__hip_atomic_load(wm, RLX_AGENT) < 3u)) break; __builtin_amdgcn_s_sleep(8); }
                FENCE();
            }
            stsw(FL + 116, 1u);
            FENCE();
            const int i = min(lane, 35);
#define K3_CSL(o) __builtin_bit_cast(f32x4, __builtin_amdgcn_raw_buffer_load_b128(rs, i * 16, WS_CSIN + (36 * pn + (o)) * 16, 16))
            const f32x4 a0 = K3_CSL(576), a1 = K3_CSL(576 + 1152), a2 = K3_CSL(576 + 2304), b0 = K3_CSL(0), b1 = K3_CSL(1152), b2 = K3_CSL(2304);
            *(LAS f32x4*)(size_t)(lds + K3_CS + i * 16) = sadd4(sadd4(a0, a1), a2);
            *(LAS f32x4*)(size_t)(lds + K3_CS + 640 + i * 16) = sadd4(sadd4(b0, b1), b2);
        }
        FENCE();
        stsw(FL + 32 + (wid - 5) * 4, 1u);
        u32 vo = lane * 16;
        PINV(vo);
        GAS u32* flg = (GAS u32*)(ws + WS_CNT + (layer * 16 + pn) * 4096);
        HST_DECL;
        if (wid != 5) {
            const int h = wid - 6;
            u32 ta = lds + K3_PK + (32 * h + 8 * g + (c >> 2)) * K3_PP + 16 * (c & 3), tx = ta + 256 - 8 * (c & 3), s2r = lds + K3_S2 + 128 * h + 32 * g;
            PINV(ta); PINV(tx); PINV(s2r);
            {
                const u32 xa = WS_X + (2 * layer + 1) * XB + (8 * pm + 4 * h) * 18432, xw = WS_WIN + (144 + 9 * pn + 8) * 18432 + h * 1024;
                u32x4 xt[4][9];
#define K3_STG(i) do { _Pragma("unroll") for (int q = 0; q < 9; ++q) {                                               \
                    xt[(i) & 3][q] = __builtin_amdgcn_raw_buffer_load_b128(rs, vo, (q < 8 ? xa + (q >> 1) * 18432 + (q & 1) * 1024 : xw) + (i) * 2048, 16); SB(); } } while (0)
                lds_wait_sleep(FL + 116, 1u);
                lds_wait_sleep(FL + 120, 4u);
                lds_wait(FL + 124, 1u);
                K3_STG(0); K3_STG(1); K3_STG(2); K3_STG(3);
#pragma unroll
                for (int i = 0; i < 9; ++i) {
                    if (i >= 6) lds_wait_sleep(FL + 16 + (lane & 3) * 4, i - 5);
#pragma unroll
                    for (int q = 0; q < 9; ++q) *(LAS u32x4*)(size_t)(lds + (i % 6) * STAGE + (q < 8 ? h * 8192 + q * 1024 : 16384 + h * 1024) + vo) = xt[i & 3][q];
                    FENCE();
                    stsw(FL + 8 + 4 * h, i < 8 ? i + 1 : 1000u);
                    FENCE();
                    if (i + 4 < 9) K3_STG(i + 4);
                }
            }
            const u32 zob = WS_Z + LSH * layer * ZB + (9 * pn * 64 + 2 * pm + h) * 1024;
            if (layer == 0) {
                if (h == 1) lds_wait_sleep(FL + 16 + (lane & 3) * 4, TG1); else lds_wait_sleep(FL + 16 + (lane & 3) * 4, TG0);
                touch_paced<TS3>((const char*)wsp + bid * 32768 + h * 16384, 16, vo);
            }
            lds_wait_sleep(FL + 48 + (lane & 3) * 4, 1u);
            HST(0);
            if (pm < HPRIO) __builtin_amdgcn_s_setprio(3);
            float zf[18][8];
#define K3_TR(fi, A, O) do { u32x2 t0_, t1_, t2_, t3_;                                                               \
                asm volatile("ds_read_b64_tr_b16 %0, %4 offset:%5\n\tds_read_b64_tr_b16 %1, %4 offset:%6\n\tds_read_b64_tr_b16 %2, %4 offset:%7\n\t" \
                             "ds_read_b64_tr_b16 %3, %4 offset:%8\n\ts_waitcnt lgkmcnt(0)" : "=&v"(t0_), "=&v"(t1_), "=&v"(t2_), "=&v"(t3_) \
                             : "v"(A), "i"(O), "i"((O) + 4 * K3_PP), "i"((O) + 64 * K3_PP), "i"((O) + 68 * K3_PP) : "memory"); \
                const u32 w_[8] = {t0_[0], t0_[1], t1_[0], t1_[1], t2_[0], t2_[1], t3_[0], t3_[1]};                  \
                _Pragma("unroll") for (int i_ = 0; i_ < 8; ++i_) { zf[2 * (fi) + (i_ >> 2)][2 * (i_ & 3)] = bflo(w_[i_]); zf[2 * (fi) + (i_ >> 2)][2 * (i_ & 3) + 1] = bfhi(w_[i_]); \
                    PINV(zf[2 * (fi) + (i_ >> 2)][2 * (i_ & 3)]); PINV(zf[2 * (fi) + (i_ >> 2)][2 * (i_ & 3) + 1]); } } while (0)
            K3_TR(0, ta, 0); K3_TR(1, ta, 8); K3_TR(2, ta, 64); K3_TR(3, ta, 72); K3_TR(4, ta, 128); K3_TR(5, ta, 136); K3_TR(6, ta, 192); K3_TR(7, ta, 200);
            K3_TR(8, tx, 0);
            HST(1);
            lds_wait_sleep(FL + 64, 1u);
            for (int i = 2; i < pm; ++i) __builtin_amdgcn_s_sleep(STAG);
            HST(2);
            f32x4 sv[2][4];
#pragma unroll
            for (int q = 0; q < 2; ++q)
#pragma unroll
                for (int k = 0; k < 4; ++k) sv[q][k] = *(LAS f32x4*)(size_t)(s2r + (k >> 1) * 512 + 256 * q + (k & 1) * 16);
#pragma unroll
            for (int ii = 0; ii < 18; ++ii) {
                const int q = ii / 9, i = 2 * (ii % 9) + q;
                u32x4 o;
                asm("v_fma_f32 %4, %4, %12, %20\n\tv_fma_f32 %5, %5, %13, %21\n\tv_fma_f32 %6, %6, %14, %22\n\tv_fma_f32 %7, %7, %15, %23\n\t"
                    "v_fma_f32 %8, %8, %16, %24\n\tv_fma_f32 %9, %9, %17, %25\n\tv_fma_f32 %10, %10, %18, %26\n\tv_fma_f32 %11, %11, %19, %27\n\t"
                    "v_cvt_pk_bf16_f32 %0, %4, %5\n\tv_cvt_pk_bf16_f32 %1, %6, %7\n\tv_cvt_pk_bf16_f32 %2, %8, %9\n\tv_cvt_pk_bf16_f32 %3, %10, %11"
                    : "=&v"(o[0]), "=&v"(o[1]), "=&v"(o[2]), "=&v"(o[3]), "+v"(zf[i][0]), "+v"(zf[i][1]), "+v"(zf[i][2]), "+v"(zf[i][3]), "+v"(zf[i][4]), "+v"(zf[i][5]), "+v"(zf[i][6]), "+v"(zf[i][7])
                    : "v"(sv[q][0][0]), "v"(sv[q][0][1]), "v"(sv[q][0][2]), "v"(sv[q][0][3]), "v"(sv[q][1][0]), "v"(sv[q][1][1]), "v"(sv[q][1][2]), "v"(sv[q][1][3]),
                      "v"(sv[q][2][0]), "v"(sv[q][2][1]), "v"(sv[q][2][2]), "v"(sv[q][2][3]), "v"(sv[q][3][0]), "v"(sv[q][3][1]), "v"(sv[q][3][2]), "v"(sv[q][3][3]));
                __builtin_amdgcn_raw_buffer_store_b128(o, rs, vo, zob + q * 32768 + (i >> 1) * 65536, 16);
                SB();
                if (ii % 9 == 8) {
                    if (q == 0) HST(3);
                    WAIT_V(0);
                    if (q == 0) HST(4);
                    u32 old = 0;
                    if (lane == 0) old = __hip_atomic_fetch_add((LAS u32*)(size_t)(FL + 76 + 4 * q), 1u, __ATOMIC_RELAXED, __HIP_MEMORY_SCOPE_WORKGROUP);
                    if (__builtin_amdgcn_readfirstlane(old) == 1 && lane < 16)
                        __hip_atomic_fetch_add(flg + lane * 64, 1u << (16 * q + pm), RLX_AGENT);
                    if (q == 0) { __builtin_amdgcn_s_setprio(0); __builtin_amdgcn_s_sleep(STAG2); }
                    SB();
                }
            }
            HST(5);
            if (wid == 6) HST_OUT(9 + layer);
            return;
        }
        u32 wsa = lds + K3_WS + lane * 16, s2w = lds + K3_S2 + lane * 8;
        PINV(wsa); PINV(s2w);
        const u32 zsb = WS_ZST + (layer * 16 + pm) * 16384;
        lds_wait_sleep(FL + 16 + (lane & 3) * 4, 9u);
        __builtin_amdgcn_s_sleep(POLLD);
        __builtin_amdgcn_s_setprio(3);
        lds_wait(FL + 48 + (lane & 3) * 4, 1u);
        STAMP(layer * 4 + 2, 8);
        {
            const f32x4 st = sadd4(sadd4(*(LAS f32x4*)(size_t)wsa, *(LAS f32x4*)(size_t)(wsa + 1024)), sadd4(*(LAS f32x4*)(size_t)(wsa + 2048), *(LAS f32x4*)(size_t)(wsa + 3072)));
            u32x4 o = __builtin_bit_cast(u32x4, st);
#pragma unroll
            for (int k = 0; k < 4; ++k) o[k] = o[k] == SENT ? 0x7fc00000u : o[k];
            BST128(o, rs, vo, zsb + pn * 1024, 16);
            __builtin_amdgcn_s_sleep(XDELAY3);
            u32x4 t[16];
#pragma unroll
            for (int k = 0; k < 16; ++k) t[k] = __builtin_amdgcn_raw_buffer_load_b128(rs, vo + (k & 3) * 1024, zsb + (k >> 2) * 4096, 16);
            u32 mx = 0;
#pragma unroll
            for (int k = 0; k < 16; ++k) mx = max(max(mx, max(t[k][0], t[k][1])), max(t[k][2], t[k][3]));
            u32 spins = 0;
            if (__builtin_expect(ANY(mx == SENT), 0)) {
#pragma nounroll
                for (spins = 1; spins < SPIN_CAP; ++spins) {
                    FENCE();
                    u32 miss = 0;
#pragma unroll
                    for (int k = 0; k < 16; ++k) miss |= ANY(max(max(t[k][0], t[k][1]), max(t[k][2], t[k][3])) == SENT) ? 1u << k : 0u;
                    if (!miss) break;
#pragma unroll
                    for (int k = 0; k < 16; ++k) if (miss >> k & 1) t[k] = __builtin_amdgcn_raw_buffer_load_b128(rs, vo + (k & 3) * 1024, zsb + (k >> 2) * 4096, 16);
                }
            }
            f32x4 sa[4];
#pragma unroll
            for (int k = 0; k < 4; ++k)
                sa[k] = sadd4(sadd4(__builtin_bit_cast(f32x4, t[4 * k]), __builtin_bit_cast(f32x4, t[4 * k + 1])), sadd4(__builtin_bit_cast(f32x4, t[4 * k + 2]), __builtin_bit_cast(f32x4, t[4 * k + 3])));
            const f32x4 ss = sadd4(sadd4(sa[0], sa[1]), sadd4(sa[2], sa[3]));
            f32x2 rr, mr;
#pragma unroll
            for (int k = 0; k < 2; ++k) {
                const float m2 = sfma(ss[2 * k], 1.f / 2304.f, 0.f);
                float v2 = fmaxf(sfma(-m2, m2, sfma(ss[2 * k + 1], 1.f / 2304.f, 0.f)), 0.f) + 1e-5f;
                PINV(v2);
                rr[k] = __builtin_amdgcn_rsqf(v2);
                float t2 = -m2 * rr[k];
                PINV(t2);
                mr[k] = t2;
            }
            *(LAS f32x2*)(size_t)s2w = rr;
            *(LAS f32x2*)(size_t)(s2w + 512) = mr;
            FENCE();
            stsw(FL + 64, 1u);
            FENCE();
        }
        STAMP(layer * 4 + 2, 9);
        STAMP(layer * 4 + 2, 10);
        u32 ready = 0;
#define K3_NEED(n) do { _Pragma("nounroll") for (u32 spins_ = 0; ready < (u32)(n) && spins_ < SPIN_CAP; ++spins_) {   \
            const u32 v_ = __builtin_amdgcn_readfirstlane(__hip_atomic_load(flg + pm * 64, RLX_AGENT));                                               \
            ready = __builtin_ctzll(~(u64)v_); if (ready < (u32)(n)) __builtin_amdgcn_s_sleep(PSL); } FENCE(); } while (0)
        __builtin_amdgcn_s_sleep(FDELAY);
        K3_NEED(3);
        stsw(FL + 84, 1u);
        FENCE();
        STAMP(layer * 4 + 2, 11);
        STAMP_OUT(layer * 4 + 2);
        __builtin_amdgcn_s_setprio(0);
        const u32 zx = WS_Z + LSH * layer * ZB + (9 * pn + 8) * 65536;
        u32x4 xr[4][2];
#pragma unroll
        for (int k = 0; k < 3; ++k) {
            xr[k][0] = __builtin_amdgcn_raw_buffer_load_b128(rs, vo, zx + k * 2048, 16);
            xr[k][1] = __builtin_amdgcn_raw_buffer_load_b128(rs, vo + 1024, zx + k * 2048, 16);
            SB();
        }
        u32 slot = 0;
        for (int s0 = 0; s0 < 32; s0 += 4) {
#pragma unroll
            for (int k = 0; k < 4; ++k) {
                const int s = s0 + k;
                lds_wait_sleep(FL + 16 + (lane & 3) * 4, 18 + s - NS + 1);
                *(LAS u32x4*)(size_t)(lds + slot * STAGE + 16384 + lane * 16) = xr[k][0];
                *(LAS u32x4*)(size_t)(lds + slot * STAGE + 17408 + lane * 16) = xr[k][1];
                FENCE();
                stsw(FL + 4, 18 + s + 1);
                FENCE();
                const u32 sn = s + 3 < 32 ? s + 3 : 31;
                if (LAZY && ready < sn + 1) lds_wait_sleep(FL + 16 + (lane & 3) * 4, 18 + s - LAZY);
                K3_NEED(sn + 1);
                xr[(k + 3) & 3][0] = __builtin_amdgcn_raw_buffer_load_b128(rs, vo, zx + sn * 2048, 16);
                xr[(k + 3) & 3][1] = __builtin_amdgcn_raw_buffer_load_b128(rs, vo + 1024, zx + sn * 2048, 16);
                slot = slot + 1 == NS ? 0 : slot + 1;
            }
        }
        lds_wait_sleep(FL + 92, 4u);
        __builtin_amdgcn_s_sleep(HDELAY);
        GAS u32* hm = (GAS u32*)(ws + WS_CNT + (layer * 16 + pm) * 4096 + pn * 256 + 128);
#pragma nounroll
        for (u32 spins = 0; spins < SPIN_CAP; ++spins) {
            const u32 v = __builtin_amdgcn_readfirstlane(__hip_atomic_load(hm, RLX_AGENT));
            FENCE();
            stsw(FL + 88, v);
            if (v == 0xffffu) break;
        }
        return;
    }
    LANES();
    STAMP(layer * 4 + 2, 0);
    u32 vA = lane * 16, vB = lane * 16 + 18432;
    u32 so = WS_WIN + (144 + 9 * pn + 2 * wid) * 18432;
    bf16x8 bb[6][2];
#define BLD(voff, imm, aux) __builtin_bit_cast(bf16x8, __builtin_amdgcn_raw_buffer_load_b128(rs, (voff) + (imm), so, aux))
#define BPRO(aux) do { _Pragma("unroll") for (int r = 0; r < 5; ++r) { if (r == 3) so += 3072;                       \
        bb[r][0] = BLD(vA, (r % 3) * 1024, aux); SB(); bb[r][1] = BLD(vB, (r % 3) * 1024, aux); SB(); } } while (0)
    lds_wait(FL + 116, 1u);
    BPRO(16);
    u32 pa[2][4];
#pragma unroll
    for (int q = 0; q < 4; ++q) { pa[0][q] = lds + lane * 16 + ((q ^ wid) * 4096); pa[1][q] = pa[0][q] + 1024; }
    u32 px = lds + 16384 + lane * 16;
    u32 myfree = FL + 16 + wid * 4, fladdr = FL + (lane & 3) * 4, cnt = 0;
    PINV(cnt); PINV(myfree); PINV(fladdr);
    int dstep = 3 * STAGE;
    f32x4 acc[8][2], accx[2];
    bf16x8 a[8], xf;
#define ALOAD() do { a[0] = LDSR(pa[0][0], 0); SB(); xf = LDSR(px, 0); SB();                                         \
        _Pragma("unroll") for (int m = 1; m < 8; ++m) { a[m] = LDSR(pa[0][m >> 1], (m & 1) * 2048); SB(); } } while (0)
#define MFS(acc, x, y) do { MFMA(acc, x, y); SB(); } while (0)
#define HALF(r, RD, kn, sf, rl, imm, aux, E0, E1, E2, E3, E4, E5) do {                                               \
        WCNT(8, 5); MFS(acc[0][0], bb[r][0], a[0]); E0; SB();                                                       \
        MFS(acc[0][1], bb[r][1], a[0]); E1; SB();                                                                   \
        MFS(accx[0], xf, a[0]); if (RD) a[0] = LDSR(pa[kn][0], (sf)); SB();                                         \
        MFS(accx[1], xf, a[1]); if (RD) xf = LDSR(px, (sf) + (kn) * 1024); SB();                                    \
        MFS(acc[1][1], bb[r][1], a[1]); bb[rl][0] = BLD(vA, imm, aux); SB();                                        \
        MFS(acc[1][0], bb[r][0], a[1]); if (RD) a[1] = LDSR(pa[kn][0], (sf) + 2048); SB();                          \
        MFS(acc[2][0], bb[r][0], a[2]); E2; SB();                                                                   \
        MFS(acc[2][1], bb[r][1], a[2]); if (RD) a[2] = LDSR(pa[kn][1], (sf)); SB();                                 \
        WCNT(63, 5); MFS(acc[3][1], bb[r][1], a[3]); SB();                                                          \
        MFS(acc[3][0], bb[r][0], a[3]); if (RD) a[3] = LDSR(pa[kn][1], (sf) + 2048); SB();                          \
        MFS(acc[4][0], bb[r][0], a[4]); bb[rl][1] = BLD(vB, imm, aux); SB();                                        \
        MFS(acc[4][1], bb[r][1], a[4]); if (RD) a[4] = LDSR(pa[kn][2], (sf)); SB();                                 \
        MFS(acc[5][1], bb[r][1], a[5]); E3; SB();                                                                   \
        MFS(acc[5][0], bb[r][0], a[5]); if (RD) a[5] = LDSR(pa[kn][2], (sf) + 2048); SB();                          \
        MFS(acc[6][0], bb[r][0], a[6]); E4; SB();                                                                   \
        MFS(acc[6][1], bb[r][1], a[6]); if (RD) a[6] = LDSR(pa[kn][3], (sf)); SB();                                 \
        MFS(acc[7][1], bb[r][1], a[7]); E5; SB();                                                                   \
        MFS(acc[7][0], bb[r][0], a[7]); if (RD) a[7] = LDSR(pa[kn][3], (sf) + 2048); SB(); } while (0)
#define STEP(u, chk, nso, aux) do { u32 fl_; u64 msk_;                                                               \
        HALF(2 * (u), 1, 1, (u) * STAGE, (2 * (u) + 5) % 6, ((2 * (u) + 5) % 3) * 1024, aux,                         \
             { if ((u) == 2) so += 3072; }, { cnt += 1; PINV(cnt); }, { FENCE(); fl_ = ldsw(fladdr); FENCE(); },     \
             { if ((u) == 2) { pa[0][0] += dstep; pa[0][1] += dstep; PINV(pa[0][0]); PINV(pa[0][1]); } },            \
             { if ((u) == 2) { pa[0][2] += dstep; pa[0][3] += dstep; PINV(pa[0][2]); PINV(pa[0][3]); } },            \
             { asm volatile("v_cmp_le_u32 %0, %1, %2" : "=s"(msk_) : "v"(fl_), "v"(cnt)); if ((u) == 2) { px += dstep; PINV(px); } }); \
        HALF(2 * (u) + 1, 1, 0, (((u) + 1) % 3) * STAGE, (2 * (u) + 6) % 6, ((2 * (u) + 6) % 3) * 1024, aux,         \
             { if (__builtin_expect(msk_ != 0, 0)) { if (chk) lds_wait_gt(fladdr, cnt); } FENCE(); }, { if ((u) == 0) so = (nso); }, \
             { FENCE(); stsw(myfree, cnt); FENCE(); },                                                               \
             { if ((u) == 2) { pa[1][0] += dstep; pa[1][1] += dstep; PINV(pa[1][0]); PINV(pa[1][1]); } },            \
             { if ((u) == 2) { pa[1][2] += dstep; pa[1][3] += dstep; PINV(pa[1][2]); PINV(pa[1][3]); } },            \
             { if ((u) == 2) dstep = -dstep; }); } while (0)
#define PINACC() do { _Pragma("unroll") for (int m = 0; m < 8; ++m) { PINV(acc[m][0]); PINV(acc[m][1]); } PINV(accx[0]); PINV(accx[1]); asm volatile("s_nop 1"); } while (0)
    u32 yp[18][2], early = 0;
    const u32 s1a = lds + K3_S1 + c * 16;
#pragma unroll
    for (int ph = 0; ph < 2; ++ph) {
        if (ph == 0) {
#pragma unroll
            for (int m = 0; m < 8; ++m) { acc[m][0] = acc[m][1] = (f32x4){0.f, 0.f, 0.f, 0.f}; if (m < 2) accx[m] = acc[m][0]; }
        } else {
            const u32 ca = lds + K3_CS + ph * 640 + g * 16;
            const f32x4 c0 = *(LAS f32x4*)(size_t)(ca + wid * 128), c1 = *(LAS f32x4*)(size_t)(ca + wid * 128 + 64), c2 = *(LAS f32x4*)(size_t)(ca + 512);
#pragma unroll
            for (int m = 0; m < 8; ++m) {
                const float nm = *(LAS float*)(size_t)(s1a + ((((m >> 1) ^ wid) << 1) + (m & 1)) * 256);
                acc[m][0] = c0 * nm; acc[m][1] = c1 * nm;
                if (m < 2) accx[m] = c2 * nm;
            }
        }
        PINACC();
        if (ph == 0) { lds_wait(fladdr, 1u); ALOAD(); STAMP(layer * 4 + 2, 1); }
        const u32 nxt = WS_WIN + (9 * pn + 2 * wid) * 18432;
        for (int j = 0; j < 3; ++j) {
            STEP(0, true, j == 2 - ph ? nxt + 6144 * (1 - ph) : so + 3072, 16);
            STEP(1, true, 0, 16);
            STEP(2, ph == 0 || j < 2, 0, 16);
        }
        asm volatile("s_nop 15" ::: "memory");
        STAMP(layer * 4 + 2, 2 + 2 * ph);
        if (ph == 1) {
            vB = lane * 16 + 65536;
            so = WS_Z + LSH * layer * ZB + (9 * pn + 2 * wid) * 65536;
            FENCE();
            early = __builtin_amdgcn_readfirstlane(ldsw(FL + 84));
            FENCE();
            if (early) BPRO(16);
        }
        float t1[8], t2[8];
        f32x4 rcs[8], cz[3];
        if (ph == 0) {
            lds_wait(FL + 32 + (lane % 3) * 4, 1u);
            const u32 ca = lds + K3_CS + g * 16;
            cz[0] = *(LAS f32x4*)(size_t)(ca + wid * 128); cz[1] = *(LAS f32x4*)(size_t)(ca + wid * 128 + 64); cz[2] = *(LAS f32x4*)(size_t)(ca + 512);
        }
#pragma unroll
        for (int m = 0; m < 8; ++m) rcs[m] = *(LAS f32x4*)(size_t)(s1a + ((((m >> 1) ^ wid) << 1) + (m & 1)) * 256);
#pragma unroll
        for (int m = 0; m < 8; ++m) {
            if (ph == 1 && m == 4 && !early) { FENCE(); early = __builtin_amdgcn_readfirstlane(ldsw(FL + 84)); FENCE(); if (early) BPRO(16); }
            constexpr int NP = 6;
            const int np = m < 2 ? 6 : 4;
            const f32x2 k1 = {rcs[m][1], rcs[m][1]}, k3 = {rcs[m][2], rcs[m][2]}, sg = {rcs[m][3], rcs[m][3]};
            f32x2 v[NP], e[NP];
#pragma unroll
            for (int i = 0; i < NP; ++i) if (i < np) {
                const f32x4 av = i < 4 ? acc[m][i >> 1] : accx[m & 1];
                v[i] = (f32x2){av[2 * (i & 1)], av[2 * (i & 1) + 1]};
                if (ph == 0) v[i] += (f32x2){cz[i >> 1][2 * (i & 1)], cz[i >> 1][2 * (i & 1) + 1]} * (f32x2){rcs[m][0], rcs[m][0]};
            }
#pragma unroll
            for (int i = 0; i < NP; ++i) if (i < np) e[i] = v[i] * v[i];
#pragma unroll
            for (int i = 0; i < NP; ++i) if (i < np) e[i] = e[i] * k3 + k1;
#pragma unroll
            for (int i = 0; i < NP; ++i) if (i < np) e[i] = e[i] * v[i];
            SB();
#pragma unroll
            for (int i = 0; i < NP; ++i) if (i < np) { e[i][0] = __builtin_amdgcn_exp2f(e[i][0]); e[i][1] = __builtin_amdgcn_exp2f(e[i][1]); }
            SB();
#pragma unroll
            for (int i = 0; i < NP; ++i) if (i < np) e[i] = e[i] * sg + sg;
            SB();
#pragma unroll
            for (int i = 0; i < NP; ++i) if (i < np) { e[i][0] = __builtin_amdgcn_rcpf(e[i][0]); e[i][1] = __builtin_amdgcn_rcpf(e[i][1]); }
            SB();
#pragma unroll
            for (int i = 0; i < NP; ++i) if (i < np) v[i] = v[i] * e[i];
#pragma unroll
            for (int i = 0; i < NP; ++i) if (i < np) yp[i < 4 ? 2 * m + (i >> 1) : 16 + m][i & 1] = pk2(v[i][0], v[i][1]);
            if (ph == 0) {
                f32x2 u1 = (v[0] + v[1]) + (v[2] + v[3]), u2 = v[0] * v[0], u3 = v[1] * v[1];
                u2 = v[2] * v[2] + u2; u3 = v[3] * v[3] + u3;
                if (m < 2) { u1 += v[4] + v[5]; u2 = v[4] * v[4] + u2; u3 = v[5] * v[5] + u3; }
                u2 += u3;
                t1[m] = u1[0] + u1[1]; t2[m] = u2[0] + u2[1];
            }
            PINV(yp[2 * m][0]); PINV(yp[2 * m][1]); PINV(yp[2 * m + 1][0]); PINV(yp[2 * m + 1][1]);
            if (m < 2) { PINV(yp[16 + m][0]); PINV(yp[16 + m][1]); }
            if (ph == 0) {
                const u32 row = (((((m >> 1) ^ wid) << 1) + (m & 1)) << 4) + c;
                u32x4 o = {yp[2 * m][0], yp[2 * m][1], yp[2 * m + 1][0], yp[2 * m + 1][1]};
                *(LAS u32x4*)(size_t)(lds + K3_PK + row * K3_PP + 64 * wid + 16 * g) = o;
                if (m < 2) { u32x2 ox = {yp[16 + m][0], yp[16 + m][1]}; *(LAS u32x2*)(size_t)(lds + K3_PK + row * K3_PP + 256 + 8 * g) = ox; }
            }
        }
        if (ph == 0) {
#pragma unroll
            for (int m = 0; m < 4; ++m) {
                auto r1 = __builtin_amdgcn_permlane32_swap(F2U(t1[m]), F2U(t1[m + 4]), false, false);
                auto r2 = __builtin_amdgcn_permlane32_swap(F2U(t2[m]), F2U(t2[m + 4]), false, false);
                t1[m] = U2F(r1[0]) + U2F(r1[1]); t2[m] = U2F(r2[0]) + U2F(r2[1]);
            }
#pragma unroll
            for (int m = 0; m < 2; ++m) {
                auto r1 = __builtin_amdgcn_permlane16_swap(F2U(t1[m]), F2U(t1[m + 2]), false, false);
                auto r2 = __builtin_amdgcn_permlane16_swap(F2U(t2[m]), F2U(t2[m + 2]), false, false);
                f32x2 o = {U2F(r1[0]) + U2F(r1[1]), U2F(r2[0]) + U2F(r2[1])};
                *(LAS f32x2*)(size_t)(lds + K3_WS + (wid * 128 + 32 * (g ^ wid) + 16 * m + c) * 8) = o;
            }
            FENCE();
            stsw(FL + 48 + wid * 4, 1u);
            FENCE();
        }
        STAMP(layer * 4 + 2, 3 + 2 * ph);
    }
    {
        float bv[8];
#pragma unroll
        for (int m = 0; m < 8; ++m) bv[m] = bflo(bsp[128 * pm + (((((m >> 1) ^ wid) << 1) + (m & 1)) << 4) + c]);
#pragma unroll
        for (int m = 0; m < 8; ++m) {
            acc[m][0] = acc[m][1] = (f32x4){bv[m], bv[m], bv[m], bv[m]};
            if (m < 2) accx[m] = acc[m][0];
        }
    }
#pragma unroll
    for (int q = 0; q < 4; ++q) { pa[0][q] = lds + (c >> 3) * 1024 + (c & 7) * 128 + ((g ^ ((c >> 1) & 7)) * 16) + ((q ^ wid) * 4096); pa[1][q] = pa[0][q] ^ 64; }
    if (!early) { lds_wait(FL + 84, 1u); BPRO(16); }
    lds_wait(fladdr, 19u);
    STAMP(layer * 4 + 2, 6);
    ALOAD();
    PINACC();
    for (int j = 0; j < 10; ++j) {
        STEP(0, true, so + BADV, BAUX);
        STEP(1, true, 0, BAUX);
        STEP(2, true, 0, BAUX);
    }
    if (BADV > 0) so -= 8192;
    STEP(0, true, so, 16);
    STEP(1, false, 0, 16);
    asm volatile("s_nop 15" ::: "memory");
    STAMP(layer * 4 + 2, 7);
    {
        const u32 hb = WS_H + LSH * layer * ZB + (8 * pm * 8 + (pn >> 1)) * 9216;
#pragma unroll
        for (int m = 0; m < 8; ++m) {
            const int mt = (((m >> 1) ^ wid) << 1) + (m & 1);
            u32x4 o;
            o[0] = pk2(bflo(yp[2 * m][0]) * acc[m][0][0], bfhi(yp[2 * m][0]) * acc[m][0][1]);
            o[1] = pk2(bflo(yp[2 * m][1]) * acc[m][0][2], bfhi(yp[2 * m][1]) * acc[m][0][3]);
            o[2] = pk2(bflo(yp[2 * m + 1][0]) * acc[m][1][0], bfhi(yp[2 * m + 1][0]) * acc[m][1][1]);
            o[3] = pk2(bflo(yp[2 * m + 1][1]) * acc[m][1][2], bfhi(yp[2 * m + 1][1]) * acc[m][1][3]);
            BST128(o, rs, vA, hb + mt * 73728 + ((pn & 1) * 4 + wid) * 1024, 16);
            if (m < 2) {
                u32x2 ox = {pk2(bflo(yp[16 + m][0]) * accx[m][0], bfhi(yp[16 + m][0]) * accx[m][1]), pk2(bflo(yp[16 + m][1]) * accx[m][2], bfhi(yp[16 + m][1]) * accx[m][3])};
                __builtin_amdgcn_raw_buffer_store_b64(ox, rs, vA, hb + mt * 73728 + 8192 + (pn & 1) * 8, 16);
            }
        }
        STAMP(layer * 4 + 2, 12);
        WAIT_V(0);
        u32 old = 0;
        if (lane == 0) old = __hip_atomic_fetch_add((LAS u32*)(size_t)(FL + 92), 1u, __ATOMIC_RELAXED, __HIP_MEMORY_SCOPE_WORKGROUP);
        if (__builtin_amdgcn_readfirstlane(old) == 3 && lane < 16)
            __hip_atomic_fetch_add((GAS u32*)(ws + WS_CNT + (layer * 16 + pm) * 4096 + lane * 256 + 128), 1u << pn, RLX_AGENT);
    }
    STAMP_OUT(layer * 4 + 2);
    STAMP(layer * 4 + 3, 0);
    {
        const int mt = 2 * pm + (MAP5 ? pn >> 3 : pn & 1), nt = MAP5 ? pn & 7 : pn >> 1;
        const u32 wb = WS_WOUT + (nt * 72 + 18 * wid) * 4608;
        const u32 vo4 = 4096 + g * 128 + (lane & 7) * 16;
        bf16x8 fa[4][4], fw[3][5], fl[5];
        f32x4 ac[4][5];
#pragma unroll
        for (int m = 0; m < 4; ++m)
#pragma unroll
            for (int n = 0; n < 5; ++n) ac[m][n] = (f32x4){0.f, 0.f, 0.f, 0.f};
        u32 abm[4];
#pragma unroll
        for (int m = 0; m < 4; ++m) abm[m] = WS_H + LSH * layer * ZB + ((4 * mt + (m ^ wid)) * 8 + 2 * wid) * 9216;
#define K5_LD(v, sof, aux) __builtin_bit_cast(bf16x8, __builtin_amdgcn_raw_buffer_load_b128(rs, v, sof, aux))
#define K5_LW(r, s) do { _Pragma("unroll") for (int n = 0; n < 4; ++n) fw[r][n] = K5_LD(vA + n * 1024, wb + (s) * 4608, 0); fw[r][4] = K5_LD(vo4, wb + (s) * 4608, 0); SB(); } while (0)
#define K5_LL(s) do { _Pragma("unroll") for (int n = 0; n < 4; ++n) fl[n] = LDSR(wl, (s) * 4608 + n * 1024); fl[4] = LDSR(wl4, (s) * 4608); SB(); } while (0)
#define K5_LA(r, s) do { _Pragma("unroll") for (int m = 0; m < 4; ++m) fa[r][m] = K5_LD(vA + ((s) & 3) * 1024, abm[m] + ((s) & ~3) * 1024, 16); SB(); } while (0)
#define K5_MM(W, r) do { _Pragma("unroll") for (int m = 0; m < 4; ++m) _Pragma("unroll") for (int n = 0; n < 5; ++n) ac[m][n] = MFMAB(W[n], fa[r][m], ac[m][n]); SB(); } while (0)
        PINV(vA);
        const u32 wl = lds + wid * 2 * STAGE + vA, wl4 = lds + wid * 2 * STAGE + vo4;
        K5_LW(0, 8); K5_LW(1, 9); K5_LW(2, 10);
        u32x4 res[2]; u32x2 res2;
        const u32 xoff = WS_X + (2 * layer + 1) * XB + (4 * mt + wid) * 18432;
        {
            const u32 need = 15u << (4 * wid);
            FENCE();
#pragma nounroll
            for (u32 n = 0; n < SPIN_CAP; ++n) { if ((__builtin_amdgcn_readfirstlane(ldsw(FL + 88)) & need) == need) break; }
            FENCE();
        }
        STAMP(layer * 4 + 3, 1);
        constexpr int ord[18] = {8, 0, 9, 1, 10, 2, 11, 3, 12, 4, 13, 5, 14, 6, 15, 7, 16, 17};
#pragma unroll
        for (int i = 0; i < 4; ++i) K5_LA(i, ord[i]);
        lds_wait(FL, 100u);
#pragma unroll
        for (int m = 0; m < 4; ++m)
#pragma unroll
            for (int n = 0; n < 5; ++n) PINV(ac[m][n]);
        asm volatile("s_nop 1");
#pragma unroll
        for (int i = 0; i < 18; ++i) {
            const int sl = ord[i];
#pragma unroll
            for (int m = 0; m < 4; ++m)
#pragma unroll
                for (int n = 0; n < 5; ++n) {
                    if (sl >= 8) MFS(ac[m][n], fw[(sl - 8) % 3][n], fa[i & 3][m]); else MFS(ac[m][n], fl[n], fa[i & 3][m]);
                    const int e = (n == 1 ? m : (m == 0 && n == 3 ? 4 : -1));
                    if (e >= 0) {
                        if (sl >= 8 && sl < 16) fl[e] = e < 4 ? LDSR(wl, (sl - 8) * 4608 + e * 1024) : LDSR(wl4, (sl - 8) * 4608);
                        if (sl < 7) fw[sl % 3][e] = e < 4 ? K5_LD(vA + e * 1024, wb + (sl + 11) * 4608, 0) : K5_LD(vo4, wb + (sl + 11) * 4608, 0);
                        if (sl == 7 && e == 0) res[0] = __builtin_amdgcn_raw_buffer_load_b128(rs, vA, xoff + 2304 * nt, 16);
                        if (sl == 7 && e == 1) res[1] = __builtin_amdgcn_raw_buffer_load_b128(rs, vA + 1024, xoff + 2304 * nt, 16);
                        if (sl == 7 && e == 2) { const int l2 = lane_id(); res2 = __builtin_amdgcn_raw_buffer_load_b64(rs, (l2 & 15) * 16 + ((l2 >> 1) & 8) + 2048, xoff + 2304 * nt, 16); }
                    }
                    if (n == 4 && i + 4 < 18) fa[i & 3][m] = K5_LD(vA + (ord[i + 4 < 18 ? i + 4 : 0] & 3) * 1024, abm[m] + (ord[i + 4 < 18 ? i + 4 : 0] & ~3) * 1024, 16);
                    SB();
                }
        }
        asm volatile("s_nop 15" ::: "memory");
        STAMP(layer * 4 + 3, 3);
        proj_tail(ac, res, res2, lds, 2 * STAGE, FL + 96, ws, rs, out, FL + 128, hand, 2 * layer + 2, mt, nt, wid);
    }
    STAMP(layer * 4 + 3, 2);
    STAMP_OUT(layer * 4 + 3);
}

__global__ __launch_bounds__(512, 2) void k1_main(char* __restrict__ ws, const u16* __restrict__ win, const u16* __restrict__ wsp, const u16* __restrict__ bsp,
                                                  u16* __restrict__ out) {
    const u32 lds = 0;
    const int wid = __builtin_amdgcn_readfirstlane(threadIdx.x >> 6);
    if (threadIdx.x >= 352) {
        const int i = threadIdx.x - 352, w = i & 63;
        stsw(lds + LDS_FL + i * 4, w == 32 ? 9u : (w == 33 ? 18u : 0u));
        WAIT_L0();
    }
#pragma nounroll
    for (int layer = 0; layer < 2; ++layer) {
        phase_a(ws, win, lds, wid, layer, layer != 0, true);
        phase_b(ws, wsp, bsp, out, lds, wid, layer, layer == 0);
    }
}

extern "C" __global__ void k9_end() {}

extern "C" void kernel_launch(void* const* d_in, const int* in_sizes, int n_in, void* d_out, int out_size, void* d_ws, size_t ws_size,
                              hipStream_t stream) {
    const u16* x = (const u16*)d_in[0];
    const u16* wqkv = (const u16*)d_in[1];
    const u16* wo = (const u16*)d_in[2];
    const u16* win = (const u16*)d_in[4];
    const u16* wsp = (const u16*)d_in[5];
    const u16* bsp = (const u16*)d_in[6];
    const u16* wout = (const u16*)d_in[7];
    char* ws = (char*)d_ws + WS_BASE(ws_size);
    u16* out = (u16*)d_out;
    (void)hipFuncSetAttribute((const void*)k1_main, hipFuncAttributeMaxDynamicSharedMemorySize, LDS_TOTAL);
    k0_prep<<<K0_GRID, 576, 0, stream>>>(x, wqkv, wo, wout, ws);
    k1_main<<<256, 512, LDS_TOTAL, stream>>>(ws, win, wsp, bsp, out);
}
```

```cpp
#include <hip/hip_runtime.h>
#include <hip/hip_bf16.h>
#include <stdint.h>

#define LAS __attribute__((address_space(3)))
#define GAS __attribute__((address_space(1)))
typedef unsigned short u16;
typedef unsigned u32;
typedef unsigned long long u64;
typedef short bf16x8 __attribute__((ext_vector_type(8)));
typedef float f32x2 __attribute__((ext_vector_type(2)));
typedef float f32x4 __attribute__((ext_vector_type(4)));
typedef unsigned u32x2 __attribute__((ext_vector_type(2)));
typedef unsigned u32x4 __attribute__((ext_vector_type(4)));
typedef __bf16 bf2_t __attribute__((ext_vector_type(2)));

#define ANY(p) (__builtin_amdgcn_ballot_w64(p) != 0)
#define U2F(x) __builtin_bit_cast(float, (u32)(x))
#define F2U(x) __builtin_bit_cast(u32, (float)(x))
#define WAIT_V(n) asm volatile("s_waitcnt vmcnt(%0)" ::"i"(n) : "memory")
#define WAIT_L0() asm volatile("s_waitcnt lgkmcnt(0)" ::: "memory")
#define FENCE() asm volatile("" ::: "memory")
#define SB() __builtin_amdgcn_sched_barrier(0)
#define BAR() do { FENCE(); __builtin_amdgcn_s_barrier(); FENCE(); } while (0)
#define WCNT(vm, lg) __builtin_amdgcn_s_waitcnt(((vm) & 15) | (7 << 4) | ((lg) << 8) | (((vm) >> 4) << 14))
#define MFMA(acc, x, y) asm volatile("v_mfma_f32_16x16x32_bf16 %0, %1, %2, %0" : "+v"(acc) : "v"(x), "v"(y))
#define MFMAB(x, y, c) __builtin_amdgcn_mfma_f32_16x16x32_bf16(x, y, c, 0, 0, 0)
#define BST128(o, rs, vo, so, aux) do { __builtin_amdgcn_raw_buffer_store_b128(o, rs, vo, so, aux); SB(); asm volatile("s_nop 1"); SB(); } while (0)
#define PINV(x) asm volatile("" : "+v"(x))
#define RLX_AGENT __ATOMIC_RELAXED, __HIP_MEMORY_SCOPE_AGENT

constexpr int XDELAY1 = 18, XDELAY3 = 20, POLLD = 16, FDELAY = 4, STAG2 = 60, HDELAY = 10, ODELAY = 12, XRD = 12, WRD = 90;
constexpr int LGATE2 = 14, HPRIO = 3, TGATE = 6, LAZY = 2, XD = 6;
#define PM_OF(x, i) (((x) & 1) * 8 + ((i) & 7))
#define PN_OF(x, i) (((x) >> 1) * 4 + ((i) >> 3))
#define NTL(p) __builtin_nontemporal_load(p)
constexpr u32 SENT = 0xFFFFFFFFu;
constexpr u32 SPIN_LDS = 1u << 19, SPIN_CAP = 1u << 15;

constexpr u32 WS_WQ = 0;
constexpr u32 WS_CSQ = WS_WQ + 16 * 129024;
constexpr u32 WS_WO = WS_CSQ + 18 * 8192;
constexpr u32 WS_WIN = WS_WO + 8 * 18 * 4608;
constexpr u32 WS_CSIN = WS_WIN + 288 * 18 * 1024;
constexpr u32 WS_WOUT = WS_CSIN + 3 * 4608 * 4;
constexpr u32 WS_X = WS_WOUT + 8 * 72 * 4608;
constexpr u32 XB = 128 * 18 * 1024, XSB = 2048 * 64, ZB = 144 * 64 * 1024;
constexpr u32 WS_XS = WS_X + 4 * XB;
constexpr u32 WS_O = WS_XS + 4 * XSB;
constexpr u32 WS_Z = WS_O + XB;
constexpr u32 WS_H = WS_Z + ZB;
constexpr u32 WS_KVP = WS_H + ZB;
constexpr u32 WS_ZST = WS_KVP + 2 * 256 * 2816;
constexpr u32 WS_CNT = WS_ZST + 2 * 256 * 1024;
constexpr u32 WS_CNO = WS_CNT + 2 * 16 * 32 * 128;
constexpr u32 WS_CNX = WS_CNO + 2 * 16 * 16 * 128;
constexpr u32 WS_CNW = WS_CNX + 4 * 16 * 16 * 128;
constexpr u32 WS_END = WS_CNW + 72 * 128;

__device__ __forceinline__ u32 pk2(float lo, float hi) {
    f32x2 v = {lo, hi};
    bf2_t b = __builtin_convertvector(v, bf2_t);
    return __builtin_bit_cast(u32, b);
}
__device__ __forceinline__ float bflo(u32 w) { return U2F(w << 16); }
__device__ __forceinline__ float bfhi(u32 w) { return U2F(w & 0xffff0000u); }
__device__ __forceinline__ bf16x8 mk8(u32 a, u32 b, u32 c, u32 d) { u32x4 v = {a, b, c, d}; return __builtin_bit_cast(bf16x8, v); }
__device__ __forceinline__ float sfma(float a, float b, float c) { float r; asm("v_fma_f32 %0, %1, %2, %3" : "=v"(r) : "v"(a), "v"(b), "v"(c)); return r; }
__device__ __forceinline__ f32x4 sadd4(f32x4 a, f32x4 b) {
    float r0 = a[0], r1 = a[1], r2 = a[2], r3 = a[3];
    asm("v_add_f32 %0, %0, %4\n\tv_add_f32 %1, %1, %5\n\tv_add_f32 %2, %2, %6\n\tv_add_f32 %3, %3, %7" : "+v"(r0), "+v"(r1), "+v"(r2), "+v"(r3) : "v"(b[0]), "v"(b[1]), "v"(b[2]), "v"(b[3]));
    f32x4 r = {r0, r1, r2, r3};
    return r;
}
__device__ __forceinline__ float rbf(float v) { return bflo(pk2(v, v)); }
__device__ __forceinline__ void ln_stats(float s1, float s2, float inv_n, float& mean, float& sd) {
    const float m = sfma(s1, inv_n, 0.f);
    const float var = fmaxf(sfma(-m, m, sfma(s2, inv_n, 0.f)), 0.f);
    mean = rbf(m);
    sd = rbf(__builtin_amdgcn_sqrtf(rbf(rbf(var) + 1e-5f)));
}
__device__ __forceinline__ f32x4 phi4(f32x4 t) {
    const f32x4 u = t * 1.44269504089f;
    f32x4 e, m;
#pragma unroll
    for (int r = 0; r < 4; ++r) { e[r] = __builtin_amdgcn_exp2f(__builtin_amdgcn_fmed3f(u[r], 0.f, -__builtin_inff())); m[r] = __builtin_amdgcn_fmed3f(t[r], 0.f, __builtin_inff()); }
    return (m + e) + 1e-6f;
}
__device__ __forceinline__ int lane_id() { int l; asm volatile("v_mbcnt_lo_u32_b32 %0, -1, 0\n\tv_mbcnt_hi_u32_b32 %0, -1, %0" : "=v"(l)); __builtin_assume(l >= 0 && l < 64); return l; }
#define LANES() const int lane = lane_id(), c = lane & 15, g = lane >> 4; (void)c; (void)g
__device__ __forceinline__ u32 mx4(u32x4 v) { return max(max(v[0], v[1]), max(v[2], v[3])); }
__device__ __forceinline__ u32 ldsw(u32 a) { return *(volatile LAS u32*)(size_t)a; }
__device__ __forceinline__ void stsw(u32 a, u32 v) { *(volatile LAS u32*)(size_t)a = v; }
#define LDSR(addr, off) (*(LAS bf16x8*)((LAS char*)(size_t)(addr) + (off)))
__device__ __forceinline__ void lds_wait(u32 a, u32 target) {
    FENCE();
#pragma nounroll
    for (u32 n = 0; n < SPIN_LDS; ++n) { const u32 v = ldsw(a); if (!ANY(v < target)) break; }
    FENCE();
}
__device__ __forceinline__ void lds_wait_gt(u32 a, u32 low) {
    FENCE();
#pragma nounroll
    for (u32 n = 0; n < SPIN_LDS; ++n) { const u32 v = ldsw(a); if (!ANY(v <= low)) break; }
    FENCE();
}
__device__ __forceinline__ void lds_wait_sleep(u32 a, u32 target) {
    FENCE();
#pragma nounroll
    for (u32 n = 0; n < SPIN_LDS; ++n) { const u32 v = ldsw(a); if (!ANY(v < target)) break; __builtin_amdgcn_s_sleep(1); }
    FENCE();
}
__device__ __forceinline__ void lds_wait_long(u32 a, u32 target) {
    FENCE();
#pragma nounroll
    for (u32 n = 0; n < SPIN_CAP; ++n) { const u32 v = ldsw(a); if (!ANY(v < target)) break; __builtin_amdgcn_s_sleep(24); }
    FENCE();
}
#define DMA4(v0, v1, v2, v3, sbase, m0v)                                                                            \
    asm volatile("s_mov_b32 m0, %5\n\ts_nop 0\n\tglobal_load_lds_dwordx4 %0, %4\n\t"                                \
                 "global_load_lds_dwordx4 %1, %4 offset:1024\n\tglobal_load_lds_dwordx4 %2, %4 offset:2048\n\t"     \
                 "global_load_lds_dwordx4 %3, %4 offset:3072" ::"v"(v0), "v"(v1), "v"(v2), "v"(v3), "s"(sbase), "s"(m0v) : "memory")
#define DMA2(v0, v1, sbase, m0v)                                                                                    \
    asm volatile("s_mov_b32 m0, %3\n\ts_nop 0\n\tglobal_load_lds_dwordx4 %0, %2\n\t"                                \
                 "global_load_lds_dwordx4 %1, %2 offset:1024" ::"v"(v0), "v"(v1), "s"(sbase), "s"(m0v) : "memory")

template <int SLP>
__device__ __forceinline__ void touch_paced(const char* p, int n, u32 vo) {
    u32x4 sink = {0u, 0u, 0u, 0u};
#pragma nounroll
    for (int i = 0; i < n; ++i) {
        asm volatile("global_load_dwordx4 %0, %1, %2" : "+v"(sink) : "v"(vo), "s"(p + i * 1024));
        __builtin_amdgcn_s_sleep(SLP);
    }
    WAIT_V(0);
    PINV(sink);
}

__device__ __forceinline__ u32x4 tcol(const u32x2 (&ld)[8], int i) {
    const u32 sel = (i & 1) ? 0x07060302u : 0x05040100u;
    u32x4 o;
    o.x = __builtin_amdgcn_perm(ld[1][i >> 1], ld[0][i >> 1], sel);
    o.y = __builtin_amdgcn_perm(ld[3][i >> 1], ld[2][i >> 1], sel);
    o.z = __builtin_amdgcn_perm(ld[5][i >> 1], ld[4][i >> 1], sel);
    o.w = __builtin_amdgcn_perm(ld[7][i >> 1], ld[6][i >> 1], sel);
    return o;
}
__device__ __forceinline__ float sum8(u32x4 o, float s) {
    const bf2_t one2 = __builtin_bit_cast(bf2_t, 0x3f803f80u);
#pragma unroll
    for (int k = 0; k < 4; ++k) { const u32 wv = o[k]; s = __builtin_amdgcn_fdot2_f32_bf16(__builtin_bit_cast(bf2_t, wv), one2, s, false); }
    return s;
}

constexpr int K0_QKV = 72, K0_X = 64, K0_WO = 90, K0_GRID = 256;
static_assert((WS_END - WS_KVP) / 16 <= K0_GRID * 576, "k0 grid too small for the hand-off words");
#define K0ST(off, val) __builtin_amdgcn_raw_buffer_store_b128(val, rs0, off, 0, 0)
__device__ __forceinline__ void k0_body(const u16* __restrict__ x, const u16* __restrict__ wqkv, const u16* __restrict__ wo,
                                        const u16* __restrict__ wout, char* __restrict__ ws) {
    __shared__ __attribute__((aligned(16))) char img[36864 + 576 * 8];
    const auto rs0 = __builtin_amdgcn_make_buffer_rsrc(ws, 0, WS_END, 0x00020000);
    const int tid = threadIdx.x, w = __builtin_amdgcn_readfirstlane(tid >> 6), lane = tid & 63, c = lane & 15, g = lane >> 4;
    int b = blockIdx.x;
    if (b < K0_QKV) {
        const int t = (b >> 3) % 3, kt = (b & 7) + 8 * (b / 24);
        if (kt >= 18) return;
        u32x2 ld[8];
#pragma unroll
        for (int j = 0; j < 8; ++j) ld[j] = NTL((const u32x2*)(wqkv + (size_t)(32 * kt + 8 * g + j) * 1728 + 576 * t + 64 * w + 4 * c));
        float* cst = (float*)(img + 36864);
        u32 dst[4], cw;
#pragma unroll
        for (int i = 0; i < 4; ++i) {
            const int col = 64 * w + 4 * c + i, h = col / 18, d = col - 18 * h;
            dst[i] = d < 16 ? h * 1024 + (d + 16 * g) * 16 : 32768 + (h >> 1) * 256 + g * 64 + (2 * (h & 1) + d - 16) * 16;
            PINV(dst[i]);
        }
        {
            const int col = 64 * w + 4 * c + g, h = col / 18, d = col - 18 * h;
            cw = (h >> 1) * 36 + (d < 16 ? (h & 1) * 16 + d : 32 + 2 * (h & 1) + d - 16);
            PINV(cw);
        }
        float cs[4];
#pragma unroll
        for (int i = 0; i < 4; ++i) {
            const u32x4 o = tcol(ld, i);
            *(u32x4*)(img + dst[i]) = o;
            cs[i] = sum8(o, 0.f);
        }
        {
            auto r0 = __builtin_amdgcn_permlane32_swap(F2U(cs[0]), F2U(cs[2]), false, false);
            auto r1 = __builtin_amdgcn_permlane32_swap(F2U(cs[1]), F2U(cs[3]), false, false);
            auto r2 = __builtin_amdgcn_permlane16_swap(F2U(U2F(r0[0]) + U2F(r0[1])), F2U(U2F(r1[0]) + U2F(r1[1])), false, false);
            cst[cw] = U2F(r2[0]) + U2F(r2[1]);
        }
        __syncthreads();
        const int rc = t == 1 ? 0 : (t == 2 ? 4 : 8);
        if (w < 8) {
#pragma unroll
            for (int k = 0; k < 4; ++k) {
                const int h = 4 * w + k;
                const u32 tile = t == 0 ? 92160 + (kt * 2 + (h & 1)) * 1024 : (kt * 5 + 2 * (t - 1) + (h & 1)) * 1024;
                K0ST(WS_WQ + (h >> 1) * 129024 + tile + lane * 16, *(const u32x4*)(img + h * 1024 + lane * 16));
            }
        } else {
#pragma unroll
            for (int k = 0; k < 4; ++k) {
                char* dst = ws + WS_WQ + (4 * k + g) * 129024 + (kt * 5 + 4) * 1024 + (c >> 2) * 256 + (rc + (c & 3)) * 16;
                *(u32x4*)dst = *(const u32x4*)(img + 32768 + k * 1024 + lane * 16);
                if (t == 0) *(u32x4*)(dst + 64) = (u32x4){0u, 0u, 0u, 0u};
            }
        }
        {
            const int p = tid / 36, e = tid - 36 * p;
            float* cq = (float*)(ws + WS_CSQ) + (kt * 16 + p) * 128;
            cq[e < 32 ? (t == 0 ? 80 : 32 * (t - 1)) + e : 64 + rc + e - 32] = cst[tid];
            if (t == 0 && e >= 32) cq[76 + e - 32] = 0.f;
        }
        return;
    }
    b -= K0_QKV;
    if (b < K0_X) {
        const int rl = tid / 18, t18 = tid - 18 * rl;
        u32x4 v[4];
#pragma unroll
        for (int i = 0; i < 4; ++i) v[i] = NTL((const u32x4*)(x + (size_t)(32 * b + rl) * 576 + (18 * i + t18) * 8));
        float s1 = 0.f, s2 = 0.f;
#pragma unroll
        for (int i = 0; i < 4; ++i) {
            const int ch = 18 * i + t18;
            *(u32x4*)(img + ((rl >> 4) * 18 + (ch >> 2)) * 1024 + ((rl & 15) + 16 * (ch & 3)) * 16) = v[i];
#pragma unroll
            for (int k = 0; k < 4; ++k) { const float a = bflo(v[i][k]), bb = bfhi(v[i][k]); s1 += a + bb; s2 += a * a + bb * bb; }
        }
        f32x2 o = {s1, s2};
        ((f32x2*)(img + 36864))[tid] = o;
        __syncthreads();
#pragma unroll
        for (int t = 0; t < 4; ++t) K0ST(WS_X + b * 36864 + (w * 4 + t) * 1024 + lane * 16, *(const u32x4*)(img + (w * 4 + t) * 1024 + lane * 16));
        if (tid < 256) {
            const int r = tid & 31, q = tid >> 5;
            const f32x2* pp = (const f32x2*)(img + 36864) + r * 18;
            o = pp[2 * q] + pp[2 * q + 1];
            if (q < 2) o += pp[16 + q];
            *(f32x2*)(ws + WS_XS + (q * 2048 + 32 * b + r) * 8) = o;
        }
        return;
    }
    b -= K0_X;
    if (b < K0_WO) {
        const bool iso = b < 18;
        const int sl = iso ? b : b - 18, G = sl / 9, t = sl - 9 * G;
        const u16* src = iso ? wo : wout;
        const int col = 64 * w + 4 * c, nt = col / 72, cl = col - 72 * nt;
        const int r0 = iso ? (sl < 16 ? 36 * sl : 288 * (sl - 16) + 16) : (t < 8 ? 288 * G + 144 * (t >> 2) + 32 * (t & 3) : 288 * G + 128);
        const int gs = (iso && sl >= 16) ? 72 : 4, s4 = iso ? (sl < 16 ? 18 : 36) : (t < 8 ? 16 : 144), s2 = (iso && sl >= 16) ? 18 : 2;
        u32x2 ld[8];
#pragma unroll
        for (int j = 0; j < 8; ++j) ld[j] = NTL((const u32x2*)(src + (size_t)(r0 + gs * g + s4 * (j >> 2) + s2 * ((j >> 1) & 1) + (j & 1)) * 576 + col));
        const int n = cl < 64 ? 2 * (cl >> 5) + ((cl >> 2) & 1) : 4, ii = cl < 64 ? 4 * ((cl >> 3) & 3) : cl - 64;
#pragma unroll
        for (int i = 0; i < 4; ++i) {
            const u32 off = n < 4 ? n * 1024 + (ii + i + 16 * g) * 16 : 4096 + g * 128 + (ii + i) * 16;
            *(u32x4*)(img + nt * 4608 + off) = tcol(ld, i);
        }
        __syncthreads();
#pragma unroll
        for (int k = 0; k < 4; ++k) {
            const u32 i = (w * 4 + k) * 1024 + lane * 16, n2 = i / 4608, off = i - 4608 * n2;
            K0ST((iso ? WS_WO + (n2 * 18 + sl) * 4608 : WS_WOUT + (n2 * 72 + sl) * 4608) + off, *(const u32x4*)(img + i));
        }
        return;
    }
}

__global__ __launch_bounds__(576) void k0_prep(const u16* __restrict__ x, const u16* __restrict__ wqkv, const u16* __restrict__ wo,
                                               const u16* __restrict__ wout, const u16* __restrict__ bsp, char* __restrict__ ws) {
    const u32 gid = blockIdx.x * 576 + threadIdx.x;
    u32 warm = 0;
    if (blockIdx.x >= 240) {
        u64 pc; int dist;
        asm volatile("s_getpc_b64 %0\n\ts_mov_b32 %1, k9_end@rel32@lo+4" : "=&s"(pc), "=&s"(dist));
        if ((int)threadIdx.x * 128 < dist) warm |= *(const GAS u32*)(pc + threadIdx.x * 128);
        if (threadIdx.x < 32) warm |= *(const u32*)(bsp + threadIdx.x * 64);
    }
    if (gid < (WS_END - WS_KVP) / 16) {
        const auto rs = __builtin_amdgcn_make_buffer_rsrc(ws, 0, WS_END, 0x00020000);
        const u32 v = gid < (WS_CNT - WS_KVP) / 16 ? SENT : 0u;
        const u32x4 o = {v, v, v, v};
        BST128(o, rs, gid * 16, WS_KVP, 16);
    }
    k0_body(x, wqkv, wo, wout, ws);
    asm volatile("" :: "v"(warm));
}

constexpr u32 LDS_B = 158080, LDS_FL = LDS_B + 512, LDS_TOTAL = LDS_FL + 640;
typedef __amdgpu_buffer_rsrc_t rsrc_t;

__device__ __forceinline__ void x_ready_stats(char* ws, rsrc_t rs, u32 FL, bool fresh, int ver, int pm, int j, int lane, f32x2& mean, f32x2& sd) {
    if (fresh) {
        lds_wait_sleep(FL + 120, 4u);
        __builtin_amdgcn_s_sleep(XRD);
        GAS u32* xm = (GAS u32*)(ws + WS_CNX + ((ver * 16 + pm) * 16 + j) * 128);
#pragma nounroll
        for (u32 n = 0; n < SPIN_CAP; ++n) { if (__builtin_amdgcn_readfirstlane(__hip_atomic_load(xm, RLX_AGENT)) == 0xffffu) break; }
        FENCE();
        stsw(FL + 124, 1u);
        FENCE();
    }
    f32x4 v[8];
#pragma unroll
    for (int k = 0; k < 8; ++k) v[k] = __builtin_bit_cast(f32x4, __builtin_amdgcn_raw_buffer_load_b128(rs, lane * 16, WS_XS + ver * XSB + (k * 2048 + 128 * pm) * 8, 16));
    const f32x4 sm = sadd4(sadd4(sadd4(v[0], v[1]), sadd4(v[2], v[3])), sadd4(sadd4(v[4], v[5]), sadd4(v[6], v[7])));
#pragma unroll
    for (int k = 0; k < 2; ++k) {
        float m, d;
        ln_stats(sm[2 * k], sm[2 * k + 1], 1.f / 576.f, m, d);
        PINV(m); PINV(d);
        mean[k] = m; sd[k] = d;
    }
}

__device__ __forceinline__ void proj_store(const f32x4 (&y)[5], char* ws, rsrc_t rs, u16* out, u32 FLN, bool hand, int ver, int mt, int nt, int wid) {
    const int lane = lane_id();
    const u32 vo = lane * 16;
    const int c = lane & 15, g = lane >> 4;
    u32x4 o[2]; u32x2 o2;
#pragma unroll
    for (int i = 0; i < 2; ++i) o[i] = (u32x4){pk2(y[2 * i][0], y[2 * i][1]), pk2(y[2 * i][2], y[2 * i][3]), pk2(y[2 * i + 1][0], y[2 * i + 1][1]), pk2(y[2 * i + 1][2], y[2 * i + 1][3])};
    o2 = (u32x2){pk2(y[4][0], y[4][1]), pk2(y[4][2], y[4][3])};
    if (ver == 4) {
        u16* op = out + (size_t)(64 * mt + 16 * wid + c) * 576 + 72 * nt;
        *(u32x4*)(op + 8 * g) = o[0];
        *(u32x4*)(op + 32 + 8 * g) = o[1];
        if (g < 2) *(u32x2*)(op + 64 + 4 * g) = o2;
    } else {
        const u32 xo = WS_X + ver * XB + (4 * mt + wid) * 18432 + 2304 * nt;
        BST128(o[0], rs, vo, xo, 16);
        BST128(o[1], rs, vo + 1024, xo, 16);
        if (g < 2) __builtin_amdgcn_raw_buffer_store_b64(o2, rs, 2048 + c * 16 + g * 8, xo, 16); else o2 = (u32x2){0u, 0u};
        float s1 = 0.f, s2 = 0.f;
#pragma unroll
        for (int k = 0; k < 10; ++k) { const u32 wv = k < 8 ? o[k >> 2][k & 3] : o2[k & 1]; const float u = bflo(wv), v = bfhi(wv); s1 += u + v; s2 += u * u + v * v; }
        auto r1 = __builtin_amdgcn_permlane32_swap(F2U(s1), F2U(s2), false, false);
        const float t = U2F(r1[0]) + U2F(r1[1]);
        auto r2 = __builtin_amdgcn_permlane16_swap(F2U(t), F2U(t), false, false);
        const float tt = U2F(r2[0]) + U2F(r2[1]);
        if ((g & 1) == 0) __builtin_amdgcn_raw_buffer_store_b32(F2U(tt), rs, c * 8 + (g >> 1) * 4, WS_XS + ver * XSB + (nt * 2048 + 64 * mt + 16 * wid) * 8, 16);
        if (hand) {
            WAIT_V(0);
            u32 old = 0;
            if (lane == 0) old = __hip_atomic_fetch_add((LAS u32*)(size_t)(FLN + 120), 1u, __ATOMIC_RELAXED, __HIP_MEMORY_SCOPE_WORKGROUP);
            if (__builtin_amdgcn_readfirstlane(old) == 3 && lane < 16)
                __hip_atomic_fetch_add((GAS u32*)(ws + WS_CNX + ((ver * 16 + (mt >> 1)) * 16 + lane) * 128), 1u << (8 * (mt & 1) + nt), RLX_AGENT);
        }
    }
}

__device__ __forceinline__ void proj_tail(f32x4 (&ac)[4][5], const u32x4 (&res)[2], u32x2 res2, u32 lds, u32 wstride, u32 flags, char* ws, rsrc_t rs, u16* out,
                                          u32 FLN, bool hand, int ver, int mt, int nt, int wid) {
    const int lane = lane_id();
    const u32 vo = lane * 16;
#pragma unroll
    for (int m = 1; m < 4; ++m)
#pragma unroll
        for (int n = 0; n < 5; ++n) *(LAS f32x4*)(size_t)(lds + wid * wstride + vo + ((m - 1) * 5 + n) * 1024) = ac[m][n];
    FENCE();
    stsw(flags + wid * 4, 1u);
#pragma unroll
    for (int i = 0; i < 4; ++i) {
        const u32 w0 = res[i >> 1][2 * (i & 1)], w1 = res[i >> 1][2 * (i & 1) + 1];
        ac[0][i] += (f32x4){bflo(w0), bfhi(w0), bflo(w1), bfhi(w1)};
        PINV(ac[0][i]);
    }
    ac[0][4] += (f32x4){bflo(res2[0]), bfhi(res2[0]), bflo(res2[1]), bfhi(res2[1])};
    PINV(ac[0][4]);
    lds_wait(flags + (lane & 3) * 4, 1u);
#pragma unroll
    for (int n = 0; n < 5; ++n)
        ac[0][n] = (ac[0][n] + *(LAS f32x4*)(size_t)(lds + (wid ^ 1) * wstride + vo + n * 1024)) +
                   (*(LAS f32x4*)(size_t)(lds + (wid ^ 2) * wstride + vo + (5 + n) * 1024) + *(LAS f32x4*)(size_t)(lds + (wid ^ 3) * wstride + vo + (10 + n) * 1024));
    proj_store(ac[0], ws, rs, out, FLN, hand, ver, mt, nt, wid);
}

constexpr u32 K1_PW = 129024, K1_OP = K1_PW + 4 * 176 * 16, K1_ST = K1_OP + 1536, K1_CS = K1_ST + 1024, K1_RT = K1_CS + 512;

__device__ __forceinline__ void phase_a(char* ws, const u16* win, const u16* bsp, u32 lds, int wid, int layer, bool fresh, bool hand) {
    int bid = blockIdx.x;
    GAS char* wsg = (GAS char*)ws;
    asm volatile("" : "+s"(bid), "+s"(wsg), "+s"(wid));
    ws = (char*)wsg;
    const int xcd = bid & 7, bi = bid >> 3;
    const int pm = PM_OF(xcd, bi), p = PN_OF(xcd, bi);
    const u32 FL = lds + LDS_FL + 256 * layer;
    const auto rs = __builtin_amdgcn_make_buffer_rsrc(ws, 0, WS_END, 0x00020000);
    if (wid == 4) {
        LANES();
        const char* src = ws + WS_WQ + p * 129024;
        const u32 vo = lane * 16;
#define K1_U10(u) do { DMA4(vo, vo, vo, vo, src + (u) * 10240, lds + (u) * 10240); DMA4(vo, vo, vo, vo, src + (u) * 10240 + 4096, lds + (u) * 10240 + 4096); \
                       DMA2(vo, vo, src + (u) * 10240 + 8192, lds + (u) * 10240 + 8192); } while (0)
#define K1_U12(u) do { DMA4(vo, vo, vo, vo, src + 92160 + (u) * 12288, lds + 92160 + (u) * 12288); DMA4(vo, vo, vo, vo, src + 92160 + (u) * 12288 + 4096, lds + 92160 + (u) * 12288 + 4096); \
                       DMA4(vo, vo, vo, vo, src + 92160 + (u) * 12288 + 8192, lds + 92160 + (u) * 12288 + 8192); } while (0)
        if (fresh) lds_wait_sleep(FL + 120, 4u);
        K1_U10(0);
        if (!fresh) BAR();
        K1_U10(1); K1_U10(2);
#pragma nounroll
        for (int u = 3; u < 9; ++u) { WAIT_V(20); stsw(FL, u - 2); FENCE(); K1_U10(u); }
        WAIT_V(20); stsw(FL, 7); FENCE(); K1_U12(0);
        WAIT_V(22); stsw(FL, 8); FENCE(); K1_U12(1);
        WAIT_V(24); stsw(FL, 9); FENCE(); K1_U12(2);
        WAIT_V(0); stsw(FL, 12);
        const bool rt = layer == 0 && bid < 216;
        const int cb = bid / 3, k0 = 6 * (bid - 3 * cb);
        u32x2 ld[6][8];
        if (rt) {
#pragma unroll
            for (int i = 0; i < 6; ++i)
#pragma unroll
                for (int j = 0; j < 8; ++j) ld[i][j] = NTL((const u32x2*)(win + (size_t)(32 * (k0 + i) + 8 * g + j) * 4608 + 64 * cb + 4 * c));
            SB();
        }
        lds_wait_sleep(FL + 16 + (lane & 3) * 4, 2u);
        src = ws + WS_WO + (p & 7) * 82944;
        WAIT_V(12);
#pragma nounroll
        for (int u = 0; u < 8; ++u) { K1_U10(u); WAIT_V(40); }
        asm volatile("s_mov_b32 m0, %2\n\ts_nop 0\n\tglobal_load_lds_dwordx4 %0, %1" ::"v"(vo), "s"(src + 81920), "s"(lds + 81920) : "memory");
        WAIT_V(0); stsw(FL, 13);
        float cs[4] = {0.f, 0.f, 0.f, 0.f};
        u32x4 rb[6][4];
        if (rt) {
#pragma unroll
            for (int i = 0; i < 6; ++i) {
#pragma unroll
                for (int e = 0; e < 4; ++e) {
                    const u32x4 o = tcol(ld[i], e);
                    *(LAS u32x4*)(size_t)(lds + K1_RT + (c >> 2) * 1024 + (4 * (c & 3) + e + 16 * g) * 16) = o;
                    cs[e] = sum8(o, cs[e]);
                }
                FENCE();
#pragma unroll
                for (int t = 0; t < 4; ++t) rb[i][t] = *(LAS u32x4*)(size_t)(lds + K1_RT + t * 1024 + vo);
                FENCE();
            }
#pragma unroll
            for (int i = 0; i < 6; ++i)
#pragma unroll
                for (int t = 0; t < 4; ++t) BST128(rb[i][t], rs, vo, WS_WIN + ((4 * cb + t) * 18 + k0 + i) * 1024, 16);
#pragma unroll
            for (int e = 0; e < 4; ++e) {
                auto r1 = __builtin_amdgcn_permlane32_swap(F2U(cs[e]), F2U(cs[e]), false, false);
                const float t1 = sfma(U2F(r1[0]), 1.f, U2F(r1[1]));
                float t2 = t1;
                PINV(t2);
                asm volatile("s_nop 1");
                auto r2 = __builtin_amdgcn_permlane16_swap(F2U(t2), F2U(t2), false, false);
                cs[e] = sfma(U2F(r2[0]), 1.f, U2F(r2[1]));
            }
            const u32x4 co = {F2U(cs[0]), F2U(cs[1]), F2U(cs[2]), F2U(cs[3])};
            if (g == 0) BST128(co, rs, c * 16, WS_CSIN + ((k0 / 6) * 4608 + 64 * cb) * 4, 16);
            WAIT_V(0);
            if (lane == 0) __hip_atomic_fetch_add((GAS u32*)(ws + WS_CNW + cb * 128), 1u, RLX_AGENT);
        }
        return;
    }
    if (wid > 4) {
        LANES();
        const int ht = (wid - 5) * 64 + lane;
        if (!fresh) BAR();
        else if (wid != 5) lds_wait_long(FL + 120, 4u);
        if (wid == 5) {
            f32x2 mean, sd, r, mr;
            x_ready_stats(ws, rs, FL, fresh, 2 * layer, pm, p, lane, mean, sd);
#pragma unroll
            for (int k = 0; k < 2; ++k) { r[k] = __builtin_amdgcn_rcpf(sd[k]); float t2 = -mean[k] * r[k]; PINV(t2); mr[k] = t2; }
            *(LAS f32x2*)(size_t)(lds + K1_ST + lane * 8) = r;
            *(LAS f32x2*)(size_t)(lds + K1_ST + 512 + lane * 8) = mr;
        } else if (wid == 6) {
            if (!fresh) {
                const u32 bw = *(const u32*)(bsp + 128 * pm + 2 * lane);
                *(LAS f32x2*)(size_t)(lds + LDS_B + lane * 8) = (f32x2){bflo(bw), bfhi(bw)};
            }
        } else {
            const f32x2* cp = (const f32x2*)(ws + WS_CSQ) + p * 64 + min(lane, 55);
            f32x2 q[18];
#pragma unroll
            for (int k = 0; k < 18; ++k) q[k] = cp[k * 1024];
            float q0 = q[0][0], q1 = q[0][1];
#pragma unroll
            for (int k = 1; k < 18; ++k) { q0 = sfma(q[k][0], 1.f, q0); q1 = sfma(q[k][1], 1.f, q1); }
            *(LAS f32x2*)(size_t)(lds + K1_CS + lane * 8) = (f32x2){q0, q1};
        }
        FENCE();
        stsw(FL + 4 + (wid - 5) * 4, 1u);
        __builtin_amdgcn_s_setprio(3);
        lds_wait_sleep(FL + 16 + (lane & 3) * 4, 1u);
        const int it = ht < 173 ? ht : 172;
        f32x4 v = *(LAS f32x4*)(size_t)(lds + K1_PW + it * 16);
#pragma unroll
        for (int k = 1; k < 4; ++k) v = sadd4(v, *(LAS f32x4*)(size_t)(lds + K1_PW + k * 2816 + it * 16));
        const u32 gbase = WS_KVP + (layer * 16 + p) * 16 * 2816;
        {
            u32x4 o = __builtin_bit_cast(u32x4, v);
#pragma unroll
            for (int k = 0; k < 4; ++k) o[k] = o[k] == SENT ? 0x7fc00000u : o[k];
            if (ht < 173) BST128(o, rs, it * 16, gbase + pm * 2816, 16);
        }
        u32x4 t[16];
        __builtin_amdgcn_s_sleep(XDELAY1);
#pragma unroll
        for (int k = 0; k < 16; ++k) t[k] = __builtin_amdgcn_raw_buffer_load_b128(rs, it * 16, gbase + k * 2816, 16);
        u32 mx = 0, spins = 0;
#pragma unroll
        for (int k = 0; k < 16; ++k) mx = max(max(mx, max(t[k][0], t[k][1])), max(t[k][2], t[k][3]));
        if (__builtin_expect(ANY(mx == SENT), 0)) {
#pragma nounroll
            for (spins = 1; spins < SPIN_CAP; ++spins) {
                FENCE();
                u32 miss = 0;
#pragma unroll
                for (int k = 0; k < 16; k += 4) miss |= ANY(max(max(mx4(t[k]), mx4(t[k + 1])), max(mx4(t[k + 2]), mx4(t[k + 3]))) == SENT) ? 1u << k : 0u;
                if (!miss) break;
#pragma unroll
                for (int k = 0; k < 16; ++k) if (miss >> (k & 12) & 1) t[k] = __builtin_amdgcn_raw_buffer_load_b128(rs, it * 16, gbase + k * 2816, 16);
            }
        }
        v = __builtin_bit_cast(f32x4, t[0]);
#pragma unroll
        for (int k = 1; k < 16; ++k) v = sadd4(v, __builtin_bit_cast(f32x4, t[k]));
        u32x2 lh = {pk2(v[0], v[1]), pk2(v[2], v[3])};
        if (ht >= 173) lh = (u32x2){0u, 0u};
        *(LAS u32x2*)(size_t)(lds + K1_OP + ht * 8) = lh;
        FENCE();
        stsw(FL + 32 + (wid - 5) * 4, 1u);
        __builtin_amdgcn_s_setprio(0);
        if (wid != 5) return;
        lds_wait_sleep(FL + 48, 4u);
        __builtin_amdgcn_s_sleep(ODELAY);
        GAS u32* om = (GAS u32*)(ws + WS_CNO + ((layer * 16 + pm) * 16 + p) * 128);
#pragma nounroll
        for (u32 n = 0; n < SPIN_CAP; ++n) { if (__builtin_amdgcn_readfirstlane(__hip_atomic_load(om, RLX_AGENT)) == 0xffffu) break; }
        FENCE();
        stsw(FL + 52, 1u);
        return;
    }
    LANES();

    const u32 xb = WS_X + 2 * layer * XB + (8 * pm + 2 * wid) * 18432, vo = lane * 16;
    bf16x8 xa[2][18];
#define K1_LX(m, kt) xa[m][kt] = __builtin_bit_cast(bf16x8, __builtin_amdgcn_raw_buffer_load_b128(rs, vo, xb + ((m) * 18 + (kt)) * 1024, 16))
    if (fresh) lds_wait(FL + 124, 1u);
#pragma unroll
    for (int kt = 0; kt < XD; ++kt) { K1_LX(0, kt); SB(); K1_LX(1, kt); SB(); }
    f32x4 acc[2][5];
#pragma unroll
    for (int f = 0; f < 5; ++f) { acc[0][f] = acc[1][f] = (f32x4){0.f, 0.f, 0.f, 0.f}; PINV(acc[0][f]); PINV(acc[1][f]); }
    u32 wb0 = lds + lane * 16, wb1 = wb0 + 61440, wb2 = wb0 + 92160;
    PINV(wb0); PINV(wb1); PINV(wb2);
#define K1_W1(kt, f) ((kt) < 12 ? LDSR(wb0, ((kt) * 5 + (f)) * 1024) : LDSR(wb1, (((kt) - 12) * 5 + (f)) * 1024))
    if (!fresh) BAR();
    lds_wait(FL, 1u);
    bf16x8 wf[2][5];
#pragma unroll
    for (int f = 0; f < 5; ++f) { wf[0][f] = K1_W1(0, f); SB(); }
#pragma unroll
    for (int f = 0; f < 5; ++f) { wf[1][f] = K1_W1(1, f); SB(); }
    u32 fl = 0;
#pragma unroll
    for (int kt = 0; kt < 18; ++kt) {
        if ((kt & 1) == 0 && kt + 2 < 18) { if (__builtin_expect(ANY(fl < (u32)(kt / 2 + 2)), 0)) lds_wait(FL, kt / 2 + 2); FENCE(); }
#pragma unroll
        for (int f = 0; f < 5; ++f) {
            MFMA(acc[0][f], xa[0][kt], wf[kt & 1][f]);
            if (f == 1 && kt + XD < 18) K1_LX(0, kt + XD);
            if (f == 3 && kt + XD < 18) K1_LX(1, kt + XD);
            if (f == 0 && (kt & 1) && kt < 16) { FENCE(); fl = ldsw(FL); FENCE(); }
            SB();
            MFMA(acc[1][f], xa[1][kt], wf[kt & 1][f]);
            if (kt + 2 < 18) wf[kt & 1][f] = K1_W1(kt + 2, f);
            SB();
        }
    }
    asm volatile("s_nop 15" ::: "memory");
    lds_wait(FL + 4 + (lane % 3) * 4, 1u);
    {
        u32 op[5][4], rk[4];
        float cs1[5];
#pragma unroll
        for (int f = 0; f < 5; ++f) cs1[f] = *(LAS float*)(size_t)(lds + K1_CS + f * 64 + c * 4);
#pragma unroll
        for (int m = 0; m < 2; ++m) {
            const f32x4 rr = *(LAS f32x4*)(size_t)(lds + K1_ST + (32 * wid + 16 * m + 4 * g) * 4);
            const f32x4 mm = *(LAS f32x4*)(size_t)(lds + K1_ST + 512 + (32 * wid + 16 * m + 4 * g) * 4);
#pragma unroll
            for (int f = 0; f < 5; ++f) {
                f32x4 v = rr * acc[m][f] + mm * cs1[f];
                if (f < 2) v = phi4(v);
                if (f == 4) { const f32x4 u = phi4(v); rk[2 * m] = pk2(u[0], u[1]); rk[2 * m + 1] = pk2(u[2], u[3]); }
                op[f][2 * m] = pk2(v[0], v[1]); op[f][2 * m + 1] = pk2(v[2], v[3]);
                if (f == 4 && c >= 12) { op[f][2 * m] = 0x3f803f80u; op[f][2 * m + 1] = 0x3f803f80u; }
            }
        }
        const bf16x8 KAa = mk8(op[0][0], op[0][1], op[0][2], op[0][3]), KAb = mk8(op[1][0], op[1][1], op[1][2], op[1][3]);
        const bf16x8 VBa = mk8(op[2][0], op[2][1], op[2][2], op[2][3]), VBb = mk8(op[3][0], op[3][1], op[3][2], op[3][3]);
        const bf16x8 RV = mk8(op[4][0], op[4][1], op[4][2], op[4][3]), RK = mk8(rk[0], rk[1], rk[2], rk[3]);
        const f32x4 z4 = {0.f, 0.f, 0.f, 0.f};
        const f32x4 T1a = MFMAB(KAa, VBa, z4), T1b = MFMAB(KAb, VBb, z4), T2a = MFMAB(KAa, RV, z4), T2b = MFMAB(KAb, RV, z4);
        const f32x4 T3a = MFMAB(RK, VBa, z4), T3b = MFMAB(RK, VBb, z4), T4 = MFMAB(RK, RV, z4);
        const u32 pw = lds + K1_PW + wid * 2816;
        *(LAS f32x4*)(size_t)(pw + lane * 16) = T1a;
        *(LAS f32x4*)(size_t)(pw + 1024 + lane * 16) = T1b;
        const f32x4 u3 = {T3a[0], T3a[1], T3b[2], T3b[3]};
        *(LAS f32x4*)(size_t)(pw + (g == 0 ? 128 + c : 175) * 16) = u3;
        *(LAS f32x4*)(size_t)(pw + (c == 12 ? 152 + g : ((c & 14) == 4 ? 144 + (c - 4) * 4 + g : 175)) * 16) = T2a;
        *(LAS f32x4*)(size_t)(pw + (c == 12 ? 164 + g : ((c & 14) == 6 ? 156 + (c - 6) * 4 + g : 175)) * 16) = T2b;
        *(LAS f32x4*)(size_t)(pw + (lane == 12 ? 172 : ((lane & 60) == 4 ? 164 + lane : 175)) * 16) = T4;
        FENCE();
        stsw(FL + 16 + wid * 4, 1u);
    }
    f32x4 qa[2][3];
#pragma unroll
    for (int f = 0; f < 3; ++f) { qa[0][f] = qa[1][f] = (f32x4){0.f, 0.f, 0.f, 0.f}; PINV(qa[0][f]); PINV(qa[1][f]); }
#define K1_W2(kt, f) ((f) < 2 ? LDSR(wb2, ((kt) * 2 + (f)) * 1024) : K1_W1(kt, 4))
    lds_wait(FL, 12u);
    bf16x8 wq[3][3];
#pragma unroll
    for (int k = 0; k < 3; ++k)
#pragma unroll
        for (int f = 0; f < 3; ++f) { wq[k][f] = K1_W2(k, f); SB(); }
#pragma unroll
    for (int kt = 0; kt < 18; ++kt) {
#pragma unroll
        for (int f = 0; f < 3; ++f) {
            MFMA(qa[0][f], wq[kt % 3][f], xa[0][kt]);
            SB();
            MFMA(qa[1][f], wq[kt % 3][f], xa[1][kt]);
            if (kt + 3 < 18) wq[kt % 3][f] = K1_W2(kt + 3, f);
            SB();
        }
    }
    asm volatile("s_nop 15" ::: "memory");
    stsw(FL + 16 + wid * 4, 2u);
    f32x4 cs2[3];
#pragma unroll
    for (int f = 0; f < 3; ++f) cs2[f] = *(LAS f32x4*)(size_t)(lds + K1_CS + (f < 2 ? 5 + f : 4) * 64 + g * 16);
    bf16x8 PQ[2][2];
#pragma unroll
    for (int m = 0; m < 2; ++m) {
        const float rr = *(LAS float*)(size_t)(lds + K1_ST + (32 * wid + 16 * m + c) * 4), mm = *(LAS float*)(size_t)(lds + K1_ST + 512 + (32 * wid + 16 * m + c) * 4);
        u32 w2[3][2];
#pragma unroll
        for (int f = 0; f < 3; ++f) {
            const f32x4 v = phi4(qa[m][f] * rr + cs2[f] * mm);
            w2[f][0] = pk2(v[0], v[1]); w2[f][1] = pk2(v[2], v[3]);
        }
        if (g != 2) { w2[2][0] = 0u; w2[2][1] = 0u; }
        PQ[m][0] = mk8(w2[0][0], w2[0][1], w2[2][0], w2[2][1]);
        PQ[m][1] = mk8(w2[1][0], w2[1][1], w2[2][0], w2[2][1]);
        PINV(PQ[m][0]); PINV(PQ[m][1]);
    }
    u32 ga[6];
    ga[0] = lane; ga[1] = g == 2 ? 128 + c : 175;
    ga[2] = c < 2 ? 144 + 4 * c + g : ((c & 3) == 2 ? 152 + g : 175);
    ga[3] = (c & 14) == 4 ? 156 + 4 * (c - 4) + g : ((c & 3) == 3 ? 164 + g : 175);
    ga[4] = g != 2 ? 175 : (c < 2 ? 168 + c : ((c & 3) == 2 ? 172 : 175));
    ga[5] = g != 2 ? 175 : ((c & 14) == 4 ? 166 + c : ((c & 3) == 3 ? 172 : 175));
#pragma unroll
    for (int i = 0; i < 6; ++i) { ga[i] = lds + K1_OP + ga[i] * 8; PINV(ga[i]); }
    const u32 ob = WS_O + (8 * pm + 2 * wid) * 18432;
    u32 orem = (16 + (p >> 3)) * 1024 + (c + 16 * ((p & 7) >> 1)) * 16 + (p & 1) * 8 + g * 4;
    PINV(orem);
    lds_wait(FL + 32 + (lane % 3) * 4, 1u);
    {
        const u32x2 l0 = *(LAS u32x2*)(size_t)ga[0], l1 = *(LAS u32x2*)(size_t)(ga[0] + 512), h01 = *(LAS u32x2*)(size_t)ga[1];
        const u32x2 l2 = *(LAS u32x2*)(size_t)ga[2], l3 = *(LAS u32x2*)(size_t)ga[3];
        const u32 h2 = *(LAS u32*)(size_t)ga[4], h3 = *(LAS u32*)(size_t)(ga[5] + 4);
        const bf16x8 A1a = mk8(l0[0], l0[1], h01[0], 0u), A1b = mk8(l1[0], l1[1], 0u, h01[1]), A2a = mk8(l2[0], l2[1], h2, 0u), A2b = mk8(l3[0], l3[1], 0u, h3);
        const f32x4 z4 = {0.f, 0.f, 0.f, 0.f};
        f32x4 O1a[2], O1b[2], O2[2];
#pragma unroll
        for (int m = 0; m < 2; ++m) O2[m] = MFMAB(A2a, PQ[m][0], z4);
#pragma unroll
        for (int m = 0; m < 2; ++m) O2[m] = MFMAB(A2b, PQ[m][1], O2[m]);
#pragma unroll
        for (int m = 0; m < 2; ++m) { O1a[m] = MFMAB(A1a, PQ[m][0], z4); O1b[m] = MFMAB(A1b, PQ[m][1], z4); }
#pragma unroll
        for (int m = 0; m < 2; ++m) {
            const float ia = __builtin_amdgcn_rcpf(O2[m][2] + 1e-6f), ib = __builtin_amdgcn_rcpf(O2[m][3] + 1e-6f);
            const f32x4 ya = O1a[m] * ia, yb = O1b[m] * ib;
            const u32x4 o = {pk2(ya[0], ya[1]), pk2(ya[2], ya[3]), pk2(yb[0], yb[1]), pk2(yb[2], yb[3])};
            BST128(o, rs, vo, ob + m * 18432 + p * 1024, 16);
            const float ir = g == 0 ? ia : ib;
            if (g < 2) __builtin_amdgcn_raw_buffer_store_b32(pk2(O2[m][0] * ir, O2[m][1] * ir), rs, orem, ob + m * 18432, 16);
        }
    }
    WAIT_V(0);
    {
        u32 old = 0;
        if (lane == 0) old = __hip_atomic_fetch_add((LAS u32*)(size_t)(FL + 48), 1u, __ATOMIC_RELAXED, __HIP_MEMORY_SCOPE_WORKGROUP);
        if (__builtin_amdgcn_readfirstlane(old) == 3 && lane < 16)
            __hip_atomic_fetch_add((GAS u32*)(ws + WS_CNO + ((layer * 16 + pm) * 16 + lane) * 128), 1u << p, RLX_AGENT);
    }
    const int mt = 2 * pm + (p >> 3), nt = p & 7;
    const u32 vo4 = 4096 + g * 128 + (lane & 7) * 16;
#define K2_LD(v, sof, aux) __builtin_bit_cast(bf16x8, __builtin_amdgcn_raw_buffer_load_b128(rs, v, sof, aux))
    bf16x8 fw[6][5], fa[18];
    f32x4 ac[5];
    {
        const u32 xo = WS_X + 2 * layer * XB + (4 * mt + wid) * 18432 + 2304 * nt;
        const u32x4 r0 = __builtin_amdgcn_raw_buffer_load_b128(rs, vo, xo, 16), r1 = __builtin_amdgcn_raw_buffer_load_b128(rs, vo + 1024, xo, 16);
        const u32x2 r2 = __builtin_amdgcn_raw_buffer_load_b64(rs, 2048 + c * 16 + (g & 1) * 8, xo, 16);
        ac[0] = (f32x4){bflo(r0[0]), bfhi(r0[0]), bflo(r0[1]), bfhi(r0[1])}; ac[1] = (f32x4){bflo(r0[2]), bfhi(r0[2]), bflo(r0[3]), bfhi(r0[3])};
        ac[2] = (f32x4){bflo(r1[0]), bfhi(r1[0]), bflo(r1[1]), bfhi(r1[1])}; ac[3] = (f32x4){bflo(r1[2]), bfhi(r1[2]), bflo(r1[3]), bfhi(r1[3])};
        ac[4] = (f32x4){bflo(r2[0]), bfhi(r2[0]), bflo(r2[1]), bfhi(r2[1])};
    }
    u32 wl[2] = {lds + vo, lds + vo + 41472}, wl4[2] = {lds + vo4, lds + vo4 + 41472};
    PINV(wl[0]); PINV(wl[1]); PINV(wl4[0]); PINV(wl4[1]);
#define K2_W(s, n) ((n) < 4 ? LDSR(wl[(s) / 9], ((s) % 9) * 4608 + (n) * 1024) : LDSR(wl4[(s) / 9], ((s) % 9) * 4608))
    lds_wait(FL, 13u);
#pragma unroll
    for (int sl = 0; sl < 6; ++sl)
#pragma unroll
        for (int n = 0; n < 5; ++n) { fw[sl][n] = K2_W(sl, n); SB(); }
#pragma unroll
    for (int n = 0; n < 5; ++n) PINV(ac[n]);
    lds_wait(FL + 52, 1u);
#pragma unroll
    for (int sl = 0; sl < 18; ++sl) { fa[sl] = K2_LD(vo + (sl & 3) * 1024, WS_O + ((4 * mt + wid) * 18 + (sl & ~3)) * 1024, 16); SB(); }
#pragma unroll
    for (int sl = 0; sl < 18; ++sl)
#pragma unroll
        for (int n = 0; n < 5; ++n) {
            MFMA(ac[n], fw[sl % 6][n], fa[sl]);
            SB();
            if (sl + 6 < 18) fw[sl % 6][n] = K2_W(sl + 6, n);
            SB();
        }
    asm volatile("s_nop 15" ::: "memory");
    proj_store(ac, ws, rs, nullptr, FL + 128, hand, 2 * layer + 1, mt, nt, wid);
}

constexpr u32 STAGE = 18432, NS = 6;
constexpr u32 K3_PK = NS * STAGE, K3_PP = 304, K3_WS = K3_PK + 128 * K3_PP, K3_S1 = K3_WS + 4096, K3_S2 = K3_S1 + 2048, K3_CS = K3_S2 + 1024;
static_assert(K3_CS + 1280 <= LDS_B, "LDS map");

__device__ __forceinline__ void phase_b(char* ws, const u16* wsp, u16* out, u32 lds, int wid, int layer, bool hand) {
    int bid = blockIdx.x;
    GAS char* wsg = (GAS char*)ws;
    asm volatile("" : "+s"(bid), "+s"(wsg), "+s"(wid));
    ws = (char*)wsg;
    const int xcd = bid & 7, bi = bid >> 3;
    const int pm = PM_OF(xcd, bi), pn = PN_OF(xcd, bi);
    const u32 FL = lds + LDS_FL + 256 * layer + 128;
    const auto rs = __builtin_amdgcn_make_buffer_rsrc(ws, 0, WS_END, 0x00020000);
    if (wid == 4) {
        LANES();
        u32 ao[16];
        const u32 vo = lane * 16;
        {
            const u32 xw = WS_WIN + (9 * pn + 8) * 18432, xa = WS_X + (2 * layer + 1) * XB + 8 * pm * 18432;
            lds_wait_sleep(FL + 16 + (lane & 3) * 4, 2u);
            u32x4 xc[6][2];
#pragma unroll
            for (int i = 0; i < 12; ++i) { xc[i >> 1][i & 1] = __builtin_amdgcn_raw_buffer_load_b128(rs, vo, xw + 6144 + i * 1024, 16); SB(); }
#pragma unroll
            for (int i = 0; i < 6; ++i) {
                lds_wait_sleep(FL + 16 + (lane & 3) * 4, 4 + i);
                *(LAS u32x4*)(size_t)(lds + ((i + 3) % 6) * STAGE + 16384 + vo) = xc[i][0];
                *(LAS u32x4*)(size_t)(lds + ((i + 3) % 6) * STAGE + 17408 + vo) = xc[i][1];
                FENCE();
                stsw(FL, 10 + i);
                FENCE();
            }
            u32x4 xt[3][18];
#pragma unroll
            for (int i = 0; i < 3; ++i)
#pragma unroll
                for (int q = 0; q < 18; ++q) { xt[i][q] = __builtin_amdgcn_raw_buffer_load_b128(rs, vo, (q < 16 ? xa + (q >> 1) * 18432 : xw) + i * 2048 + (q & 1) * 1024, 16); SB(); }
#pragma unroll
            for (int i = 0; i < 3; ++i) {
                lds_wait_sleep(FL + 16 + (lane & 3) * 4, 10 + i);
#pragma unroll
                for (int q = 0; q < 18; ++q) *(LAS u32x4*)(size_t)(lds + (i + 3) * STAGE + q * 1024 + vo) = xt[i][q];
                FENCE();
                stsw(FL, 16 + i);
                FENCE();
            }
        }
        u32 slot = 0;
#define K3_ISSUE() do { const u32 dst = lds + slot * STAGE;                                                          \
            DMA4(ao[0], ao[1], ao[2], ao[3], ap, dst); DMA4(ao[4], ao[5], ao[6], ao[7], ap, dst + 4096);             \
            DMA4(ao[8], ao[9], ao[10], ao[11], ap, dst + 8192); DMA4(ao[12], ao[13], ao[14], ao[15], ap, dst + 12288); } while (0)
#pragma unroll
        for (int q = 0; q < 16; ++q) { ao[q] = (q * 8 + (lane >> 3)) * 4096 + (((lane & 7) ^ ((q & 1) * 4 + g)) * 16) - (q & 3) * 1024 + 4096; PINV(ao[q]); }
        const char* ap = (const char*)wsp + (size_t)pm * 128 * 4096 - 4096;
        lds_wait_sleep(FL + 16 + (lane & 3) * 4, LGATE2);
#pragma nounroll
        for (int s = 18; s < 53; ++s) {
            if (s >= 21) {
                if (s <= 50) WAIT_V(32); else if (s == 51) WAIT_V(16); else WAIT_V(0);
                stsw(FL, s - 2);
                FENCE();
            }
            if (s < 50) {
                lds_wait_sleep(FL + 16 + (lane & 3) * 4, s - NS + 1);
                K3_ISSUE();
                ap += s == 33 ? 128 - 15 * 256 : 256;
                slot = slot + 1 == NS ? 0 : slot + 1;
            }
        }
        lds_wait_sleep(FL + 80, 2u);
#pragma nounroll
        for (int i = 0; i < 8; ++i) {
            const int r = i < 2 ? 6 + i : (i < 6 ? i : i - 6);
            if (i >= 2) lds_wait_sleep(FL + 16 + (lane & 3) * 4, 43 + i);
            const char* wp = ws + WS_WOUT + ((pn & 7) * 72 + 18 * (r >> 1) + 4 * (r & 1)) * 4608;
            const u32 dst = lds + r * STAGE;
            DMA4(vo, vo, vo, vo, wp, dst); DMA4(vo, vo, vo, vo, wp + 4096, dst + 4096); DMA4(vo, vo, vo, vo, wp + 8192, dst + 8192);
            DMA4(vo, vo, vo, vo, wp + 12288, dst + 12288); DMA2(vo, vo, wp + 16384, dst + 16384);
            if (i >= 2) WAIT_V(36);
        }
        WAIT_V(0);
        stsw(FL, 100u);
        return;
    }
    if (wid > 4) {
        LANES();
        if (wid == 5) {
            f32x2 mean, sd;
            x_ready_stats(ws, rs, FL, true, 2 * layer + 1, pm, pn, lane, mean, sd);
#pragma unroll
            for (int k = 0; k < 2; ++k) {
                float r = __builtin_amdgcn_rcpf(sd[k]);
                r = fmaxf(r, 0.f);
                const f32x4 o = {-mean[k], sfma(r, -2.302208198f, 0.f), sfma(sfma(sfma(r, r, 0.f), r, 0.f), -0.1029432397f, 0.f), sd[k]};
                *(LAS f32x4*)(size_t)(lds + K3_S1 + (2 * lane + k) * 16) = o;
            }
        } else if (wid == 7) {
            if (layer == 0) {
                GAS u32* wm = (GAS u32*)(ws + WS_CNW + (((144 * pn + min(64 * (lane & 3), 143)) >> 6) + 36 * ((lane >> 2) & 1)) * 128);
                __builtin_amdgcn_s_sleep(WRD);
#pragma nounroll
                for (u32 n = 0; n < SPIN_CAP; ++n) { if (!ANY(__hip_atomic_load(wm, RLX_AGENT) < 3u)) break; __builtin_amdgcn_s_sleep(8); }
                FENCE();
            }
            stsw(FL + 116, 1u);
            FENCE();
            const int i = min(lane, 35);
#define K3_CSL(o) __builtin_bit_cast(f32x4, __builtin_amdgcn_raw_buffer_load_b128(rs, i * 16, WS_CSIN + (36 * pn + (o)) * 16, 16))
            const f32x4 a0 = K3_CSL(576), a1 = K3_CSL(576 + 1152), a2 = K3_CSL(576 + 2304), b0 = K3_CSL(0), b1 = K3_CSL(1152), b2 = K3_CSL(2304);
            *(LAS f32x4*)(size_t)(lds + K3_CS + i * 16) = sadd4(sadd4(a0, a1), a2);
            *(LAS f32x4*)(size_t)(lds + K3_CS + 640 + i * 16) = sadd4(sadd4(b0, b1), b2);
        }
        FENCE();
        stsw(FL + 32 + (wid - 5) * 4, 1u);
        u32 vo = lane * 16;
        PINV(vo);
        GAS u32* flg = (GAS u32*)(ws + WS_CNT + (layer * 16 + pn) * 4096);
        if (wid != 5) {
            const int h = wid - 6;
            u32 ta = lds + K3_PK + (32 * h + 8 * g + (c >> 2)) * K3_PP + 16 * (c & 3), tx = ta + 256 - 8 * (c & 3), s2r = lds + K3_S2 + 128 * h + 32 * g;
            PINV(ta); PINV(tx); PINV(s2r);
            {
                const u32 xa = WS_X + (2 * layer + 1) * XB + (8 * pm + 4 * h) * 18432, xw = WS_WIN + (144 + 9 * pn + 8) * 18432 + h * 1024;
                u32x4 xt[4][9];
#define K3_STG(i) do { _Pragma("unroll") for (int q = 0; q < 9; ++q) {                                               \
                    xt[(i) & 3][q] = __builtin_amdgcn_raw_buffer_load_b128(rs, vo, (q < 8 ? xa + (q >> 1) * 18432 + (q & 1) * 1024 : xw) + (i) * 2048, 16); SB(); } } while (0)
                lds_wait_sleep(FL + 116, 1u);
                lds_wait_sleep(FL + 120, 4u);
                lds_wait(FL + 124, 1u);
                K3_STG(0); K3_STG(1); K3_STG(2); K3_STG(3);
#pragma unroll
                for (int i = 0; i < 9; ++i) {
                    if (i >= 6) lds_wait_sleep(FL + 16 + (lane & 3) * 4, i - 5);
#pragma unroll
                    for (int q = 0; q < 9; ++q) *(LAS u32x4*)(size_t)(lds + (i % 6) * STAGE + (q < 8 ? h * 8192 + q * 1024 : 16384 + h * 1024) + vo) = xt[i & 3][q];
                    FENCE();
                    stsw(FL + 8 + 4 * h, i < 8 ? i + 1 : 1000u);
                    FENCE();
                    if (i + 4 < 9) K3_STG(i + 4);
                }
            }
            const u32 zob = WS_Z + (9 * pn * 64 + 2 * pm + h) * 1024;
            if (layer == 0) {
                lds_wait_sleep(FL + 16 + (lane & 3) * 4, TGATE);
                touch_paced<2>((const char*)wsp + bid * 32768 + h * 16384, 16, vo);
            }
            if (pm < HPRIO) __builtin_amdgcn_s_setprio(3);
#pragma nounroll
            for (int q = 0; q < 2; ++q) {
                lds_wait_sleep(FL + 48 + (lane & 3) * 4, q + 1);
                float zf[9][8];
#define K3_TR(fi, A0, O0, A1, O1, A2, O2) do { u32x2 t_[6];                                                          \
                    asm volatile("ds_read_b64_tr_b16 %0, %6 offset:%9\n\tds_read_b64_tr_b16 %1, %6 offset:%10\n\tds_read_b64_tr_b16 %2, %7 offset:%11\n\t" \
                                 "ds_read_b64_tr_b16 %3, %7 offset:%12\n\tds_read_b64_tr_b16 %4, %8 offset:%13\n\tds_read_b64_tr_b16 %5, %8 offset:%14\n\ts_waitcnt lgkmcnt(0)" \
                                 : "=&v"(t_[0]), "=&v"(t_[1]), "=&v"(t_[2]), "=&v"(t_[3]), "=&v"(t_[4]), "=&v"(t_[5]) \
                                 : "v"(A0), "v"(A1), "v"(A2), "i"(O0), "i"((O0) + 4 * K3_PP), "i"(O1), "i"((O1) + 4 * K3_PP), "i"(O2), "i"((O2) + 4 * K3_PP) : "memory"); \
                    _Pragma("unroll") for (int i_ = 0; i_ < 12; ++i_) { const u32 w_ = t_[i_ >> 1][i_ & 1];          \
                        zf[(fi) + (i_ >> 2)][2 * (i_ & 3)] = bflo(w_); zf[(fi) + (i_ >> 2)][2 * (i_ & 3) + 1] = bfhi(w_); \
                        PINV(zf[(fi) + (i_ >> 2)][2 * (i_ & 3)]); PINV(zf[(fi) + (i_ >> 2)][2 * (i_ & 3) + 1]); } } while (0)
                K3_TR(0, ta, 0, ta, 8, ta, 64); K3_TR(3, ta, 72, ta, 128, ta, 136); K3_TR(6, ta, 192, ta, 200, tx, 0);
                lds_wait_sleep(FL + 64, q + 1);
                f32x4 sv[4];
#pragma unroll
                for (int k = 0; k < 4; ++k) sv[k] = *(LAS f32x4*)(size_t)(s2r + (k >> 1) * 512 + (k & 1) * 16);
#pragma unroll
                for (int i = 0; i < 9; ++i) {
                    u32x4 o;
                    asm("v_fma_f32 %4, %4, %12, %20\n\tv_fma_f32 %5, %5, %13, %21\n\tv_fma_f32 %6, %6, %14, %22\n\tv_fma_f32 %7, %7, %15, %23\n\t"
                        "v_fma_f32 %8, %8, %16, %24\n\tv_fma_f32 %9, %9, %17, %25\n\tv_fma_f32 %10, %10, %18, %26\n\tv_fma_f32 %11, %11, %19, %27\n\t"
                        "v_cvt_pk_bf16_f32 %0, %4, %5\n\tv_cvt_pk_bf16_f32 %1, %6, %7\n\tv_cvt_pk_bf16_f32 %2, %8, %9\n\tv_cvt_pk_bf16_f32 %3, %10, %11"
                        : "=&v"(o[0]), "=&v"(o[1]), "=&v"(o[2]), "=&v"(o[3]), "+v"(zf[i][0]), "+v"(zf[i][1]), "+v"(zf[i][2]), "+v"(zf[i][3]), "+v"(zf[i][4]), "+v"(zf[i][5]), "+v"(zf[i][6]), "+v"(zf[i][7])
                        : "v"(sv[0][0]), "v"(sv[0][1]), "v"(sv[0][2]), "v"(sv[0][3]), "v"(sv[1][0]), "v"(sv[1][1]), "v"(sv[1][2]), "v"(sv[1][3]),
                          "v"(sv[2][0]), "v"(sv[2][1]), "v"(sv[2][2]), "v"(sv[2][3]), "v"(sv[3][0]), "v"(sv[3][1]), "v"(sv[3][2]), "v"(sv[3][3]));
                    __builtin_amdgcn_raw_buffer_store_b128(o, rs, vo, zob + q * 32768 + i * 65536, 16);
                    SB();
                }
                WAIT_V(0);
                u32 old = 0;
                if (lane == 0) old = __hip_atomic_fetch_add((LAS u32*)(size_t)(FL + 76 + 4 * q), 1u, __ATOMIC_RELAXED, __HIP_MEMORY_SCOPE_WORKGROUP);
                if (__builtin_amdgcn_readfirstlane(old) == 1 && lane < 16)
                    __hip_atomic_fetch_add(flg + lane * 64, 1u << (16 * q + pm), RLX_AGENT);
                __builtin_amdgcn_s_setprio(0);
                if (q == 0) __builtin_amdgcn_s_sleep(STAG2);
                ta += 64 * K3_PP; tx += 64 * K3_PP; s2r += 256;
            }
            return;
        }
        u32 wsa = lds + K3_WS + lane * 8, s2w = lds + K3_S2 + lane * 4, zv = lane * 8;
        PINV(wsa); PINV(s2w); PINV(zv);
        const u32 zsb = WS_ZST + (layer * 16 + pm) * 16384;
        lds_wait_sleep(FL + 16 + (lane & 3) * 4, 9u);
        __builtin_amdgcn_s_sleep(POLLD);
        __builtin_amdgcn_s_setprio(3);
#pragma nounroll
        for (int q = 0; q < 2; ++q) {
            lds_wait(FL + 48 + (lane & 3) * 4, q + 1);
            const f32x2 p0 = *(LAS f32x2*)(size_t)wsa, p1 = *(LAS f32x2*)(size_t)(wsa + 1024), p2 = *(LAS f32x2*)(size_t)(wsa + 2048), p3 = *(LAS f32x2*)(size_t)(wsa + 3072);
            const f32x4 st = sadd4((f32x4){p0[0], p0[1], p1[0], p1[1]}, (f32x4){p2[0], p2[1], p3[0], p3[1]});
            u32x2 o = {F2U(sfma(st[0], 1.f, st[2])), F2U(sfma(st[1], 1.f, st[3]))};
#pragma unroll
            for (int k = 0; k < 2; ++k) o[k] = o[k] == SENT ? 0x7fc00000u : o[k];
            __builtin_amdgcn_raw_buffer_store_b64(o, rs, zv, zsb + pn * 1024, 16);
            __builtin_amdgcn_s_sleep(XDELAY3);
            u32x2 t[16];
#define ZS_LD(k) __builtin_amdgcn_raw_buffer_load_b64(rs, zv + ((k) & 3) * 1024, zsb + ((k) >> 2) * 4096, 16)
#pragma unroll
            for (int k = 0; k < 16; ++k) t[k] = ZS_LD(k);
            u32 mx = 0;
#pragma unroll
            for (int k = 0; k < 16; ++k) mx = max(mx, max(t[k][0], t[k][1]));
            if (__builtin_expect(ANY(mx == SENT), 0)) {
#pragma nounroll
                for (u32 spins = 1; spins < SPIN_CAP; ++spins) {
                    FENCE();
                    u32 miss = 0;
#pragma unroll
                    for (int k = 0; k < 16; k += 4) miss |= ANY(max(max(max(t[k][0], t[k][1]), max(t[k + 1][0], t[k + 1][1])), max(max(t[k + 2][0], t[k + 2][1]), max(t[k + 3][0], t[k + 3][1]))) == SENT) ? 1u << k : 0u;
                    if (!miss) break;
#pragma unroll
                    for (int k = 0; k < 16; ++k) if (miss >> (k & 12) & 1) t[k] = ZS_LD(k);
                }
            }
            f32x4 sa[4];
#pragma unroll
            for (int k = 0; k < 4; ++k)
                sa[k] = sadd4((f32x4){U2F(t[4 * k][0]), U2F(t[4 * k][1]), U2F(t[4 * k + 1][0]), U2F(t[4 * k + 1][1])}, (f32x4){U2F(t[4 * k + 2][0]), U2F(t[4 * k + 2][1]), U2F(t[4 * k + 3][0]), U2F(t[4 * k + 3][1])});
            const f32x4 ss = sadd4(sadd4(sa[0], sa[1]), sadd4(sa[2], sa[3]));
            float m2, d2;
            ln_stats(sfma(ss[0], 1.f, ss[2]), sfma(ss[1], 1.f, ss[3]), 1.f / 2304.f, m2, d2);
            const float rr = __builtin_amdgcn_rcpf(d2);
            *(LAS float*)(size_t)s2w = rr;
            *(LAS float*)(size_t)(s2w + 512) = -m2 * rr;
            FENCE();
            stsw(FL + 64, q + 1);
            FENCE();
            wsa += 512; s2w += 256; zv += 512;
        }
        u32 ready = 0;
#define K3_NEED(n) do { _Pragma("nounroll") for (u32 spins_ = 0; ready < (u32)(n) && spins_ < SPIN_CAP; ++spins_) {   \
            const u32 v_ = __builtin_amdgcn_readfirstlane(__hip_atomic_load(flg + pm * 64, RLX_AGENT));                                               \
            ready = __builtin_ctzll(~(u64)v_); } FENCE(); } while (0)
        __builtin_amdgcn_s_sleep(FDELAY);
        K3_NEED(3);
        stsw(FL + 84, 1u);
        FENCE();
        __builtin_amdgcn_s_setprio(0);
        const u32 zx = WS_Z + (9 * pn + 8) * 65536;
        u32x4 xr[4][2];
#pragma unroll
        for (int k = 0; k < 3; ++k) {
            xr[k][0] = __builtin_amdgcn_raw_buffer_load_b128(rs, vo, zx + k * 2048, 16);
            xr[k][1] = __builtin_amdgcn_raw_buffer_load_b128(rs, vo + 1024, zx + k * 2048, 16);
            SB();
        }
        u32 slot = 0;
#pragma nounroll
        for (int s0 = 0; s0 < 32; s0 += 4) {
#pragma unroll
            for (int k = 0; k < 4; ++k) {
                const int s = s0 + k;
                lds_wait_sleep(FL + 16 + (lane & 3) * 4, 18 + s - NS + 1);
                *(LAS u32x4*)(size_t)(lds + slot * STAGE + 16384 + lane * 16) = xr[k][0];
                *(LAS u32x4*)(size_t)(lds + slot * STAGE + 17408 + lane * 16) = xr[k][1];
                FENCE();
                stsw(FL + 4, 18 + s + 1);
                FENCE();
                const u32 sn = s + 3 < 32 ? s + 3 : 31;
                if (ready < sn + 1) lds_wait_sleep(FL + 16 + (lane & 3) * 4, 18 + s - LAZY);
                K3_NEED(sn + 1);
                xr[(k + 3) & 3][0] = __builtin_amdgcn_raw_buffer_load_b128(rs, vo, s + 3 < 32 ? zx + sn * 2048 : WS_END, 16);
                xr[(k + 3) & 3][1] = __builtin_amdgcn_raw_buffer_load_b128(rs, vo + 1024, s + 3 < 32 ? zx + sn * 2048 : WS_END, 16);
                slot = slot + 1 == NS ? 0 : slot + 1;
            }
        }
        lds_wait_sleep(FL + 92, 4u);
        __builtin_amdgcn_s_sleep(HDELAY);
        GAS u32* hm = (GAS u32*)(ws + WS_CNT + (layer * 16 + pm) * 4096 + pn * 256 + 128);
#pragma nounroll
        for (u32 spins = 0; spins < SPIN_CAP; ++spins) {
            const u32 v = __builtin_amdgcn_readfirstlane(__hip_atomic_load(hm, RLX_AGENT));
            FENCE();
            stsw(FL + 88, v);
            if (v == 0xffffu) break;
        }
        return;
    }
    LANES();
#define MT(m) ((((m) >> 1) ^ wid) + 4 * ((m) & 1))
    u32 vA = lane * 16, vB = lane * 16 + 18432;
    u32 so = WS_WIN + (144 + 9 * pn + 2 * wid) * 18432;
    bf16x8 bb[6][2];
#define BLD(voff, imm, aux) __builtin_bit_cast(bf16x8, __builtin_amdgcn_raw_buffer_load_b128(rs, (voff) + (imm), so, aux))
#define BPRO(aux) do { _Pragma("unroll") for (int r = 0; r < 5; ++r) { if (r == 3) so += 3072;                       \
        bb[r][0] = BLD(vA, (r % 3) * 1024, aux); SB(); bb[r][1] = BLD(vB, (r % 3) * 1024, aux); SB(); } } while (0)
    lds_wait(FL + 116, 1u);
    BPRO(16);
    u32 pa[2][4];
#pragma unroll
    for (int q = 0; q < 4; ++q) { pa[0][q] = lds + lane * 16 + ((q ^ wid) * 2048); pa[1][q] = pa[0][q] + 1024; }
    u32 px = lds + 16384 + lane * 16;
    u32 myfree = FL + 16 + wid * 4, fladdr = FL + (lane & 3) * 4, cnt = 0;
    PINV(cnt); PINV(myfree); PINV(fladdr);
    int dstep = 3 * STAGE;
    f32x4 acc[8][2], accx[2];
    bf16x8 a[8], xf;
#define ALOAD() do { a[0] = LDSR(pa[0][0], 0); SB(); xf = LDSR(px, 0); SB();                                         \
        _Pragma("unroll") for (int m = 1; m < 8; ++m) { a[m] = LDSR(pa[0][m >> 1], (m & 1) * 8192); SB(); } } while (0)
#define MFS(acc, x, y) do { MFMA(acc, x, y); SB(); } while (0)
#define HALF(r, RD, kn, sf, rl, imm, aux, E0, E1, E2, E3, E4, E5) do {                                               \
        WCNT(8, 5); MFS(acc[0][0], bb[r][0], a[0]); E0; SB();                                                       \
        MFS(acc[0][1], bb[r][1], a[0]); E1; SB();                                                                   \
        MFS(accx[0], xf, a[0]); if (RD) a[0] = LDSR(pa[kn][0], (sf)); SB();                                         \
        MFS(accx[1], xf, a[1]); if (RD) xf = LDSR(px, (sf) + (kn) * 1024); SB();                                    \
        MFS(acc[1][1], bb[r][1], a[1]); bb[rl][0] = BLD(vA, imm, aux); SB();                                        \
        MFS(acc[1][0], bb[r][0], a[1]); if (RD) a[1] = LDSR(pa[kn][0], (sf) + 8192); SB();                          \
        MFS(acc[2][0], bb[r][0], a[2]); E2; SB();                                                                   \
        MFS(acc[2][1], bb[r][1], a[2]); if (RD) a[2] = LDSR(pa[kn][1], (sf)); SB();                                 \
        WCNT(63, 5); MFS(acc[3][1], bb[r][1], a[3]); SB();                                                          \
        MFS(acc[3][0], bb[r][0], a[3]); if (RD) a[3] = LDSR(pa[kn][1], (sf) + 8192); SB();                          \
        MFS(acc[4][0], bb[r][0], a[4]); bb[rl][1] = BLD(vB, imm, aux); SB();                                        \
        MFS(acc[4][1], bb[r][1], a[4]); if (RD) a[4] = LDSR(pa[kn][2], (sf)); SB();                                 \
        MFS(acc[5][1], bb[r][1], a[5]); E3; SB();                                                                   \
        MFS(acc[5][0], bb[r][0], a[5]); if (RD) a[5] = LDSR(pa[kn][2], (sf) + 8192); SB();                          \
        MFS(acc[6][0], bb[r][0], a[6]); E4; SB();                                                                   \
        MFS(acc[6][1], bb[r][1], a[6]); if (RD) a[6] = LDSR(pa[kn][3], (sf)); SB();                                 \
        MFS(acc[7][1], bb[r][1], a[7]); E5; SB();                                                                   \
        MFS(acc[7][0], bb[r][0], a[7]); if (RD) a[7] = LDSR(pa[kn][3], (sf) + 8192); SB(); } while (0)
#define STEP(u, chk, nso, aux) do { u32 fl_; u64 msk_;                                                               \
        HALF(2 * (u), 1, 1, (u) * STAGE, (2 * (u) + 5) % 6, ((2 * (u) + 5) % 3) * 1024, aux,                         \
             { if ((u) == 2) so += 3072; }, { cnt += 1; PINV(cnt); }, { FENCE(); fl_ = ldsw(fladdr); FENCE(); },     \
             { if ((u) == 2) { pa[0][0] += dstep; pa[0][1] += dstep; PINV(pa[0][0]); PINV(pa[0][1]); } },            \
             { if ((u) == 2) { pa[0][2] += dstep; pa[0][3] += dstep; PINV(pa[0][2]); PINV(pa[0][3]); } },            \
             { asm volatile("v_cmp_le_u32 %0, %1, %2" : "=s"(msk_) : "v"(fl_), "v"(cnt)); if ((u) == 2) { px += dstep; PINV(px); } }); \
        HALF(2 * (u) + 1, 1, 0, (((u) + 1) % 3) * STAGE, (2 * (u) + 6) % 6, ((2 * (u) + 6) % 3) * 1024, aux,         \
             { if (__builtin_expect(msk_ != 0, 0)) { if (chk) lds_wait_gt(fladdr, cnt); } FENCE(); }, { if ((u) == 0) so = (nso); }, \
             { FENCE(); stsw(myfree, cnt); FENCE(); },                                                               \
             { if ((u) == 2) { pa[1][0] += dstep; pa[1][1] += dstep; PINV(pa[1][0]); PINV(pa[1][1]); } },            \
             { if ((u) == 2) { pa[1][2] += dstep; pa[1][3] += dstep; PINV(pa[1][2]); PINV(pa[1][3]); } },            \
             { if ((u) == 2) dstep = -dstep; }); } while (0)
#define PINACC() do { _Pragma("unroll") for (int m = 0; m < 8; ++m) { PINV(acc[m][0]); PINV(acc[m][1]); } PINV(accx[0]); PINV(accx[1]); asm volatile("s_nop 1"); } while (0)
    u32 yp[18][2], early = 0;
    const u32 s1a = lds + K3_S1 + c * 16;
#pragma unroll
    for (int ph = 0; ph < 2; ++ph) {
        if (ph == 0) {
#pragma unroll
            for (int m = 0; m < 8; ++m) { acc[m][0] = acc[m][1] = (f32x4){0.f, 0.f, 0.f, 0.f}; if (m < 2) accx[m] = acc[m][0]; }
        } else {
            const u32 ca = lds + K3_CS + ph * 640 + g * 16;
            const f32x4 c0 = *(LAS f32x4*)(size_t)(ca + wid * 128), c1 = *(LAS f32x4*)(size_t)(ca + wid * 128 + 64), c2 = *(LAS f32x4*)(size_t)(ca + 512);
#pragma unroll
            for (int m = 0; m < 8; ++m) {
                const float nm = *(LAS float*)(size_t)(s1a + MT(m) * 256);
                acc[m][0] = c0 * nm; acc[m][1] = c1 * nm;
                if (m < 2) accx[m] = c2 * nm;
            }
        }
        PINACC();
        if (ph == 0) { lds_wait(fladdr, 1u); ALOAD();  }
        const u32 nxt = WS_WIN + (9 * pn + 2 * wid) * 18432;
        for (int j = 0; j < 3; ++j) {
            STEP(0, true, j == 2 - ph ? nxt + 6144 * (1 - ph) : (j == 2 ? WS_END : so + 3072), 16);
            STEP(1, true, 0, 16);
            STEP(2, ph == 0 || j < 2, 0, 16);
        }
        asm volatile("s_nop 15" ::: "memory");
        if (ph == 1) {
            vB = lane * 16 + 65536;
            so = WS_Z + (9 * pn + 2 * wid) * 65536;
            FENCE();
            early = __builtin_amdgcn_readfirstlane(ldsw(FL + 84));
            FENCE();
            if (early) BPRO(16);
        }
        float t1[8], t2[8];
        f32x4 rcs[8], cz[3];
        if (ph == 0) {
            lds_wait(FL + 32 + (lane % 3) * 4, 1u);
            const u32 ca = lds + K3_CS + g * 16;
            cz[0] = *(LAS f32x4*)(size_t)(ca + wid * 128); cz[1] = *(LAS f32x4*)(size_t)(ca + wid * 128 + 64); cz[2] = *(LAS f32x4*)(size_t)(ca + 512);
        }
#pragma unroll
        for (int m = 0; m < 8; ++m) rcs[m] = *(LAS f32x4*)(size_t)(s1a + MT(m) * 256);
#pragma unroll
        for (int mi = 0; mi < 8; ++mi) {
            const int m = 2 * (mi & 3) + (mi >> 2);
            if (ph == 1 && mi == 4 && !early) { FENCE(); early = __builtin_amdgcn_readfirstlane(ldsw(FL + 84)); FENCE(); if (early) BPRO(16); }
            constexpr int NP = 6;
            const int np = m < 2 ? 6 : 4;
            const f32x2 k1 = {rcs[m][1], rcs[m][1]}, k3 = {rcs[m][2], rcs[m][2]}, sg = {rcs[m][3], rcs[m][3]};
            f32x2 v[NP], e[NP];
#pragma unroll
            for (int i = 0; i < NP; ++i) if (i < np) {
                const f32x4 av = i < 4 ? acc[m][i >> 1] : accx[m & 1];
                v[i] = (f32x2){av[2 * (i & 1)], av[2 * (i & 1) + 1]};
                if (ph == 0) v[i] += (f32x2){cz[i >> 1][2 * (i & 1)], cz[i >> 1][2 * (i & 1) + 1]} * (f32x2){rcs[m][0], rcs[m][0]};
            }
#pragma unroll
            for (int i = 0; i < NP; ++i) if (i < np) e[i] = v[i] * v[i];
#pragma unroll
            for (int i = 0; i < NP; ++i) if (i < np) e[i] = e[i] * k3 + k1;
#pragma unroll
            for (int i = 0; i < NP; ++i) if (i < np) e[i] = e[i] * v[i];
            SB();
#pragma unroll
            for (int i = 0; i < NP; ++i) if (i < np) { e[i][0] = __builtin_amdgcn_exp2f(e[i][0]); e[i][1] = __builtin_amdgcn_exp2f(e[i][1]); }
            SB();
#pragma unroll
            for (int i = 0; i < NP; ++i) if (i < np) e[i] = e[i] * sg + sg;
            SB();
#pragma unroll
            for (int i = 0; i < NP; ++i) if (i < np) { e[i][0] = __builtin_amdgcn_rcpf(e[i][0]); e[i][1] = __builtin_amdgcn_rcpf(e[i][1]); }
            SB();
#pragma unroll
            for (int i = 0; i < NP; ++i) if (i < np) v[i] = v[i] * e[i];
#pragma unroll
            for (int i = 0; i < NP; ++i) if (i < np) yp[i < 4 ? 2 * m + (i >> 1) : 16 + m][i & 1] = pk2(v[i][0], v[i][1]);
            if (ph == 0) {
                f32x2 u1 = (v[0] + v[1]) + (v[2] + v[3]), u2 = v[0] * v[0], u3 = v[1] * v[1];
                u2 = v[2] * v[2] + u2; u3 = v[3] * v[3] + u3;
                if (m < 2) { u1 += v[4] + v[5]; u2 = v[4] * v[4] + u2; u3 = v[5] * v[5] + u3; }
                u2 += u3;
                t1[m] = u1[0] + u1[1]; t2[m] = u2[0] + u2[1];
            }
            PINV(yp[2 * m][0]); PINV(yp[2 * m][1]); PINV(yp[2 * m + 1][0]); PINV(yp[2 * m + 1][1]);
            if (m < 2) { PINV(yp[16 + m][0]); PINV(yp[16 + m][1]); }
            if (ph == 0) {
                const u32 row = (MT(m) << 4) + c;
                u32x4 o = {yp[2 * m][0], yp[2 * m][1], yp[2 * m + 1][0], yp[2 * m + 1][1]};
                *(LAS u32x4*)(size_t)(lds + K3_PK + row * K3_PP + 64 * wid + 16 * g) = o;
                if (m < 2) { u32x2 ox = {yp[16 + m][0], yp[16 + m][1]}; *(LAS u32x2*)(size_t)(lds + K3_PK + row * K3_PP + 256 + 8 * g) = ox; }
            }
            if (ph == 0 && (mi & 3) == 3) {
                const int q = mi >> 2;
                float w1[2], w2[2];
#pragma unroll
                for (int k = 0; k < 2; ++k) {
                    auto r1 = __builtin_amdgcn_permlane32_swap(F2U(t1[2 * k + q]), F2U(t1[2 * k + 4 + q]), false, false);
                    auto r2 = __builtin_amdgcn_permlane32_swap(F2U(t2[2 * k + q]), F2U(t2[2 * k + 4 + q]), false, false);
                    w1[k] = U2F(r1[0]) + U2F(r1[1]); w2[k] = U2F(r2[0]) + U2F(r2[1]);
                }
                auto r1 = __builtin_amdgcn_permlane16_swap(F2U(w1[0]), F2U(w1[1]), false, false);
                auto r2 = __builtin_amdgcn_permlane16_swap(F2U(w2[0]), F2U(w2[1]), false, false);
                f32x2 o = {U2F(r1[0]) + U2F(r1[1]), U2F(r2[0]) + U2F(r2[1])};
                *(LAS f32x2*)(size_t)(lds + K3_WS + (wid * 128 + 64 * q + 16 * (g ^ wid) + c) * 8) = o;
                FENCE();
                stsw(FL + 48 + wid * 4, q + 1);
                FENCE();
            }
        }
    }
    {
        float bv[8];
#pragma unroll
        for (int m = 0; m < 8; ++m) bv[m] = *(LAS float*)(size_t)(lds + LDS_B + ((MT(m) << 4) + c) * 4);
#pragma unroll
        for (int m = 0; m < 8; ++m) {
            acc[m][0] = acc[m][1] = (f32x4){bv[m], bv[m], bv[m], bv[m]};
            if (m < 2) accx[m] = acc[m][0];
        }
    }
#pragma unroll
    for (int q = 0; q < 4; ++q) { pa[0][q] = lds + (c >> 3) * 1024 + (c & 7) * 128 + ((g ^ ((c >> 1) & 7)) * 16) + ((q ^ wid) * 2048); pa[1][q] = pa[0][q] ^ 64; }
    if (!early) { lds_wait(FL + 84, 1u); BPRO(16); }
    lds_wait(fladdr, 19u);
    ALOAD();
    PINACC();
    for (int j = 0; j < 10; ++j) {
        STEP(0, true, so + 3072, 16);
        STEP(1, true, 0, 16);
        STEP(2, true, 0, 16);
    }
    so -= 8192;
    STEP(0, true, so, 16);
    STEP(1, false, 0, 16);
    asm volatile("s_nop 15" ::: "memory");
    {
        const u32 hb = WS_H + (8 * pm * 8 + (pn >> 1)) * 9216;
#pragma unroll
        for (int m = 0; m < 8; ++m) {
            const int mt = MT(m);
            u32x4 o;
            o[0] = pk2(bflo(yp[2 * m][0]) * acc[m][0][0], bfhi(yp[2 * m][0]) * acc[m][0][1]);
            o[1] = pk2(bflo(yp[2 * m][1]) * acc[m][0][2], bfhi(yp[2 * m][1]) * acc[m][0][3]);
            o[2] = pk2(bflo(yp[2 * m + 1][0]) * acc[m][1][0], bfhi(yp[2 * m + 1][0]) * acc[m][1][1]);
            o[3] = pk2(bflo(yp[2 * m + 1][1]) * acc[m][1][2], bfhi(yp[2 * m + 1][1]) * acc[m][1][3]);
            BST128(o, rs, vA, hb + mt * 73728 + ((pn & 1) * 4 + wid) * 1024, 16);
            if (m < 2) {
                u32x2 ox = {pk2(bflo(yp[16 + m][0]) * accx[m][0], bfhi(yp[16 + m][0]) * accx[m][1]), pk2(bflo(yp[16 + m][1]) * accx[m][2], bfhi(yp[16 + m][1]) * accx[m][3])};
                __builtin_amdgcn_raw_buffer_store_b64(ox, rs, vA, hb + mt * 73728 + 8192 + (pn & 1) * 8, 16);
            }
        }
        WAIT_V(0);
        u32 old = 0;
        if (lane == 0) old = __hip_atomic_fetch_add((LAS u32*)(size_t)(FL + 92), 1u, __ATOMIC_RELAXED, __HIP_MEMORY_SCOPE_WORKGROUP);
        if (__builtin_amdgcn_readfirstlane(old) == 3 && lane < 16)
            __hip_atomic_fetch_add((GAS u32*)(ws + WS_CNT + (layer * 16 + pm) * 4096 + lane * 256 + 128), 1u << pn, RLX_AGENT);
    }
    {
        const int mt = 2 * pm + (pn >> 3), nt = pn & 7;
        const u32 wb = WS_WOUT + (nt * 72 + 18 * wid) * 4608;
        const u32 vo4 = 4096 + g * 128 + (lane & 7) * 16;
        bf16x8 fa[4][4], fw[3][5], fl[5];
        f32x4 ac[4][5];
#pragma unroll
        for (int m = 0; m < 4; ++m)
#pragma unroll
            for (int n = 0; n < 5; ++n) ac[m][n] = (f32x4){0.f, 0.f, 0.f, 0.f};
        u32 abm[4];
#pragma unroll
        for (int m = 0; m < 4; ++m) abm[m] = WS_H + ((4 * mt + (m ^ wid)) * 8 + 2 * wid) * 9216;
#define K5_LD(v, sof, aux) __builtin_bit_cast(bf16x8, __builtin_amdgcn_raw_buffer_load_b128(rs, v, sof, aux))
#define K5_LW(r, s) do { _Pragma("unroll") for (int n = 0; n < 4; ++n) fw[r][n] = K5_LD(vA + n * 1024, wb + (s) * 4608, 0); fw[r][4] = K5_LD(vo4, wb + (s) * 4608, 0); SB(); } while (0)
#define K5_LA(r, s) do { _Pragma("unroll") for (int m = 0; m < 4; ++m) fa[r][m] = K5_LD(vA + ((s) & 3) * 1024, abm[m] + ((s) & ~3) * 1024, 16); SB(); } while (0)
        PINV(vA);
        const u32 wl = lds + wid * 2 * STAGE + vA, wl4 = lds + wid * 2 * STAGE + vo4;
        K5_LW(0, 8); K5_LW(1, 9); K5_LW(2, 10);
        u32x4 res[2]; u32x2 res2;
        const u32 xoff = WS_X + (2 * layer + 1) * XB + (4 * mt + wid) * 18432;
        {
            const u32 need = 15u << (4 * wid);
            FENCE();
#pragma nounroll
            for (u32 n = 0; n < SPIN_LDS; ++n) { if ((__builtin_amdgcn_readfirstlane(ldsw(FL + 88)) & need) == need) break; }
            FENCE();
        }
        constexpr int ord[18] = {8, 0, 9, 1, 10, 2, 11, 3, 12, 4, 13, 5, 14, 6, 15, 7, 16, 17};
#pragma unroll
        for (int i = 0; i < 4; ++i) K5_LA(i, ord[i]);
        lds_wait(FL, 100u);
#pragma unroll
        for (int m = 0; m < 4; ++m)
#pragma unroll
            for (int n = 0; n < 5; ++n) PINV(ac[m][n]);
        asm volatile("s_nop 1");
#pragma unroll
        for (int i = 0; i < 18; ++i) {
            const int sl = ord[i];
#pragma unroll
            for (int m = 0; m < 4; ++m)
#pragma unroll
                for (int n = 0; n < 5; ++n) {
                    if (sl >= 8) MFS(ac[m][n], fw[(sl - 8) % 3][n], fa[i & 3][m]); else MFS(ac[m][n], fl[n], fa[i & 3][m]);
                    const int e = (n == 1 ? m : (m == 0 && n == 3 ? 4 : -1));
                    if (e >= 0) {
                        if (sl >= 8 && sl < 16) fl[e] = e < 4 ? LDSR(wl, (sl - 8) * 4608 + e * 1024) : LDSR(wl4, (sl - 8) * 4608);
                        if (sl < 7) fw[sl % 3][e] = e < 4 ? K5_LD(vA + e * 1024, wb + (sl + 11) * 4608, 0) : K5_LD(vo4, wb + (sl + 11) * 4608, 0);
                        if (sl == 7 && e == 0) res[0] = __builtin_amdgcn_raw_buffer_load_b128(rs, vA, xoff + 2304 * nt, 16);
                        if (sl == 7 && e == 1) res[1] = __builtin_amdgcn_raw_buffer_load_b128(rs, vA + 1024, xoff + 2304 * nt, 16);
                        if (sl == 7 && e == 2) { const int l2 = lane_id(); res2 = __builtin_amdgcn_raw_buffer_load_b64(rs, (l2 & 15) * 16 + ((l2 >> 1) & 8) + 2048, xoff + 2304 * nt, 16); }
                    }
                    if (n == 4 && i + 4 < 18) fa[i & 3][m] = K5_LD(vA + (ord[i + 4 < 18 ? i + 4 : 0] & 3) * 1024, abm[m] + (ord[i + 4 < 18 ? i + 4 : 0] & ~3) * 1024, 16);
                    SB();
                }
        }
        asm volatile("s_nop 15" ::: "memory");
        proj_tail(ac, res, res2, lds, 2 * STAGE, FL + 96, ws, rs, out, FL + 128, hand, 2 * layer + 2, mt, nt, wid);
    }
}

__global__ __launch_bounds__(512, 2) void k1_main(char* __restrict__ ws, const u16* __restrict__ win, const u16* __restrict__ wsp, const u16* __restrict__ bsp,
                                                  u16* __restrict__ out) {
    const u32 lds = 0;
    const int wid = __builtin_amdgcn_readfirstlane(threadIdx.x >> 6);
    if (threadIdx.x >= 352) {
        const int i = threadIdx.x - 352, w = i & 63;
        stsw(lds + LDS_FL + i * 4, w == 32 ? 9u : (w == 33 ? 18u : 0u));
        WAIT_L0();
    }
#pragma nounroll
    for (int layer = 0; layer < 2; ++layer) {
        phase_a(ws, win, bsp, lds, wid, layer, layer != 0, true);
        phase_b(ws, wsp, out, lds, wid, layer, layer == 0);
    }
}

extern "C" __global__ void k9_end() {}

extern "C" void kernel_launch(void* const* d_in, const int* in_sizes, int n_in, void* d_out, int out_size, void* d_ws, size_t ws_size,
                              hipStream_t stream) {
    const u16* x = (const u16*)d_in[0];
    const u16* wqkv = (const u16*)d_in[1];
    const u16* wo = (const u16*)d_in[2];
    const u16* win = (const u16*)d_in[4];
    const u16* wsp = (const u16*)d_in[5];
    const u16* bsp = (const u16*)d_in[6];
    const u16* wout = (const u16*)d_in[7];
    char* ws = (char*)d_ws;
    u16* out = (u16*)d_out;
    (void)hipFuncSetAttribute((const void*)k1_main, hipFuncAttributeMaxDynamicSharedMemorySize, LDS_TOTAL);
    k0_prep<<<K0_GRID, 576, 0, stream>>>(x, wqkv, wo, wout, bsp, ws);
    k1_main<<<256, 512, LDS_TOTAL, stream>>>(ws, win, wsp, bsp, out);
}
```
